```python
import torch

def _softmax(x, axis=-1):
    return torch.softmax(x, dim=axis)
def _gelu(x):
    return torch.nn.functional.gelu(x, approximate="tanh")
def _silu(x):
    return torch.nn.functional.silu(x)
def _sigmoid(x):
    return torch.sigmoid(x)
def _layernorm(x, axis=-1, eps=1e-5):
    m = x.mean(dim=axis, keepdim=True)
    v = x.var(dim=axis, keepdim=True, unbiased=False)
    return (x - m) / torch.sqrt(v + eps)
def _scatter_add(tgt, idx, src, axis=0):
    return tgt.clone().index_add_(axis, idx, src)
def _segment_sum(src, seg, n):
    out = torch.zeros((n,) + src.shape[1:], dtype=src.dtype)
    return out.index_add_(0, seg, src)
def _pad(x, pw):
    flat = tuple(v for pair in reversed(pw) for v in pair)
    return torch.nn.functional.pad(x, flat)

kernel_name = 'sequential_preLN__sliding_block__mixer_token_mlp__prompt'

def setup_inputs():

    _w = (lambda a: (lambda t: t.to(torch.bfloat16) if t.is_floating_point() else t)(torch.as_tensor(a)))
    def _A_setup():
        B, S, D = 1, 2048, 512
        return {
            "x": _w((lambda *s: torch.randn(*s, dtype=torch.bfloat16) * 0.05)(B, S, D)),
            "wqkv": _w((lambda *s: torch.randn(*s, dtype=torch.bfloat16) * 0.05)(D, 3 * D)),
            "wo": _w((lambda *s: torch.randn(*s, dtype=torch.bfloat16) * 0.05)(D, D)),
        }
    _A_in = _A_setup()

    def _B_setup():
        B, S, D, F = 1, 2048, 512, 1376
        return {
            "x": _w((lambda *s: torch.randn(*s, dtype=torch.bfloat16) * 0.05)(B, S, D)),
            "w1": _w((lambda *s: torch.randn(*s, dtype=torch.bfloat16) * 0.8)(S, F) * (S ** -0.5)),
            "b1": _w((lambda *s: torch.randn(*s, dtype=torch.bfloat16) * 0.8)(F) * 0.02),
            "w2": _w((lambda *s: torch.randn(*s, dtype=torch.bfloat16) * 0.8)(F, S) * (F ** -0.5)),
            "b2": _w((lambda *s: torch.randn(*s, dtype=torch.bfloat16) * 0.8)(S) * 0.02),
        }
    _B_in = _B_setup()

    return {
        "A_x": _A_in["x"],
        "A_wqkv": _A_in["wqkv"],
        "A_wo": _A_in["wo"],
        "B_x": _B_in["x"],
        "B_w1": _B_in["w1"],
        "B_b1": _B_in["b1"],
        "B_w2": _B_in["w2"],
        "B_b2": _B_in["b2"],
    }

def reference(A_x, A_wqkv, A_wo, B_x, B_w1, B_b1, B_w2, B_b2):
    def _A_ref(x, wqkv, wo):
        B, S, D = x.shape
        H = 32; Dh = D // H; W = 256
        qkv = (lambda x, *d: x.permute(*d))((x @ wqkv).reshape(B, S, 3, H, Dh), 2, 0, 3, 1, 4)
        q, k, v = qkv[0], qkv[1], qkv[2]
        scores = torch.einsum("bhqd,bhkd->bhqk", q, k) * (Dh ** -0.5)
        idx = torch.arange(S)
        mask = (idx[:, None] - idx[None, :] >= 0) & (idx[:, None] - idx[None, :] < W)
        scores = torch.where(mask[None, None], scores, -1e9)
        p = _softmax(scores, axis=-1)
        o = torch.einsum("bhqk,bhkd->bhqd", p, v)
        return (lambda x, *d: x.permute(*d))(o, 0, 2, 1, 3).reshape(B, S, D) @ wo

    def _B_ref(x, w1, b1, w2, b2):
        B, S, D = x.shape
        h = _layernorm(x, axis=-1)
        u = torch.einsum("bsd,sf->bdf", h, w1) + b1
        u = _gelu(u)
        y = torch.einsum("bdf,fs->bds", u, w2) + b2
        return (lambda x, *d: x.permute(*d))(y, 0, 2, 1)

    x = A_x
    h = x + _A_ref(_layernorm(x, axis=-1), A_wqkv, A_wo)
    h2 = h + _B_ref(_layernorm(h, axis=-1), B_w1, B_b1, B_w2, B_b2)
    return h2
```

```cpp
#include <hip/hip_runtime.h>
#include <hip/hip_bf16.h>
#include <stdint.h>
typedef unsigned short u16;
typedef short bf16x8 __attribute__((ext_vector_type(8)));
typedef float f32x4 __attribute__((ext_vector_type(4)));
__device__ __forceinline__ float bf2f(u16 x){ return __uint_as_float(((unsigned)x)<<16); }
__device__ __forceinline__ u16 f2bf(float f){ unsigned u=__float_as_uint(f); u += 0x7fffu + ((u>>16)&1u); return (u16)(u>>16); }

__global__ void k_ln(const u16* x, u16* y, int twice){
  int row = blockIdx.x*4 + (threadIdx.x>>6), lane = threadIdx.x&63;
  float v[8];
  uint4 r = *(const uint4*)(x + row*512 + lane*8);
  unsigned w[4]={r.x,r.y,r.z,r.w};
  for(int i=0;i<4;i++){ v[2*i]=__uint_as_float(w[i]<<16); v[2*i+1]=__uint_as_float(w[i]&0xffff0000u);}
  for(int t=0;t<=twice;t++){
    float s=0; for(int i=0;i<8;i++) s+=v[i];
    for(int m=32;m>=1;m>>=1) s+=__shfl_xor(s,m);
    float mu=s/512.f; float q=0; for(int i=0;i<8;i++){ v[i]-=mu; q+=v[i]*v[i]; }
    for(int m=32;m>=1;m>>=1) q+=__shfl_xor(q,m);
    float rs=rsqrtf(q/512.f+1e-5f);
    for(int i=0;i<8;i++) v[i]*=rs;
  }
  for(int i=0;i<4;i++) w[i] = (unsigned)f2bf(v[2*i]) | ((unsigned)f2bf(v[2*i+1])<<16);
  *(uint4*)(y + row*512 + lane*8) = make_uint4(w[0],w[1],w[2],w[3]);
}
template<int MODE>
__global__ void __launch_bounds__(256) k_gemm(const u16* A, int sam, int sak, const u16* B, int ldb, u16* C, int ldc, int N, int K, const u16* res, const u16* bias){
  __shared__ u16 As[64][40], Bs[64][40];
  int tid=threadIdx.x, lane=tid&63, w=tid>>6, m0=blockIdx.y*64, n0=blockIdx.x*64;
  int wm=w>>1, wn=w&1;
  f32x4 acc[2][2]; for(int i=0;i<2;i++)for(int j=0;j<2;j++) acc[i][j]=f32x4{0,0,0,0};
  for(int k0=0;k0<K;k0+=32){
    if (sak==1) { int m=tid>>2, kc=(tid&3)*8; *(uint4*)&As[m][kc] = *(const uint4*)(A + (size_t)(m0+m)*sam + k0+kc); }
    else { int k=tid>>3, mc=(tid&7)*8; uint4 r=*(const uint4*)(A + (size_t)(k0+k)*sak + m0+mc); u16* p=(u16*)&r; for(int i=0;i<8;i++) As[mc+i][k]=p[i]; }
    { int k=tid>>3, nc=(tid&7)*8; uint4 r=make_uint4(0,0,0,0); if(n0+nc<N) r=*(const uint4*)(B + (size_t)(k0+k)*ldb + n0+nc); u16* p=(u16*)&r; for(int i=0;i<8;i++) Bs[nc+i][k]=p[i]; }
    __syncthreads();
    bf16x8 a[2], b[2];
    for(int i=0;i<2;i++){ a[i]=*(const bf16x8*)&As[wm*32+i*16+(lane&15)][(lane>>4)*8]; b[i]=*(const bf16x8*)&Bs[wn*32+i*16+(lane&15)][(lane>>4)*8]; }
    for(int i=0;i<2;i++)for(int j=0;j<2;j++) acc[i][j]=__builtin_amdgcn_mfma_f32_16x16x32_bf16(a[i],b[j],acc[i][j],0,0,0);
    __syncthreads();
  }
  for(int i=0;i<2;i++)for(int j=0;j<2;j++)for(int r=0;r<4;r++){
    int m=m0+wm*32+i*16+(lane>>4)*4+r, n=n0+wn*32+j*16+(lane&15);
    if(n>=N) continue;
    float v=acc[i][j][r];
    if(MODE==0) C[(size_t)m*ldc+n]=f2bf(v);
    if(MODE==1) C[(size_t)m*ldc+n]=f2bf(v+bf2f(res[(size_t)m*ldc+n]));
    if(MODE==2){ v+=bf2f(bias[n]); float t=0.7978845608f*(v+0.044715f*v*v*v); float th=1.f-2.f/(__expf(2.f*t)+1.f); C[(size_t)m*ldc+n]=f2bf(0.5f*v*(1.f+th)); }
    if(MODE==3) C[(size_t)n*512+m]=f2bf(v+bf2f(bias[n])+bf2f(res[(size_t)n*512+m]));
  }
}
__global__ void __launch_bounds__(256) k_attn(const u16* qkv, u16* o){
  __shared__ float ks[512][17], vs[512][17];
  int h=blockIdx.y, q0=blockIdx.x*256, tid=threadIdx.x;
  for(int i=tid;i<512*2;i+=256){ int j=i>>1, half=i&1; int t=q0-256+j;
    if(t>=0){ uint4 r=*(const uint4*)(qkv+(size_t)t*1536+512+h*16+half*8); u16*p=(u16*)&r; for(int e=0;e<8;e++) ks[j][half*8+e]=bf2f(p[e]);
      r=*(const uint4*)(qkv+(size_t)t*1536+1024+h*16+half*8); for(int e=0;e<8;e++) vs[j][half*8+e]=bf2f(p[e]); } }
  __syncthreads();
  int q=q0+tid; float qv[16]; for(int e=0;e<16;e++) qv[e]=bf2f(qkv[(size_t)q*1536+h*16+e])*0.25f;
  float m=-1e30f,l=0,acc[16]; for(int e=0;e<16;e++) acc[e]=0;
  int lo=max(0,q-255);
  for(int t=lo;t<=q;t++){ int j=t-q0+256; float s=0; for(int e=0;e<16;e++) s+=qv[e]*ks[j][e];
    float mn=fmaxf(m,s), a=__expf(m-mn), p=__expf(s-mn); l=l*a+p; for(int e=0;e<16;e++) acc[e]=acc[e]*a+p*vs[j][e]; m=mn; }
  for(int e=0;e<16;e++) o[(size_t)q*512+h*16+e]=f2bf(acc[e]/l);
}
__global__ void k_nop(unsigned* p){ if(threadIdx.x==0 && blockIdx.x==0) p[0]=1; }
#define EXTRA 0
#define MEMSET 0
extern "C" void kernel_launch(void* const* d_in, const int* in_sizes, int n_in, void* d_out, int out_size, void* d_ws, size_t ws_size, hipStream_t stream) {
  const u16 *x=(const u16*)d_in[0], *wqkv=(const u16*)d_in[1], *wo=(const u16*)d_in[2], *w1=(const u16*)d_in[4], *b1=(const u16*)d_in[5], *w2=(const u16*)d_in[6], *b2=(const u16*)d_in[7];
  u16* ws=(u16*)d_ws; u16 *xn=ws+65536, *qkv=xn+2048*512, *o=qkv+2048*1536, *h=o+2048*512, *hn=h+2048*512, *g=hn+2048*512;
  if (MEMSET) hipMemsetAsync(d_ws, 0, 16384, stream);
  for(int i=0;i<EXTRA;i++) k_nop<<<256,256,0,stream>>>((unsigned*)d_ws + 8192);
  k_ln<<<512,256,0,stream>>>(x,xn,0);
  k_gemm<0><<<dim3(24,32),256,0,stream>>>(xn,512,1,wqkv,1536,qkv,1536,1536,512,nullptr,nullptr);
  k_attn<<<dim3(8,32),256,0,stream>>>(qkv,o);
  k_gemm<1><<<dim3(8,32),256,0,stream>>>(o,512,1,wo,512,h,512,512,512,x,nullptr);
  k_ln<<<512,256,0,stream>>>(h,hn,1);
  k_gemm<2><<<dim3(22,8),256,0,stream>>>(hn,1,512,w1,1376,g,1376,1376,2048,nullptr,b1);
  k_gemm<3><<<dim3(32,8),256,0,stream>>>(g,1376,1,w2,2048,(u16*)d_out,512,2048,1376,h,b2);
}
```

```cpp
#include <hip/hip_runtime.h>
#include <stdint.h>

typedef unsigned u32;
typedef unsigned short u16;
typedef u32 v4u __attribute__((ext_vector_type(4)));
typedef u32 v2u __attribute__((ext_vector_type(2)));
typedef int v4i __attribute__((ext_vector_type(4)));
typedef float f32x2 __attribute__((ext_vector_type(2)));
typedef float f32x4 __attribute__((ext_vector_type(4)));
typedef float f32x16 __attribute__((ext_vector_type(16)));
typedef short bf16x8 __attribute__((ext_vector_type(8)));
typedef short s16x4 __attribute__((ext_vector_type(4)));
typedef __bf16 bf2 __attribute__((ext_vector_type(2)));
#define LAS __attribute__((address_space(3)))
#define DI __device__ __forceinline__

#define STAMPS 0

constexpr u32 WS_EPOCH = 0, WS_FLAGA = 4096, WS_FLAGB = 8192, WS_DBG = 65536;
constexpr u32 WS_KV = 1u << 20;
constexpr u32 WS_O = WS_KV + (4u << 20);
constexpr u32 WS_HN = WS_O + (2u << 20);
constexpr u32 WS_G = WS_HN + (2u << 20);
constexpr u32 WS_ST = WS_G + (2u << 20);
constexpr u32 WS_END = WS_ST + (1u << 17);
constexpr u32 SENT = 0xFFFFFFFFu;
constexpr u32 OOB = 0x7FFF0000u;
constexpr int SPIN_CAP = 1 << 16;

constexpr u32 L_XN = 0;
constexpr u32 KVT = 8448;
constexpr u32 L_RING = 86016;
constexpr u32 L_CNT = L_RING + 8 * 9216;
constexpr u32 L_WO = 0;
constexpr u32 L_PC = 98304;
constexpr u32 PD_PITCH = 208, PD_SLOT = 64 * PD_PITCH;
constexpr u32 LDS_BYTES = 163840;

DI u32 cvtpk(float a, float b) { f32x2 v = {a, b}; bf2 r = __builtin_convertvector(v, bf2); return __builtin_bit_cast(u32, r); }
DI float bflo(u32 x) { return __uint_as_float(x << 16); }
DI float bfhi(u32 x) { return __uint_as_float(x & 0xffff0000u); }
DI u32 guard(u32 x) { return x < 0xFFFFFFFEu ? x : 0xFFFFFFFEu; }
DI v4u guard4(v4u v) { v4u r = {guard(v.x), guard(v.y), guard(v.z), guard(v.w)}; return r; }
DI u32 umax3(u32 a, u32 b, u32 c) { u32 m = a > b ? a : b; return m > c ? m : c; }
DI u32 smax4(u32 m, v4u v) { return umax3(umax3(m, v.x, v.y), v.z, v.w); }
DI bool any_sent(u32 m) { return __builtin_amdgcn_ballot_w64(m == SENT) != 0; }
DI bf16x8 asfrag(v4u v) { return __builtin_bit_cast(bf16x8, v); }
DI f32x16 mfma32(bf16x8 a, bf16x8 b, f32x16 c) { return __builtin_amdgcn_mfma_f32_32x32x16_bf16(a, b, c, 0, 0, 0); }
DI f32x4 mfma16(bf16x8 a, bf16x8 b, f32x4 c) { return __builtin_amdgcn_mfma_f32_16x16x32_bf16(a, b, c, 0, 0, 0); }
DI v4u lds_r128(u32 a) { return *(const LAS v4u*)(uintptr_t)a; }
DI f32x4 lds_rf4(u32 a) { return *(const LAS f32x4*)(uintptr_t)a; }
DI void lds_w128(u32 a, v4u v) { *(LAS v4u*)(uintptr_t)a = v; }
DI void lds_wf4(u32 a, f32x4 v) { *(LAS f32x4*)(uintptr_t)a = v; }
DI bf16x8 lds_tr(u32 a0, u32 a1) {
  s16x4 lo = __builtin_amdgcn_ds_read_tr16_b64_v4i16((LAS s16x4*)(uintptr_t)a0);
  s16x4 hi = __builtin_amdgcn_ds_read_tr16_b64_v4i16((LAS s16x4*)(uintptr_t)a1);
  return __builtin_shufflevector(lo, hi, 0, 1, 2, 3, 4, 5, 6, 7);
}
template <int CTRL> DI float dpp(float v) { return __int_as_float(__builtin_amdgcn_update_dpp(0, __float_as_int(v), CTRL, 0xf, 0xf, true)); }
DI float sum8(float v) { v += dpp<0xB1>(v); v += dpp<0x4E>(v); v += dpp<0x141>(v); return v; }
DI v4i mksrd(const void* p, u32 bytes) { v4i s = {(int)(u32)(uintptr_t)p, (int)(u32)((uintptr_t)p >> 32), (int)bytes, 0x00020000}; return s; }
DI __amdgpu_buffer_rsrc_t mkrs(const void* p, u32 bytes) { return __builtin_amdgcn_make_buffer_rsrc((void*)p, 0, bytes, 0x00020000); }
DI void dma16(u32 lds, u32 voff, v4i srd, u32 soff) {
  asm volatile("s_mov_b32 m0, %0\n\ts_nop 0\n\tbuffer_load_dwordx4 %1, %2, %3 offen lds" ::"s"(lds), "v"(voff), "s"(srd), "s"(soff) : "memory");
}
DI v4u aload_sc1(u32 voff, v4i srd, u32 soff) {
  v4u r;
  asm volatile("buffer_load_dwordx4 %0, %1, %2, %3 offen sc1" : "=v"(r) : "v"(voff), "s"(srd), "s"(soff) : "memory");
  return r;
}
DI v4u aload(u32 voff, v4i srd, u32 soff) {
  v4u r;
  asm volatile("buffer_load_dwordx4 %0, %1, %2, %3 offen" : "=v"(r) : "v"(voff), "s"(srd), "s"(soff) : "memory");
  return r;
}
DI void ld4(v4u (&f)[4], u32 voff, v4i srd, u32 soff) {
  asm volatile("buffer_load_dwordx4 %0, %4, %5, %6 offen sc1\n\tbuffer_load_dwordx4 %1, %4, %5, %6 offen offset:1024 sc1\n\t"
               "buffer_load_dwordx4 %2, %4, %5, %6 offen offset:2048 sc1\n\tbuffer_load_dwordx4 %3, %4, %5, %6 offen offset:3072 sc1"
               : "=&v"(f[0]), "=&v"(f[1]), "=&v"(f[2]), "=&v"(f[3]) : "v"(voff), "s"(srd), "s"(soff) : "memory");
}
template <int N> DI void take4(v4u (&f)[4], u32 voff, v4i srd, u32 soff, int& spins) {
  asm volatile("s_waitcnt vmcnt(%4)" : "+v"(f[0]), "+v"(f[1]), "+v"(f[2]), "+v"(f[3]) : "n"(N) : "memory");
  if (__builtin_expect(any_sent(smax4(smax4(smax4(smax4(0, f[0]), f[1]), f[2]), f[3])), 0)) {
#pragma clang loop unroll(disable)
    do {
      asm volatile("s_sleep 1\n\tbuffer_load_dwordx4 %0, %4, %5, %6 offen sc1\n\tbuffer_load_dwordx4 %1, %4, %5, %6 offen offset:1024 sc1\n\t"
                   "buffer_load_dwordx4 %2, %4, %5, %6 offen offset:2048 sc1\n\tbuffer_load_dwordx4 %3, %4, %5, %6 offen offset:3072 sc1\n\t"
                   "s_waitcnt vmcnt(0)"
                   : "+v"(f[0]), "+v"(f[1]), "+v"(f[2]), "+v"(f[3]) : "v"(voff), "s"(srd), "s"(soff) : "memory");
    } while (any_sent(smax4(smax4(smax4(smax4(0, f[0]), f[1]), f[2]), f[3])) && ++spins < SPIN_CAP);
  }
}
template <int N> DI void vmwait() { asm volatile("s_waitcnt vmcnt(%0)" ::"n"(N) : "memory"); }
DI void bar() { asm volatile("s_waitcnt lgkmcnt(0)\n\ts_barrier" ::: "memory"); }
DI v4u ld_sc1(__amdgpu_buffer_rsrc_t rs, u32 voff, u32 soff) { return __builtin_amdgcn_raw_buffer_load_b128(rs, voff, soff, 16); }
DI void st_sc1(v4u v, v4i srd, u32 voff, u32 soff) {
  asm volatile("buffer_store_dwordx4 %0, %1, %2, %3 offen sc1\n\ts_nop 1" ::"v"(v), "v"(voff), "s"(srd), "s"(soff) : "memory");
}
DI void pin() { __builtin_amdgcn_sched_barrier(0); }

#define STAMP(i) do {} while (0)

struct W1Pre {
  v4i srd; u32 vo[3]; u32 so; u32 slot; u32 rd; bool on;
  DI void issue(int step) const {
    if (on) {
#pragma unroll
      for (int u = 0; u < 3; ++u) dma16(slot + (step % 3) * 3072 + u * 1024, vo[u], srd, so + step * (32 * 2752));
    }
  }
  DI void take(int step, bf16x8 (&f)[8][3]) const {
#pragma unroll
    for (int b = 0; b < 3; ++b) { const u32 a = rd + (step % 3) * 3072 + 32 * b; f[step][b] = lds_tr(a, a + 16 * 96); }
  }
};

template <bool FULL>
DI v4u attention(bf16x8 qf, int tb, int hh, int jmin, int lane, const W1Pre& pre, bf16x8 (&w1f)[8][3],
                 __amdgpu_buffer_rsrc_t rws, u32 tag, int wg) {
  const int r = lane & 31, h = lane >> 5;
  const u32 kb = tb * KVT + hh * 2048 + lane * 16;
  const u32 ones = tb * KVT + 8192 + ((r >= 20 && r < 28) ? 64 : 0);
  const u32 vb0 = r < 16 ? tb * KVT + hh * 2048 + 1024 + h * 256 + r * 16 : ones;
  const u32 vb1 = r < 16 ? vb0 + 512 : ones;
  f32x16 z16;
#pragma unroll
  for (int i = 0; i < 16; ++i) z16[i] = 0.f;
  float mx = -3.0e38f;
#pragma unroll
  for (int i = 1; i <= 7; ++i) {
    if (FULL || tb + i >= jmin) {
      const f32x16 s = mfma32(asfrag(lds_r128(kb + i * KVT)), qf, z16);
#pragma unroll
      for (int j = 0; j < 16; j += 2) mx = fmaxf(fmaxf(mx, s[j]), s[j + 1]);
    }
  }
  f32x16 sm = mfma32(asfrag(lds_r128(kb + 8 * KVT)), qf, z16);
  const int thr = r - 4 * h;
  if (FULL) {
    const f32x16 sf = mfma32(asfrag(lds_r128(kb)), qf, z16);
#pragma unroll
    for (int i = 0; i < 16; ++i) sm[i] = ((i & 3) + 8 * (i >> 2)) <= thr ? sm[i] : sf[i];
  } else {
#pragma unroll
    for (int i = 0; i < 16; ++i) sm[i] = ((i & 3) + 8 * (i >> 2)) <= thr ? sm[i] : -3.0e38f;
  }
#pragma unroll
  for (int j = 0; j < 16; j += 2) mx = fmaxf(fmaxf(mx, sm[j]), sm[j + 1]);
  {
    const u32 a = __float_as_uint(mx);
    const auto sw = __builtin_amdgcn_permlane32_swap(a, a, false, false);
    const u32 s0 = sw[0], s1 = sw[1];
    mx = fmaxf(__uint_as_float(s0), __uint_as_float(s1));
  }
  f32x16 o = z16;
  {
    float pl[16], pf[16];
#pragma unroll
    for (int i = 0; i < 16; ++i) {
      const float p = __builtin_amdgcn_exp2f(sm[i] - mx);
      pl[i] = ((i & 3) + 8 * (i >> 2)) <= thr ? p : 0.f;
      pf[i] = p - pl[i];
    }
#pragma unroll
    for (int s = 0; s < 2; ++s) {
      const v4u a = {cvtpk(pl[8 * s], pl[8 * s + 1]), cvtpk(pl[8 * s + 2], pl[8 * s + 3]), cvtpk(pl[8 * s + 4], pl[8 * s + 5]), cvtpk(pl[8 * s + 6], pl[8 * s + 7])};
      o = mfma32(asfrag(lds_r128((s ? vb1 : vb0) + 8 * KVT)), asfrag(a), o);
      if (FULL) {
        const v4u b = {cvtpk(pf[8 * s], pf[8 * s + 1]), cvtpk(pf[8 * s + 2], pf[8 * s + 3]), cvtpk(pf[8 * s + 4], pf[8 * s + 5]), cvtpk(pf[8 * s + 6], pf[8 * s + 7])};
        o = mfma32(asfrag(lds_r128(s ? vb1 : vb0)), asfrag(b), o);
      }
    }
  }
  vmwait<6>();
  {
    if (!pre.on) vmwait<0>();
    u32 old = 0;
    if (lane == 0) old = __hip_atomic_fetch_add((LAS u32*)(uintptr_t)L_CNT, 1u, __ATOMIC_RELAXED, __HIP_MEMORY_SCOPE_WORKGROUP);
    if (__builtin_amdgcn_readfirstlane(old) == 7 && lane == 0) __builtin_amdgcn_raw_buffer_store_b32(tag, rws, WS_FLAGB + wg * 4, 0, 16);
  }
  pre.take(0, w1f);
  f32x16 nm;
#pragma unroll
  for (int i = 0; i < 16; ++i) nm[i] = -mx;
  v4u kn = lds_r128(kb + KVT);
#pragma unroll
  for (int i = 1; i <= 7; ++i) {
    const v4u kc = kn;
    if (i < 7) kn = lds_r128(kb + (i + 1) * KVT);
    if (FULL || tb + i >= jmin) {
      const f32x16 s = mfma32(asfrag(kc), qf, nm);
      float p[16];
#pragma unroll
      for (int j = 0; j < 16; ++j) p[j] = __builtin_amdgcn_exp2f(s[j]);
      const v4u a = {cvtpk(p[0], p[1]), cvtpk(p[2], p[3]), cvtpk(p[4], p[5]), cvtpk(p[6], p[7])};
      const v4u b = {cvtpk(p[8], p[9]), cvtpk(p[10], p[11]), cvtpk(p[12], p[13]), cvtpk(p[14], p[15])};
      o = mfma32(asfrag(lds_r128(vb0 + i * KVT)), asfrag(a), o);
      o = mfma32(asfrag(lds_r128(vb1 + i * KVT)), asfrag(b), o);
    }
    asm volatile("s_waitcnt lgkmcnt(0)" ::: "memory");
    if (i + 2 < 8) pre.issue(i + 2);
    if (i + 2 < 8) vmwait<6>(); else if (i == 6) vmwait<3>(); else vmwait<0>();
    pre.take(i, w1f);
  }
  const float inv = __builtin_amdgcn_rcpf(o[8]);
  const v4u res = {cvtpk(o[0] * inv, o[1] * inv), cvtpk(o[2] * inv, o[3] * inv), cvtpk(o[4] * inv, o[5] * inv), cvtpk(o[6] * inv, o[7] * inv)};
  return guard4(res);
}

DI float gelu_tanh(float x) {
  const float c1 = -2.0f * 0.7978845608028654f * 1.4426950408889634f, c2 = c1 * 0.044715f;
  const float e = __builtin_amdgcn_exp2f(x * (c1 + c2 * x * x));
  return x * __builtin_amdgcn_rcpf(1.0f + e);
}

__global__ void __launch_bounds__(512, 2)
mega(const u16* __restrict__ gx, const u16* __restrict__ gwqkv, const u16* __restrict__ gwo, const u16* __restrict__ gw1,
     const u16* __restrict__ gb1, const u16* __restrict__ gw2, const u16* __restrict__ gb2, u16* __restrict__ gout, char* ws) {
  const int tid = threadIdx.x, lane = tid & 63;
  const int w = __builtin_amdgcn_readfirstlane(tid >> 6);
  const int wg = blockIdx.x, xcd = wg & 7, loc = wg >> 3;
  const int tt = 8 * (xcd >> 1) + (loc & 7), cg = 4 * (xcd & 1) + (loc >> 3);
  const int dt = 4 * (xcd & 1) + (loc & 3), fq = xcd >> 1, fi = loc >> 2;
  const int fg = fq == 0 ? fi : 1 + 7 * fq + fi;
  const bool don = fq == 0 || fi < 7;
  const int rot = ((loc & 7) + 2 * (loc >> 3)) & 7;
  STAMP(0);
  const v4i sx = mksrd(gx, 2048 * 512 * 2), swqkv = mksrd(gwqkv, 512 * 1536 * 2);
  const __amdgpu_buffer_rsrc_t rws = mkrs(ws, WS_END);
  const v4i sws = mksrd(ws, WS_END);
  const u32 l16 = lane * 16;
  u32 epoch;
  asm volatile("s_load_dword %0, %1, 0x0" : "=s"(epoch) : "s"(ws) : "memory");
  const v4u sent4 = {SENT, SENT, SENT, SENT};
  int spins = 0;

  const int l8 = lane >> 3, c8 = lane & 7;
  v4u xr[8];
  {
    const u32 vo = (64 * tt + 8 * w + l8) * 1024 + c8 * 16;
#pragma unroll
    for (int i = 0; i < 4; ++i) xr[i] = aload(vo, sx, ((i + rot) & 7) * 128);
    st_sc1(sent4, sws, tid * 16, WS_KV + (tt * 8 + cg) * 16384);
    st_sc1(sent4, sws, tid * 16 + 8192, WS_KV + (tt * 8 + cg) * 16384);
#pragma unroll
    for (int i = 4; i < 8; ++i) xr[i] = aload(vo, sx, ((i + rot) & 7) * 128);
  }
  const u32 wvo = (8 * w + l8) * 3072 + ((c8 ^ (((l8 >> 1) & 1) << 2)) * 16) + cg * 128;
  auto issue_w = [&](int st) {
    const u32 slot = L_RING + (st & 3) * 16384 + w * 1024;
    if (st < 8) {
      const u32 so = ((st + rot) & 7) * (64 * 3072);
      dma16(slot, wvo, swqkv, so + 1024);
      dma16(slot + 8192, wvo, swqkv, so + 2048);
    } else {
      const u32 so = ((st + (rot >> 1)) & 3) * (128 * 3072);
      dma16(slot, wvo, swqkv, so);
      dma16(slot + 8192, wvo, swqkv, so + 64 * 3072);
    }
  };
#pragma unroll
  for (int st = 0; st < 4; ++st) issue_w(st);
  {
    float xv[64];
    asm volatile("s_waitcnt vmcnt(14)" : "+v"(xr[0]), "+v"(xr[1]), "+v"(xr[2]), "+v"(xr[3])::"memory");
    const float sh0 = dpp<0x00>(bflo(xr[0].x)), sh1 = dpp<0x141>(sh0);
    const float shift = (lane & 4) ? sh1 : sh0;
    float s1 = 0.f, s2 = 0.f;
    auto acc4 = [&](int i) {
      const u32 d[4] = {xr[i].x, xr[i].y, xr[i].z, xr[i].w};
#pragma unroll
      for (int j = 0; j < 4; ++j) {
        const float a = bflo(d[j]) - shift, b = bfhi(d[j]) - shift;
        xv[8 * i + 2 * j] = a; xv[8 * i + 2 * j + 1] = b;
        s1 += a + b; s2 = fmaf(a, a, fmaf(b, b, s2));
      }
    };
#pragma unroll
    for (int i = 0; i < 4; ++i) acc4(i);
    asm volatile("s_waitcnt vmcnt(8)" : "+v"(xr[4]), "+v"(xr[5]), "+v"(xr[6]), "+v"(xr[7])::"memory");
#pragma unroll
    for (int i = 4; i < 8; ++i) acc4(i);
    s1 = sum8(s1) * (1.f / 512.f); s2 = sum8(s2) * (1.f / 512.f);
    const float rs = __builtin_amdgcn_rsqf(fmaxf(s2 - s1 * s1, 0.f) + 1e-5f), nb = -s1 * rs;
    const int row = 8 * w + l8;
    const u32 xa = L_XN + row * 128 + ((c8 ^ ((row >> 1) & 7)) * 16);
#pragma unroll
    for (int i = 0; i < 8; ++i) {
      const v4u o = {cvtpk(fmaf(xv[8 * i], rs, nb), fmaf(xv[8 * i + 1], rs, nb)), cvtpk(fmaf(xv[8 * i + 2], rs, nb), fmaf(xv[8 * i + 3], rs, nb)),
                     cvtpk(fmaf(xv[8 * i + 4], rs, nb), fmaf(xv[8 * i + 5], rs, nb)), cvtpk(fmaf(xv[8 * i + 6], rs, nb), fmaf(xv[8 * i + 7], rs, nb))};
      lds_w128(xa + ((i + rot) & 7) * 8192, o);
    }
    if (tid == 0) *(LAS u32*)(uintptr_t)L_CNT = 0;
  }
  asm volatile("s_waitcnt lgkmcnt(0)" : "+s"(epoch)::"memory");
  const u32 tag = epoch + 1;
  STAMP(1);

  const int r32 = lane & 31, h = lane >> 5, blk = (lane >> 4) & 1, q4 = (lane & 15) >> 2, p4 = lane & 3;
  {
    const int isV = w >> 2, tb = w & 1, hp = (w >> 1) & 1;
    const int xrow = 32 * tb + r32;
    u32 xa[4];
#pragma unroll
    for (int s = 0; s < 4; ++s) xa[s] = L_XN + xrow * 128 + (((2 * s + h) ^ ((xrow >> 1) & 7)) * 16);
    const u32 wa = L_RING + isV * 8192 + (8 * h + q4) * 128 + (((4 * hp + 2 * blk + (p4 >> 1)) ^ (((q4 >> 1) & 1) << 2)) * 16) + 8 * (p4 & 1);
    f32x16 acc;
#pragma unroll
    for (int i = 0; i < 16; ++i) acc[i] = 0.f;
#pragma clang loop unroll(disable)
    for (int st = 0; st < 8; ++st) {
      if (st == 0) vmwait<6>(); else vmwait<4>();
      bar();
      if (st == 0 && tid == 0) __builtin_amdgcn_raw_buffer_store_b32(tag, rws, WS_FLAGA + wg * 4, 0, 16);
      if (st >= 1) issue_w(st + 3);
      const u32 xo = ((st + rot) & 7) * 8192, wo_ = wa + (st & 3) * 16384;
      if (isV) {
#pragma unroll
        for (int s = 0; s < 4; ++s) acc = mfma32(asfrag(lds_r128(xa[s] + xo)), lds_tr(wo_ + s * 2048, wo_ + s * 2048 + 512), acc);
      } else {
#pragma unroll
        for (int s = 0; s < 4; ++s) acc = mfma32(lds_tr(wo_ + s * 2048, wo_ + s * 2048 + 512), asfrag(lds_r128(xa[s] + xo)), acc);
      }
    }
    STAMP(2);
    const v4u a = guard4(v4u{cvtpk(acc[0], acc[1]), cvtpk(acc[2], acc[3]), cvtpk(acc[4], acc[5]), cvtpk(acc[6], acc[7])});
    const v4u b = guard4(v4u{cvtpk(acc[8], acc[9]), cvtpk(acc[10], acc[11]), cvtpk(acc[12], acc[13]), cvtpk(acc[14], acc[15])});
    const u32 oa = isV ? (2 * hp + (r32 >> 4)) * 2048 + 1024 + h * 256 + (r32 & 15) * 16 : 2 * hp * 2048 + lane * 16;
    const u32 ob = oa + (isV ? 512 : 2048);
    const u32 gso = WS_KV + (tt * 8 + cg) * 16384 + tb * 8192;
    st_sc1(a, sws, oa, gso);
    st_sc1(b, sws, ob, gso);
    lds_w128((8 + tb) * KVT + oa, a);
    lds_w128((8 + tb) * KVT + ob, b);
  }
  const int hh = w & 3, tb = w >> 2;
  bf16x8 qf;
  v4u halo[2][4];
  u32 hso[2], hvo[2];
  {
    const int ptt = tt - 4 + (lane & 3);
    const int pwg = ((ptt & 7) + 8 * (cg & 3)) * 8 + 2 * (ptt >> 3) + (cg >> 2);
    const u32 fao = ptt >= 0 ? WS_FLAGA + pwg * 4 : OOB;
    u32 fa = __builtin_amdgcn_raw_buffer_load_b32(rws, fao, 0, 16);
    const int xrow = 32 * tb + r32;
    u32 xa[4];
#pragma unroll
    for (int s = 0; s < 4; ++s) xa[s] = L_XN + xrow * 128 + (((2 * s + h) ^ ((xrow >> 1) & 7)) * 16);
    const u32 wa = L_RING + (8 * h + q4) * 128 + (((2 * hh + (p4 >> 1)) ^ (((q4 >> 1) & 1) << 2)) * 16) + 8 * (p4 & 1);
    f32x16 acc;
#pragma unroll
    for (int i = 0; i < 16; ++i) acc[i] = 0.f;
    auto qstage = [&](int j) {
      const u32 xo = ((j + (rot >> 1)) & 3) * 16384, wo_ = wa + j * 16384;
#pragma unroll
      for (int s = 0; s < 8; ++s)
        acc = mfma32(lds_tr(wo_ + s * 2048, wo_ + s * 2048 + 512), asfrag(lds_r128(xa[s & 3] + xo + (s >> 2) * 8192)), acc);
    };
    vmwait<7>(); bar(); issue_w(11); qstage(0);
    vmwait<7>(); bar(); qstage(1);
    vmwait<5>(); bar(); qstage(2);
    vmwait<0>();
_Pragma("clang loop unroll(disable)")
    while (__builtin_expect(__builtin_amdgcn_ballot_w64(ptt >= 0 && fa != tag) != 0, 0) && ++spins < SPIN_CAP) {
      asm volatile("s_sleep 1" ::: "memory");
      fa = __builtin_amdgcn_raw_buffer_load_b32(rws, fao, 0, 16);
    }
    asm volatile("" ::: "memory");
#pragma unroll
    for (int i = 0; i < 2; ++i) {
      const int pt = tt - 4 + 2 * (w >> 2) + i;
      hso[i] = pt >= 0 ? WS_KV + (pt * 8 + cg) * 16384 + (w & 3) * 4096 : 0;
      hvo[i] = pt >= 0 ? l16 : OOB;
      ld4(halo[i], hvo[i], sws, hso[i]);
    }
    pin();
    __builtin_amdgcn_s_barrier();
    asm volatile("" ::: "memory");
    qstage(3);
    const float qs = 0.25f * 1.4426950408889634f;
    qf = asfrag(v4u{cvtpk(acc[0] * qs, acc[1] * qs), cvtpk(acc[2] * qs, acc[3] * qs), cvtpk(acc[4] * qs, acc[5] * qs), cvtpk(acc[6] * qs, acc[7] * qs)});
  }
  bar();
  STAMP(3);
  W1Pre pre;
  pre.srd = mksrd(gw1, 2048 * 1376 * 2);
  pre.on = don;
  pre.slot = L_RING + w * 9216;
  pre.so = w * (256 * 2752) + fg * 96;
  pre.rd = pre.slot + (4 * (lane >> 4) + q4) * 96 + 8 * p4;
#pragma unroll
  for (int u = 0; u < 3; ++u) {
    const int q = 64 * u + lane, row = q / 6;
    int c = q - 6 * row;
    if (fg == 28 && c >= 4) c -= 2;
    pre.vo[u] = row * 2752 + c * 16;
  }
  {
    if (tid < 160) { const v4u one4 = {0x3F803F80u, 0x3F803F80u, 0x3F803F80u, 0x3F803F80u}; lds_w128((tid >> 4) * KVT + 8192 + (tid & 15) * 16, one4); }
    take4<4>(halo[0], hvo[0], sws, hso[0], spins);
#pragma unroll
    for (int u = 0; u < 4; ++u) lds_w128((4 * (w >> 2) + ((w & 3) >> 1)) * KVT + (4 * (w & 1) + u) * 1024 + l16, halo[0][u]);
    take4<0>(halo[1], hvo[1], sws, hso[1], spins);
#pragma unroll
    for (int u = 0; u < 4; ++u) lds_w128((4 * (w >> 2) + 2 + ((w & 3) >> 1)) * KVT + (4 * (w & 1) + u) * 1024 + l16, halo[1][u]);
  }
  const u32 g_ab = WS_G + (dt * 43 + fg) * 4096, g_c = WS_G + (dt * 43 + 29 + (fg >> 1)) * 4096 + (fg & 1) * 512;
  st_sc1(sent4, sws, l16, WS_O + tt * 65536 + cg * 4096 + (w >> 2) * 32768 + (w & 3) * 1024);
  st_sc1(sent4, sws, tid * 16, WS_HN + (cg * 64 + 2 * tt) * 4096);
  st_sc1(sent4, sws, !don ? OOB : tid < 256 ? g_ab + tid * 16 : (tid < 384 && fg < 28) ? g_c + ((tid - 256) >> 5) * 1024 + (tid & 31) * 16 : OOB, 0);
  st_sc1(sent4, sws, tid < 32 ? tid * 16 : OOB, WS_ST + (tt * 8 + cg) * 512);
  pre.issue(0); pre.issue(1); pre.issue(2);
  bar();
  STAMP(4);

  bf16x8 w1f[8][3];
  v4u of;
  if (tt >= 4) of = attention<true>(qf, tb, hh, 0, lane, pre, w1f, rws, tag, wg);
  else of = attention<false>(qf, tb, hh, 8 - 2 * tt, lane, pre, w1f, rws, tag, wg);
  st_sc1(of, sws, (r32 >> 4) * 1024 + ((2 * (hh & 1) + h) * 16 + (r32 & 15)) * 16, WS_O + tt * 65536 + tb * 32768 + (2 * cg + (hh >> 1)) * 2048);
  STAMP(5);

  const int g4 = lane >> 4;
  const u32 tra = (4 * g4 + q4) * 128 + 8 * (p4 & 1);
  const u32 trx = ((4 * g4 + q4) >> 1) & 3;
  v4u hres;
  {
    const __amdgpu_buffer_rsrc_t rx = mkrs(gx, 2048 * 512 * 2);
    const v4u xres = __builtin_amdgcn_raw_buffer_load_b128(rx, (64 * tt + 8 * w + l8) * 1024 + cg * 128 + c8 * 16, 0, 0);
    v4u fb = aload_sc1(l16, sws, WS_FLAGB);
    bar();
    const int a = w >> 2, kq = w & 3;
    {
      const v4i swo = mksrd(gwo, 512 * 512 * 2);
      const u32 vo = l8 * 1024 + ((c8 ^ (((l8 >> 1) & 3) << 1)) * 16);
#pragma unroll
      for (int u = 0; u < 8; ++u) {
        const int uu = u & 3, ks = 4 * kq + 2 * a + (u >> 2);
        dma16(L_WO + ks * 4096 + uu * 1024, vo, swo, ks * 32768 + (16 * (uu & 1) + 8 * (uu >> 1)) * 1024 + cg * 128);
      }
    }
    asm volatile("s_waitcnt vmcnt(8)" : "+v"(fb)::"memory");
_Pragma("clang loop unroll(disable)")
    while (__builtin_expect(__builtin_amdgcn_ballot_w64(fb.x != tag || fb.y != tag || fb.z != tag || fb.w != tag) != 0, 0) && ++spins < SPIN_CAP) {
      asm volatile("s_sleep 1" ::: "memory");
      fb = ld_sc1(rws, lane * 16, WS_FLAGB);
    }
    asm volatile("" ::: "memory");
    if (wg == 0 && tid == 0) __builtin_amdgcn_raw_buffer_store_b32(tag, rws, WS_EPOCH, 0, 16);
    v4u ofr[2][4];
    const u32 oso = WS_O + tt * 65536 + a * 32768 + kq * 8192;
    ld4(ofr[0], l16, sws, oso);
    ld4(ofr[1], l16, sws, oso + 4096);
    vmwait<8>();
    __builtin_amdgcn_s_barrier();
    asm volatile("" ::: "memory");
    STAMP(6);
    f32x4 acc[2][4];
#pragma unroll
    for (int m = 0; m < 2; ++m)
#pragma unroll
      for (int n = 0; n < 4; ++n) acc[m][n] = f32x4{0.f, 0.f, 0.f, 0.f};
#pragma unroll
    for (int k = 0; k < 4; ++k) {
      bf16x8 wf[4];
#pragma unroll
      for (int n = 0; n < 4; ++n) { const u32 ad = L_WO + (4 * kq + k) * 4096 + tra + (((2 * n + (p4 >> 1)) ^ (trx << 1)) * 16); wf[n] = lds_tr(ad, ad + 2048); }
      if (k == 0) take4<4>(ofr[0], l16, sws, oso, spins);
      if (k == 2) take4<0>(ofr[1], l16, sws, oso + 4096, spins);
#pragma unroll
      for (int m = 0; m < 2; ++m)
#pragma unroll
        for (int n = 0; n < 4; ++n) acc[m][n] = mfma16(wf[n], asfrag(ofr[k >> 1][2 * (k & 1) + m]), acc[m][n]);
    }
    STAMP(7);
#pragma unroll
    for (int m = 0; m < 2; ++m)
#pragma unroll
      for (int n = 0; n < 4; ++n) {
        const int tok = 32 * a + 16 * m + (lane & 15);
        lds_wf4(L_PC + kq * 16384 + tok * 256 + (((4 * n + g4) ^ (lane & 15)) * 16), acc[m][n]);
      }
    bar();
    const int tok = 8 * w + l8;
    f32x4 s0 = {bflo(xres.x), bfhi(xres.x), bflo(xres.y), bfhi(xres.y)}, s1 = {bflo(xres.z), bfhi(xres.z), bflo(xres.w), bfhi(xres.w)};
    f32x4 t0 = {0.f, 0.f, 0.f, 0.f}, t1 = t0;
#pragma unroll
    for (int k = 0; k < 4; ++k) {
      t0 += lds_rf4(L_PC + k * 16384 + tok * 256 + (((2 * c8) ^ (tok & 15)) * 16));
      t1 += lds_rf4(L_PC + k * 16384 + tok * 256 + (((2 * c8 + 1) ^ (tok & 15)) * 16));
    }
    s0 += t0; s1 += t1;
    hres = v4u{cvtpk(s0.x, s0.y), cvtpk(s0.z, s0.w), cvtpk(s1.x, s1.y), cvtpk(s1.z, s1.w)};
  }
  const v4i sw2 = mksrd(gw2, 1376 * 2048 * 2);
  auto issue_w2 = [&](int j0) {
    const u32 vo = (8 * (l8 >> 2) + (l8 & 3)) * 4096 + ((c8 ^ (((l8 >> 1) & 3) << 1)) * 16);
#pragma unroll
    for (int j = j0; j < j0 + 3; ++j) {
      const int step = w + 8 * j;
      if (step < 43) {
        const u32 r0 = step < 29 ? step * 48 : (step - 29) * 96 + 32, r1 = r0 + (step < 29 ? 16 : 48);
#pragma unroll
        for (int u = 0; u < 4; ++u) dma16(w * 12288 + (j % 3) * 4096 + u * 1024, vo, sw2, (((u & 1) ? r1 : r0) + 4 * (u >> 1)) * 4096 + tt * 128);
      }
    }
  };
  auto take_w2 = [&](int j, bf16x8 (&f)[4]) {
#pragma unroll
    for (int n = 0; n < 4; ++n) { const u32 ad = w * 12288 + (j % 3) * 4096 + tra + (((2 * n + (p4 >> 1)) ^ (trx << 1)) * 16); f[n] = lds_tr(ad, ad + 2048); }
  };
  bf16x8 w2f[3][4];
  {
    float hv[8] = {bflo(hres.x), bfhi(hres.x), bflo(hres.y), bfhi(hres.y), bflo(hres.z), bfhi(hres.z), bflo(hres.w), bfhi(hres.w)};
    const float pm = sum8(((hv[0] + hv[1]) + (hv[2] + hv[3])) + ((hv[4] + hv[5]) + (hv[6] + hv[7]))) * (1.f / 64.f);
    float q = 0.f;
#pragma unroll
    for (int i = 0; i < 8; ++i) q = fmaf(hv[i] - pm, hv[i] - pm, q);
    q = sum8(q);
    const v2u stv = {guard(__float_as_uint(pm)), guard(__float_as_uint(q))};
    __builtin_amdgcn_raw_buffer_store_b64(stv, rws, c8 == 0 ? (8 * w + l8) * 8 : OOB, WS_ST + (tt * 8 + cg) * 512, 16);
    STAMP(8);
    issue_w2(0);
    const u32 sto = c8 * 512 + (8 * w + l8) * 8;
    v2u st = __builtin_amdgcn_raw_buffer_load_b64(rws, sto, WS_ST + tt * 4096, 16);
_Pragma("clang loop unroll(disable)")
    while (__builtin_expect(any_sent(st.x > st.y ? st.x : st.y), 0) && ++spins < SPIN_CAP) {
      asm volatile("" ::: "memory");
      st = __builtin_amdgcn_raw_buffer_load_b64(rws, sto, WS_ST + tt * 4096, 16);
    }
    STAMP(9);
    const u32 sx_ = st.x, sy_ = st.y;
    const float mc = __uint_as_float(sx_), mean = sum8(mc) * 0.125f;
    const float var = sum8(fmaf(64.f * (mc - mean), mc - mean, __uint_as_float(sy_))) * (1.f / 512.f);
    const float r1 = __builtin_amdgcn_rsqf(var + 1e-5f);
    const float r2 = __builtin_amdgcn_rsqf(var * r1 * r1 + 1e-5f);
    const float sc = r1 * r2;
    const v4u hn = {cvtpk((hv[0] - mean) * sc, (hv[1] - mean) * sc), cvtpk((hv[2] - mean) * sc, (hv[3] - mean) * sc),
                    cvtpk((hv[4] - mean) * sc, (hv[5] - mean) * sc), cvtpk((hv[6] - mean) * sc, (hv[7] - mean) * sc)};
    lds_w128(L_PC + w * 2048 + l8 * 128 + ((c8 ^ (((l8 >> 1) & 3) << 1)) * 16), hn);
    bar();
    const int a = w >> 2, n = w & 3, row = 4 * g4 + q4;
    const u32 ad = L_PC + (4 * a + (row >> 3)) * 2048 + (row & 7) * 128 + (((2 * n + (p4 >> 1)) ^ (trx << 1)) * 16) + 8 * (p4 & 1);
    const v4u fr = guard4(__builtin_bit_cast(v4u, lds_tr(ad, ad + 2 * 2048)));
    st_sc1(fr, sws, lane * 16 + n * 1024, WS_HN + (cg * 64 + 2 * tt + a) * 4096);
    STAMP(10);
#pragma unroll
    for (int j = 0; j < 3; ++j) take_w2(j, w2f[j]);
    bar();
  }
  if (don) {
    f32x4 acc[3][4];
#pragma unroll
    for (int b = 0; b < 3; ++b)
#pragma unroll
      for (int d = 0; d < 4; ++d) acc[b][d] = f32x4{0.f, 0.f, 0.f, 0.f};
    const u32 so = WS_HN + (dt * 64 + 8 * w) * 4096;
    v4u hf[2][4], b1v;
    vmwait<0>();
    ld4(hf[0], l16, sws, so);
    ld4(hf[1], l16, sws, so + 4096);
#pragma unroll
    for (int s = 0; s < 8; ++s) {
      if (s < 7) take4<4>(hf[s & 1], l16, sws, so + s * 4096, spins); else take4<0>(hf[s & 1], l16, sws, so + s * 4096, spins);
      if (s == 7) {
        b1v = aload(w < 6 ? (48 * fg + 16 * ((2 * w + (lane >> 5)) >> 2) + 8 * ((lane >> 4) & 1)) * 2 : OOB, mksrd(gb1, 1376 * 2), 0);
        issue_w2(3);
      }
#pragma unroll
      for (int d = 0; d < 4; ++d)
#pragma unroll
        for (int b = 0; b < 3; ++b) acc[b][d] = mfma16(w1f[s][b], asfrag(hf[s & 1][d]), acc[b][d]);
      pin();
      if (s + 2 < 8) ld4(hf[s & 1], l16, sws, so + (s + 2) * 4096);
      pin();
    }
    STAMP(11);
    const u32 slot = L_PC + (w & 3) * PD_SLOT;
    if (w >= 4) {
#pragma unroll
      for (int b = 0; b < 3; ++b)
#pragma unroll
        for (int d = 0; d < 4; ++d) lds_wf4(slot + (b * 4 + d) * 1024 + lane * 16, acc[b][d]);
    }
    bar();
    if (w < 4) {
#pragma unroll
      for (int b = 0; b < 3; ++b)
#pragma unroll
        for (int d = 0; d < 4; ++d) acc[b][d] += lds_rf4(slot + (b * 4 + d) * 1024 + lane * 16);
      asm volatile("s_waitcnt lgkmcnt(0)" ::: "memory");
#pragma unroll
      for (int b = 0; b < 3; ++b)
#pragma unroll
        for (int d = 0; d < 4; ++d) lds_wf4(slot + (16 * d + (lane & 15)) * PD_PITCH + (4 * b + g4) * 16, acc[b][d]);
    }
    bar();
    if (w >= 3) vmwait<8>();
    asm volatile("s_waitcnt vmcnt(12)" : "+v"(b1v)::"memory");
    if (w < 6) {
      const int hf2 = 2 * w + h, b = hf2 >> 2, d = hf2 & 3;
      const u32 ad = L_PC + (16 * d + (lane & 15)) * PD_PITCH + (4 * b + 2 * blk) * 16;
      f32x4 s0 = {bflo(b1v.x), bfhi(b1v.x), bflo(b1v.y), bfhi(b1v.y)}, s1 = {bflo(b1v.z), bfhi(b1v.z), bflo(b1v.w), bfhi(b1v.w)};
      f32x4 t0 = {0.f, 0.f, 0.f, 0.f}, t1 = t0;
#pragma unroll
      for (int k = 0; k < 4; ++k) { t0 += lds_rf4(ad + k * PD_SLOT); t1 += lds_rf4(ad + k * PD_SLOT + 16); }
      s0 += t0; s1 += t1;
      const v4u gv = guard4(v4u{cvtpk(gelu_tanh(s0.x), gelu_tanh(s0.y)), cvtpk(gelu_tanh(s0.z), gelu_tanh(s0.w)),
                                cvtpk(gelu_tanh(s1.x), gelu_tanh(s1.y)), cvtpk(gelu_tanh(s1.z), gelu_tanh(s1.w))});
      const u32 go = b < 2 ? g_ab + d * 1024 + ((2 * b + blk) * 16 + (lane & 15)) * 16
                           : (fg < 28 ? g_c + d * 1024 + (blk * 16 + (lane & 15)) * 16 : OOB);
      st_sc1(gv, sws, go, 0);
    }
  } else {
    issue_w2(3);
  }
  STAMP(12);
  bar();
  {
    f32x4 acc[4][4];
#pragma unroll
    for (int d = 0; d < 4; ++d)
#pragma unroll
      for (int n = 0; n < 4; ++n) acc[d][n] = f32x4{0.f, 0.f, 0.f, 0.f};
    const u32 so = WS_G + (cg * 43 + w) * 4096;
    v4u gf[2][4];
    const u32 b2v = __builtin_amdgcn_raw_buffer_load_b16(mkrs(gb2, 2048 * 2), (64 * tt + 8 * w + l8) * 2, 0, 0);
    vmwait<0>();
    ld4(gf[0], l16, sws, so);
    ld4(gf[1], l16, sws, so + 32768);
#pragma unroll
    for (int j = 0; j < 6; ++j) {
      if (j < 5 || w < 3) {
        bf16x8 wl[4];
        if (j >= 3) take_w2(j, wl);
        if (j == 4 && w >= 3) vmwait<0>();
        if (j < 5) take4<4>(gf[j & 1], l16, sws, so + j * 32768, spins); else take4<0>(gf[j & 1], l16, sws, so + j * 32768, spins);
#pragma unroll
        for (int d = 0; d < 4; ++d)
#pragma unroll
          for (int n = 0; n < 4; ++n) acc[d][n] = mfma16(asfrag(gf[j & 1][d]), j < 3 ? w2f[j < 3 ? j : 0][n] : wl[n], acc[d][n]);
        pin();
        if (j + 2 < 5 || (j + 2 == 5 && w < 3)) ld4(gf[j & 1], l16, sws, so + (j + 2) * 32768);
      }
      pin();
    }
    STAMP(13);
    asm volatile("s_waitcnt lgkmcnt(0)" ::: "memory");
#pragma unroll
    for (int d = 0; d < 4; ++d)
#pragma unroll
      for (int n = 0; n < 4; ++n) {
        const u32 base = n < 3 ? w * 12288 + n * 4096 : 98304 + w * 4096;
        lds_wf4(base + (lane & 15) * 256 + (((4 * d + g4) ^ (lane & 15)) * 16), acc[d][n]);
      }
    bar();
    const int tok = 8 * w + l8;
    const float bb = bflo(b2v);
    f32x4 s0 = {bflo(hres.x) + bb, bfhi(hres.x) + bb, bflo(hres.y) + bb, bfhi(hres.y) + bb};
    f32x4 s1 = {bflo(hres.z) + bb, bfhi(hres.z) + bb, bflo(hres.w) + bb, bfhi(hres.w) + bb};
    f32x4 t0 = {0.f, 0.f, 0.f, 0.f}, t1 = t0;
    const u32 rb = (w < 6 ? tok * 256 : 98304 + (tok - 48) * 256), rstride = w < 6 ? 12288 : 4096;
#pragma unroll
    for (int k = 0; k < 8; ++k) {
      t0 += lds_rf4(rb + k * rstride + (((2 * c8) ^ (tok & 15)) * 16));
      t1 += lds_rf4(rb + k * rstride + (((2 * c8 + 1) ^ (tok & 15)) * 16));
    }
    s0 += t0; s1 += t1;
    const v4u ov = {cvtpk(s0.x, s0.y), cvtpk(s0.z, s0.w), cvtpk(s1.x, s1.y), cvtpk(s1.z, s1.w)};
    *(v4u*)(gout + (64 * tt + tok) * 512 + cg * 64 + c8 * 8) = ov;
  }
  STAMP(14);
}

extern "C" void kernel_launch(void* const* d_in, const int* in_sizes, int n_in, void* d_out, int out_size, void* d_ws, size_t ws_size,
                              hipStream_t stream) {
  (void)hipFuncSetAttribute((const void*)mega, hipFuncAttributeMaxDynamicSharedMemorySize, LDS_BYTES);
  mega<<<256, 512, LDS_BYTES, stream>>>((const u16*)d_in[0], (const u16*)d_in[1], (const u16*)d_in[2], (const u16*)d_in[4], (const u16*)d_in[5],
                                        (const u16*)d_in[6], (const u16*)d_in[7], (u16*)d_out, (char*)d_ws);
}
```

```cpp
#include <hip/hip_runtime.h>
#include <stdint.h>

typedef unsigned u32;
typedef unsigned short u16;
typedef u32 v4u __attribute__((ext_vector_type(4)));
typedef u32 v2u __attribute__((ext_vector_type(2)));
typedef int v4i __attribute__((ext_vector_type(4)));
typedef float f32x2 __attribute__((ext_vector_type(2)));
typedef float f32x4 __attribute__((ext_vector_type(4)));
typedef float f32x16 __attribute__((ext_vector_type(16)));
typedef short bf16x8 __attribute__((ext_vector_type(8)));
typedef short s16x4 __attribute__((ext_vector_type(4)));
typedef __bf16 bf2 __attribute__((ext_vector_type(2)));
#define LAS __attribute__((address_space(3)))
#define DI __device__ __forceinline__

#define CODE_PF 0
template <bool B> struct BoolC { static constexpr bool value = B; };
#define STAMPS 0

constexpr u32 WS_EPOCH = 0, WS_FLAGA = 1u << 19, WS_FLAGB = WS_FLAGA + 32768, WS_FLAGW = WS_FLAGB + 32768, WS_DBG = 65536;
constexpr u32 WS_KV = 1u << 20;
constexpr u32 WS_O = WS_KV + (4u << 20);
constexpr u32 WS_HN = WS_O + (2u << 20);
constexpr u32 WS_G = WS_HN + (2u << 20);
constexpr u32 WS_ST = WS_G + (2u << 20);
constexpr u32 WS_PAD = WS_ST + (1u << 17);
constexpr u32 WS_W1T = WS_PAD + (1u << 18);
constexpr u32 WS_END = WS_W1T + 29 * 64 * 3072;
constexpr u32 SENT = 0xFFFFFFFFu;
constexpr u32 OOB = 0x7FFF0000u;
constexpr int SPIN_CAP = 1 << 16;

constexpr u32 L_XN = 0;
constexpr u32 KVT = 8448;
constexpr u32 L_CNT = 84480;
constexpr u32 L_RING = 86016;
constexpr u32 L_JUNK = L_RING + 65536;
constexpr u32 L_WO = 98304;
constexpr u32 L_PC = 0;
constexpr u32 PD_PITCH = 208, PD_SLOT = 64 * PD_PITCH;
constexpr u32 L_PD = 110592;
constexpr u32 LDS_BYTES = 163840;

DI u32 cvtpk(float a, float b) { f32x2 v = {a, b}; bf2 r = __builtin_convertvector(v, bf2); return __builtin_bit_cast(u32, r); }
DI float bflo(u32 x) { return __uint_as_float(x << 16); }
DI float bfhi(u32 x) { return __uint_as_float(x & 0xffff0000u); }
DI u32 guard(u32 x) { return x < 0xFFFFFFFEu ? x : 0xFFFFFFFEu; }
DI v4u guard4(v4u v) { v4u r = {guard(v.x), guard(v.y), guard(v.z), guard(v.w)}; return r; }
DI u32 umax3(u32 a, u32 b, u32 c) { u32 m = a > b ? a : b; return m > c ? m : c; }
DI u32 smax4(u32 m, v4u v) { return umax3(umax3(m, v.x, v.y), v.z, v.w); }
DI bool any_sent(u32 m) { return __builtin_amdgcn_ballot_w64(m == SENT) != 0; }
DI bf16x8 asfrag(v4u v) { return __builtin_bit_cast(bf16x8, v); }
DI f32x16 mfma32(bf16x8 a, bf16x8 b, f32x16 c) { return __builtin_amdgcn_mfma_f32_32x32x16_bf16(a, b, c, 0, 0, 0); }
DI f32x4 mfma16(bf16x8 a, bf16x8 b, f32x4 c) { return __builtin_amdgcn_mfma_f32_16x16x32_bf16(a, b, c, 0, 0, 0); }
DI v4u lds_r128(u32 a) { return *(const LAS v4u*)(uintptr_t)a; }
DI f32x4 lds_rf4(u32 a) { return *(const LAS f32x4*)(uintptr_t)a; }
DI void lds_w128(u32 a, v4u v) { *(LAS v4u*)(uintptr_t)a = v; }
DI void lds_wf4(u32 a, f32x4 v) { *(LAS f32x4*)(uintptr_t)a = v; }
DI bf16x8 lds_tr(u32 a0, u32 a1) {
  s16x4 lo = __builtin_amdgcn_ds_read_tr16_b64_v4i16((LAS s16x4*)(uintptr_t)a0);
  s16x4 hi = __builtin_amdgcn_ds_read_tr16_b64_v4i16((LAS s16x4*)(uintptr_t)a1);
  return __builtin_shufflevector(lo, hi, 0, 1, 2, 3, 4, 5, 6, 7);
}
template <int CTRL> DI float dpp(float v) { return __int_as_float(__builtin_amdgcn_update_dpp(0, __float_as_int(v), CTRL, 0xf, 0xf, true)); }
DI float sum8(float v) { v += dpp<0xB1>(v); v += dpp<0x4E>(v); v += dpp<0x141>(v); return v; }
DI v4i mksrd(const void* p, u32 bytes) { v4i s = {(int)(u32)(uintptr_t)p, (int)(u32)((uintptr_t)p >> 32), (int)bytes, 0x00020000}; return s; }
DI __amdgpu_buffer_rsrc_t mkrs(const void* p, u32 bytes) { return __builtin_amdgcn_make_buffer_rsrc((void*)p, 0, bytes, 0x00020000); }
DI void dma16(u32 lds, u32 voff, v4i srd, u32 soff) {
  asm volatile("s_mov_b32 m0, %0\n\ts_nop 0\n\tbuffer_load_dwordx4 %1, %2, %3 offen lds" ::"s"(lds), "v"(voff), "s"(srd), "s"(soff) : "memory");
}
DI v4u aload_sc1(u32 voff, v4i srd, u32 soff) {
  v4u r;
  asm volatile("buffer_load_dwordx4 %0, %1, %2, %3 offen sc1" : "=v"(r) : "v"(voff), "s"(srd), "s"(soff) : "memory");
  return r;
}
DI v4u aload(u32 voff, v4i srd, u32 soff) {
  v4u r;
  asm volatile("buffer_load_dwordx4 %0, %1, %2, %3 offen" : "=v"(r) : "v"(voff), "s"(srd), "s"(soff) : "memory");
  return r;
}
DI void ld4(v4u (&f)[4], u32 voff, v4i srd, u32 soff) {
  asm volatile("buffer_load_dwordx4 %0, %4, %5, %6 offen sc1\n\tbuffer_load_dwordx4 %1, %4, %5, %6 offen offset:1024 sc1\n\t"
               "buffer_load_dwordx4 %2, %4, %5, %6 offen offset:2048 sc1\n\tbuffer_load_dwordx4 %3, %4, %5, %6 offen offset:3072 sc1"
               : "=&v"(f[0]), "=&v"(f[1]), "=&v"(f[2]), "=&v"(f[3]) : "v"(voff), "s"(srd), "s"(soff) : "memory");
}
template <int N> DI void take4(v4u (&f)[4], u32 voff, v4i srd, u32 soff, int& spins) {
  asm volatile("s_waitcnt vmcnt(%4)" : "+v"(f[0]), "+v"(f[1]), "+v"(f[2]), "+v"(f[3]) : "n"(N) : "memory");
  if (__builtin_expect(any_sent(smax4(smax4(smax4(smax4(0, f[0]), f[1]), f[2]), f[3])), 0)) {
#pragma clang loop unroll(disable)
    do {
      asm volatile("s_sleep 1\n\tbuffer_load_dwordx4 %0, %4, %5, %6 offen sc1\n\tbuffer_load_dwordx4 %1, %4, %5, %6 offen offset:1024 sc1\n\t"
                   "buffer_load_dwordx4 %2, %4, %5, %6 offen offset:2048 sc1\n\tbuffer_load_dwordx4 %3, %4, %5, %6 offen offset:3072 sc1\n\t"
                   "s_waitcnt vmcnt(0)"
                   : "+v"(f[0]), "+v"(f[1]), "+v"(f[2]), "+v"(f[3]) : "v"(voff), "s"(srd), "s"(soff) : "memory");
    } while (any_sent(smax4(smax4(smax4(smax4(0, f[0]), f[1]), f[2]), f[3])) && ++spins < SPIN_CAP);
  }
}
template <int N> DI void vmwait() { asm volatile("s_waitcnt vmcnt(%0)" ::"n"(N) : "memory"); }
DI void bar() { asm volatile("s_waitcnt lgkmcnt(0)\n\ts_barrier" ::: "memory"); }
DI v4u ld_sc1(__amdgpu_buffer_rsrc_t rs, u32 voff, u32 soff) { return __builtin_amdgcn_raw_buffer_load_b128(rs, voff, soff, 16); }
DI void st_sc1(v4u v, v4i srd, u32 voff, u32 soff) {
  asm volatile("buffer_store_dwordx4 %0, %1, %2, %3 offen sc1\n\ts_nop 1" ::"v"(v), "v"(voff), "s"(srd), "s"(soff) : "memory");
}
DI void pin() { __builtin_amdgcn_sched_barrier(0); }

#define STAMP(i) do {} while (0)
#define CSTAMP(i) do {} while (0)

DI void max16(float (&m)[4], const f32x16& s) {
#pragma unroll
  for (int j = 0; j < 8; ++j) m[j & 3] = fmaxf(fmaxf(m[j & 3], s[2 * j]), s[2 * j + 1]);
}
DI v4u pack8(const float* p) { return v4u{cvtpk(p[0], p[1]), cvtpk(p[2], p[3]), cvtpk(p[4], p[5]), cvtpk(p[6], p[7])}; }
DI float gelu_tanh(float x) {
  const float c1 = -2.0f * 0.7978845608028654f * 1.4426950408889634f, c2 = c1 * 0.044715f;
  const float e = __builtin_amdgcn_exp2f(x * (c1 + c2 * x * x));
  return x * __builtin_amdgcn_rcpf(1.0f + e);
}
struct Frag4 { bf16x8 x[4], w[4]; };
template <bool XFIRST>
DI void gemm4(f32x16& acc, const Frag4& c, Frag4& n, const u32 (&xa)[4], u32 xo, u32 wo) {
#pragma unroll
  for (int s = 0; s < 4; ++s) {
    acc = XFIRST ? mfma32(c.x[s], c.w[s], acc) : mfma32(c.w[s], c.x[s], acc);
    n.x[s] = asfrag(lds_r128(xa[s] + xo));
    n.w[s] = lds_tr(wo + s * 2048, wo + s * 2048 + 512);
    pin();
  }
}
DI void read4(Frag4& n, const u32 (&xa)[4], u32 xo, u32 wo) {
#pragma unroll
  for (int s = 0; s < 4; ++s) { n.x[s] = asfrag(lds_r128(xa[s] + xo)); n.w[s] = lds_tr(wo + s * 2048, wo + s * 2048 + 512); }
}
DI void ld3(v4u (&f)[3], u32 voff, v4i srd, u32 soff) {
  asm volatile("buffer_load_dwordx4 %0, %3, %4, %5 offen sc1\n\tbuffer_load_dwordx4 %1, %3, %4, %5 offen offset:1024 sc1\n\t"
               "buffer_load_dwordx4 %2, %3, %4, %5 offen offset:2048 sc1"
               : "=&v"(f[0]), "=&v"(f[1]), "=&v"(f[2]) : "v"(voff), "s"(srd), "s"(soff) : "memory");
}
DI void landed3(v4u (&f)[3]) { asm volatile("" : "+v"(f[0]), "+v"(f[1]), "+v"(f[2])); }
DI void signal(u32 cnt, int lane, __amdgpu_buffer_rsrc_t rws, u32 flag, u32 tag) {
  u32 old = 0;
  if (lane == 0) old = __hip_atomic_fetch_add((LAS u32*)(uintptr_t)cnt, 1u, __ATOMIC_RELAXED, __HIP_MEMORY_SCOPE_WORKGROUP);
  if (__builtin_amdgcn_readfirstlane(old) == 7 && lane == 0) __builtin_amdgcn_raw_buffer_store_b32(tag, rws, flag, 0, 16);
}
DI int wg_of(int tt, int cg) { return ((tt & 7) + 8 * (cg & 3)) * 8 + 2 * (tt >> 3) + (cg >> 2); }
DI u32 ld_flag(u32 off, v4i sws) {
  u32 f;
  asm volatile("buffer_load_dword %0, %1, %2, 0 offen sc1" : "=v"(f) : "v"(off), "s"(sws) : "memory");
  return f;
}
DI void wait_flag(u32& f, u32 off, v4i sws, u32 tag, int& spins) {
  if (__builtin_expect(__builtin_amdgcn_ballot_w64(off != OOB && f != tag) != 0, 0)) {
#pragma clang loop unroll(disable)
    do {
      asm volatile("s_sleep 2\n\tbuffer_load_dword %0, %1, %2, 0 offen sc1\n\ts_waitcnt vmcnt(0)" : "+v"(f) : "v"(off), "s"(sws) : "memory");
    } while (__builtin_amdgcn_ballot_w64(off != OOB && f != tag) != 0 && ++spins < SPIN_CAP);
  }
}

__global__ void __launch_bounds__(512, 2)
mega(const u16* __restrict__ gx, const u16* __restrict__ gwqkv, const u16* __restrict__ gwo, const u16* __restrict__ gw1,
     const u16* __restrict__ gb1, const u16* __restrict__ gw2, const u16* __restrict__ gb2, u16* __restrict__ gout, char* ws) {
  const int tid = threadIdx.x, lane = tid & 63;
  const int w = __builtin_amdgcn_readfirstlane(tid >> 6);
  const int wg = blockIdx.x, xcd = wg & 7, loc = wg >> 3;
  const int tt = 8 * (xcd >> 1) + (loc & 7), cg = 4 * (xcd & 1) + (loc >> 3);
  const int dt = 4 * (xcd & 1) + (loc & 3), fq = xcd >> 1, fi = loc >> 2;
  const int fg = fq == 0 ? fi : 1 + 7 * fq + fi;
  const bool don = fq == 0 || fi < 7;
  const int rot = ((loc & 7) + 2 * (loc >> 3)) & 7, drot = (fi + 2 * (loc & 3)) & 7;
  STAMP(0);
  const u32 l16 = lane * 16;
  const int l8 = lane >> 3, c8 = lane & 7;
  const v4i sx = mksrd(gx, 2048 * 512 * 2), swqkv = mksrd(gwqkv, 512 * 1536 * 2);
  const __amdgpu_buffer_rsrc_t rws = mkrs(ws, WS_END);
  const v4i sws = mksrd(ws, WS_END);
  const v4u sent4 = {SENT, SENT, SENT, SENT};
  int spins = 0;

  v4u xr[8];
  {
    const u32 vo = (64 * tt + 8 * w + l8) * 1024 + c8 * 16;
#pragma unroll
    for (int i = 0; i < 4; ++i) xr[i] = aload(vo, sx, ((i + rot) & 7) * 128);
#pragma unroll
    for (int i = 4; i < 8; ++i) xr[i] = aload(vo, sx, ((i + rot) & 7) * 128);
  }
  const int grp = loc & 3;
  auto clear_kv = [&](int g) {
    const u32 vo = grp == g ? tid * 16 : OOB;
    st_sc1(sent4, sws, vo, WS_KV + (tt * 8 + cg) * 16384);
    st_sc1(sent4, sws, vo, WS_KV + (tt * 8 + cg) * 16384 + 8192);
  };
  u32 epoch;
  asm volatile("s_load_dword %0, %1, 0x0" : "=s"(epoch) : "s"(ws) : "memory");
  const u32 wvq = (8 * w + l8) * 3072 + ((c8 ^ (((l8 >> 1) & 1) << 2)) * 16) + cg * 128, wvk = wvq + 1024, wvv = wvq + 2048;
  u32 ko[8];
#pragma unroll
  for (int i = 0; i < 8; ++i) ko[i] = ((i + rot) & 7) * (64 * 3072);
  auto issue_w = [&](int st) {
    const u32 slot = L_RING + (st & 3) * 16384 + w * 1024;
    dma16(slot, st < 8 ? wvk : wvq, swqkv, st < 8 ? ko[st & 7] : ko[(2 * st) & 7]);
    dma16(slot + 8192, st < 8 ? wvv : wvq, swqkv, st < 8 ? ko[st & 7] : ko[(2 * st + 1) & 7]);
  };
  const int r32 = lane & 31, h = lane >> 5, blk = (lane >> 4) & 1, q4 = (lane & 15) >> 2, p4 = lane & 3;
  const int hh = w & 3, tb = w >> 2;
  const int isV = w & 1, hp = (w >> 1) & 1;
  u32 xa[4];
#pragma unroll
  for (int s = 0; s < 4; ++s) xa[s] = L_XN + (32 * tb + r32) * 128 + (((2 * s + h) ^ ((r32 >> 1) & 7)) * 16);
  u32 wa = L_RING + isV * 8192 + (8 * h + q4) * 128 + (((4 * hp + 2 * blk + (p4 >> 1)) ^ (((q4 >> 1) & 1) << 2)) * 16) + 8 * (p4 & 1);
  asm volatile("" : "+v"(xa[0]), "+v"(xa[1]), "+v"(xa[2]), "+v"(xa[3]), "+v"(wa));
  constexpr int PF = CODE_PF ? 1 : 0;
  {
    f32x2 xv[32];
    f32x2 s1a = {0.f, 0.f}, s1b = s1a, s2a = s1a, s2b = s1a, shift;
    auto acc4 = [&](int i) {
      const u32 d[4] = {xr[i].x, xr[i].y, xr[i].z, xr[i].w};
#pragma unroll
      for (int j = 0; j < 4; ++j) {
        const f32x2 a = f32x2{bflo(d[j]), bfhi(d[j])} - shift;
        xv[4 * i + j] = a;
        if (j & 1) { s1b += a; s2b = __builtin_elementwise_fma(a, a, s2b); } else { s1a += a; s2a = __builtin_elementwise_fma(a, a, s2a); }
      }
    };
    CSTAMP(40);
    asm volatile("s_waitcnt vmcnt(%2)" : "+v"(xr[0]), "+v"(xr[1]) : "n"(6 + PF) : "memory");
    CSTAMP(41);
    {
      const float sh0 = dpp<0x00>(bflo(xr[0].x)), sh1 = dpp<0x141>(sh0), sh = (lane & 4) ? sh1 : sh0;
      shift = f32x2{sh, sh};
    }
    acc4(0); acc4(1); pin();
    issue_w(0);
    asm volatile("s_waitcnt vmcnt(%2)" : "+v"(xr[2]), "+v"(xr[3]) : "n"(6 + PF) : "memory");
    CSTAMP(43);
    acc4(2); acc4(3); pin();
    issue_w(1); clear_kv(0);
    asm volatile("s_waitcnt vmcnt(8)" : "+v"(xr[4]), "+v"(xr[5])::"memory");
    CSTAMP(45);
    acc4(4); acc4(5); pin();
    issue_w(2);
    asm volatile("s_waitcnt vmcnt(8)" : "+v"(xr[6]), "+v"(xr[7])::"memory");
    CSTAMP(47);
    acc4(6); acc4(7);
    s1a += s1b; s2a += s2b;
    const float s1 = sum8(s1a.x + s1a.y) * (1.f / 512.f), s2 = sum8(s2a.x + s2a.y) * (1.f / 512.f);
    const float rs = __builtin_amdgcn_rsqf(fmaxf(s2 - s1 * s1, 0.f) + 1e-5f), nb = -s1 * rs;
    const f32x2 rs2 = {rs, rs}, nb2 = {nb, nb};
    const int row = 8 * w + l8;
    const u32 xw = L_XN + row * 128 + ((c8 ^ ((row >> 1) & 7)) * 16);
#pragma unroll
    for (int i = 0; i < 8; ++i) {
      u32 o[4];
#pragma unroll
      for (int j = 0; j < 4; ++j) { const f32x2 y = __builtin_elementwise_fma(xv[4 * i + j], rs2, nb2); o[j] = cvtpk(y.x, y.y); }
      lds_w128(xw + i * 8192, v4u{o[0], o[1], o[2], o[3]});
    }
    if (tid < 3) *(LAS u32*)(uintptr_t)(L_CNT + tid * 4) = 0;
    pin();
    issue_w(3); clear_kv(1);
    CSTAMP(48);
  }
  asm volatile("s_waitcnt lgkmcnt(0)" : "+s"(epoch)::"memory");
  const u32 tag = epoch + 1;
  STAMP(1);

  Frag4 fa, fb;
  const u32 qwa = L_RING + (8 * h + q4) * 128 + (((2 * hh + (p4 >> 1)) ^ (((q4 >> 1) & 1) << 2)) * 16) + 8 * (p4 & 1);
  const int rks = wg & 63, rfg = 8 * (wg >> 6) + w;
  {
    f32x16 acc;
#pragma unroll
    for (int i = 0; i < 16; ++i) acc[i] = 0.f;
    vmwait<10>(); bar();
    CSTAMP(51);
    auto loop = [&](auto xfirst) {
      constexpr bool XF = decltype(xfirst)::value;
      read4(fa, xa, 0, wa);
      vmwait<8>(); bar(); issue_w(4); clear_kv(2);
      gemm4<XF>(acc, fa, fb, xa, 1 * 8192, wa + 1 * 16384);
      vmwait<8>(); bar(); issue_w(5); clear_kv(3);
      gemm4<XF>(acc, fb, fa, xa, 2 * 8192, wa + 2 * 16384);
      CSTAMP(0);
      vmwait<10>(); bar(); issue_w(6);
      gemm4<XF>(acc, fa, fb, xa, 3 * 8192, wa + 3 * 16384);
      vmwait<8>(); bar(); issue_w(7);
      gemm4<XF>(acc, fb, fa, xa, 4 * 8192, wa);
      CSTAMP(2);
      vmwait<6>(); bar(); issue_w(8);
      gemm4<XF>(acc, fa, fb, xa, 5 * 8192, wa + 1 * 16384);
      vmwait<4>(); bar(); issue_w(9);
      if (tid == 0) __builtin_amdgcn_raw_buffer_store_b32(tag, rws, WS_FLAGA + wg * 128, 0, 16);
      gemm4<XF>(acc, fb, fa, xa, 6 * 8192, wa + 2 * 16384);
      CSTAMP(4);
      vmwait<4>(); bar();
      gemm4<XF>(acc, fa, fb, xa, 7 * 8192, wa + 3 * 16384);
      vmwait<2>(); bar();
      gemm4<XF>(acc, fb, fa, xa, 0, qwa);
      CSTAMP(6);
      const v4u a = guard4(v4u{cvtpk(acc[0], acc[1]), cvtpk(acc[2], acc[3]), cvtpk(acc[4], acc[5]), cvtpk(acc[6], acc[7])});
      const v4u b = guard4(v4u{cvtpk(acc[8], acc[9]), cvtpk(acc[10], acc[11]), cvtpk(acc[12], acc[13]), cvtpk(acc[14], acc[15])});
      const u32 oa = decltype(xfirst)::value ? (2 * hp + (r32 >> 4)) * 2048 + 1024 + h * 256 + (r32 & 15) * 16 : 2 * hp * 2048 + l16;
      const u32 ob = oa + (decltype(xfirst)::value ? 512 : 2048);
      const u32 gso = WS_KV + (tt * 8 + cg) * 16384 + tb * 8192;
      st_sc1(a, sws, oa, gso);
      st_sc1(b, sws, ob, gso);
      issue_w(10); issue_w(11);
      lds_w128((8 + tb) * KVT + oa, a);
      lds_w128((8 + tb) * KVT + ob, b);
    };
    if (isV) loop(BoolC<true>{}); else loop(BoolC<false>{});
    STAMP(2);
  }
  bf16x8 qf;
  v4u halo[2][4];
  u32 hso[2], hvo[2];
  {
    const int ptt = tt - 4 + (lane & 3);
    const u32 fao = (ptt >= 0 && lane < 4) ? WS_FLAGA + wg_of(ptt, cg) * 128 : OOB;
    u32 fl = ld_flag(fao, sws);
    f32x16 acc;
#pragma unroll
    for (int i = 0; i < 16; ++i) acc[i] = 0.f;
    gemm4<false>(acc, fa, fb, xa, 1 * 8192, qwa + 1 * 8192);
    vmwait<7>(); bar();
    gemm4<false>(acc, fb, fa, xa, 2 * 8192, qwa + 2 * 8192);
    gemm4<false>(acc, fa, fb, xa, 3 * 8192, qwa + 3 * 8192);
    CSTAMP(7);
    vmwait<3>(); bar();
    CSTAMP(8);
    {
      asm volatile("s_waitcnt vmcnt(0)" : "+v"(fl)::"memory");
      wait_flag(fl, fao, sws, tag, spins);
#pragma unroll
      for (int i = 0; i < 2; ++i) {
        const int pt = tt - 4 + 2 * (w >> 2) + i;
        hso[i] = pt >= 0 ? WS_KV + (pt * 8 + cg) * 16384 + (w & 3) * 4096 : 0;
        hvo[i] = pt >= 0 ? l16 : OOB;
        ld4(halo[i], hvo[i], sws, hso[i]);
      }
    }
    CSTAMP(9);
    gemm4<false>(acc, fb, fa, xa, 4 * 8192, qwa + 4 * 8192);
    gemm4<false>(acc, fa, fb, xa, 5 * 8192, qwa + 5 * 8192);
    bar();
    gemm4<false>(acc, fb, fa, xa, 6 * 8192, qwa + 6 * 8192);
    gemm4<false>(acc, fa, fb, xa, 7 * 8192, qwa + 7 * 8192);
#pragma unroll
    for (int s = 0; s < 4; ++s) acc = mfma32(fb.w[s], fb.x[s], acc);
    const float qs = 0.25f * 1.4426950408889634f;
    qf = asfrag(v4u{cvtpk(acc[0] * qs, acc[1] * qs), cvtpk(acc[2] * qs, acc[3] * qs), cvtpk(acc[4] * qs, acc[5] * qs), cvtpk(acc[6] * qs, acc[7] * qs)});
  }
  bar();
  STAMP(3);
  const int g4 = lane >> 4;
  {
    if (tid < 160) { const v4u one4 = {0x3F803F80u, 0x3F803F80u, 0x3F803F80u, 0x3F803F80u}; lds_w128((tid >> 4) * KVT + 8192 + (tid & 15) * 16, one4); }
    CSTAMP(10);
    take4<4>(halo[0], hvo[0], sws, hso[0], spins);
    CSTAMP(11);
#pragma unroll
    for (int u = 0; u < 4; ++u) lds_w128((4 * (w >> 2) + ((w & 3) >> 1)) * KVT + (4 * (w & 1) + u) * 1024 + l16, halo[0][u]);
    take4<0>(halo[1], hvo[1], sws, hso[1], spins);
    CSTAMP(12);
#pragma unroll
    for (int u = 0; u < 4; ++u) lds_w128((4 * (w >> 2) + 2 + ((w & 3) >> 1)) * KVT + (4 * (w & 1) + u) * 1024 + l16, halo[1][u]);
  }
  const u32 g_ab = WS_G + (dt * 43 + fg) * 4096, g_c = WS_G + (dt * 43 + 29 + (fg >> 1)) * 4096 + (fg & 1) * 512;
  const v4i sw2 = mksrd(gw2, 1376 * 2048 * 2);
  const u32 w2vo = (8 * (l8 >> 2) + (l8 & 3)) * 4096 + ((c8 ^ (((l8 >> 1) & 3) << 1)) * 16);
  auto dma_w2 = [&](int step, u32 slot, int u) {
    const u32 r0 = step < 29 ? step * 48 : (step - 29) * 96 + 32, r1 = r0 + (step < 29 ? 16 : 48);
    dma16(slot + u * 1024, w2vo, sw2, (((u & 1) ? r1 : r0) + 4 * (u >> 1)) * 4096 + tt * 128);
  };
  auto issue_w2 = [&](int step, u32 slot) {
#pragma unroll
    for (int u = 0; u < 4; ++u) dma_w2(step, slot, u);
  };
  v4u w1f[8][3];
  const u32 w1vo = don ? l16 : OOB;
  auto ld_w1 = [&](int s) { ld3(w1f[s], w1vo, sws, WS_W1T + ((don ? fg : 0) * 64 + 8 * w + ((s + drot) & 7)) * 3072); };

  {
    const int jmin = tt >= 4 ? 0 : 8 - 2 * tt;
    const u32 kb = tb * KVT + hh * 2048 + l16;
    const u32 ones = tb * KVT + 8192 + ((r32 >= 20 && r32 < 28) ? 64 : 0);
    u32 vb0 = r32 < 16 ? tb * KVT + hh * 2048 + 1024 + h * 256 + r32 * 16 : ones;
    u32 vb1 = r32 < 16 ? vb0 + 512 : ones;
    u32 gclr = !don ? OOB : tid < 256 ? g_ab + tid * 16 : (tid < 384 && fg < 28) ? g_c + ((tid - 256) >> 5) * 1024 + (tid & 31) * 16 : OOB;
    u32 sclr = tid < 32 ? tid * 16 : OOB;
    asm volatile("" : "+v"(vb0), "+v"(vb1), "+v"(gclr), "+v"(sclr));
    f32x16 z16;
#pragma unroll
    for (int i = 0; i < 16; ++i) z16[i] = 0.f;
    const int ca = w >> 2, ckq = w & 3;
    const u32 wos = L_WO + (4 * ckq + 2 * ca) * 4096;
    v4u raw[3];
    {
      const v4i sw1 = mksrd(gw1, 2048 * 1376 * 2);
#pragma unroll
      for (int u = 0; u < 3; ++u) {
        const int q = 64 * u + lane, row = q / 6;
        int c = q - 6 * row;
        if (rfg == 28 && c >= 4) c -= 2;
        raw[u] = aload(rfg < 29 ? row * 2752 + c * 16 : OOB, sw1, rks * (32 * 2752) + (rfg < 29 ? rfg : 0) * 96);
      }
    }
    auto clear_b = [&](int g) {
      if (grp == g) {
        st_sc1(sent4, sws, l16, WS_O + tt * 65536 + cg * 4096 + (w >> 2) * 32768 + (w & 3) * 1024);
        st_sc1(sent4, sws, l16, WS_HN + (cg * 64 + 2 * tt) * 4096 + w * 1024);
        st_sc1(sent4, sws, gclr, 0);
        st_sc1(sent4, sws, sclr, WS_ST + (tt * 8 + cg) * 512);
      }
    };
    bar();
    STAMP(4);
    bf16x8 kf[9];
#pragma unroll
    for (int i = 1; i <= 9; ++i) kf[i % 9] = asfrag(lds_r128(kb + (i % 9) * KVT));
    v4u v0 = lds_r128(vb0 + 8 * KVT), v1 = lds_r128(vb1 + 8 * KVT), v2 = lds_r128(vb0), v3 = lds_r128(vb1);
    pin();
    clear_b(0);
    float m4[4] = {-3.0e38f, -3.0e38f, -3.0e38f, -3.0e38f};
    const int thr = r32 - 4 * h;
    f32x16 sa = mfma32(kf[1], qf, z16), sb = mfma32(kf[2], qf, z16), sc = mfma32(kf[3], qf, z16);
    pin();
    if (tb + 1 >= jmin) max16(m4, sa);
    pin(); sa = mfma32(kf[4], qf, z16); pin();
    if (tb + 2 >= jmin) max16(m4, sb);
    pin(); sb = mfma32(kf[5], qf, z16); pin();
    if (tb + 3 >= jmin) max16(m4, sc);
    pin(); sc = mfma32(kf[6], qf, z16); pin();
    if (tb + 4 >= jmin) max16(m4, sa);
    pin(); sa = mfma32(kf[7], qf, z16); pin();
    clear_b(1);
    if (tb + 5 >= jmin) max16(m4, sb);
    pin(); sb = mfma32(kf[8], qf, z16); pin();
    if (tb + 6 >= jmin) max16(m4, sc);
    pin(); sc = mfma32(kf[0], qf, z16); pin();
    if (tb + 7 >= jmin) max16(m4, sa);
    pin();
    if (tb < jmin) {
#pragma unroll
      for (int i = 0; i < 16; ++i) sc[i] = -3.0e38f;
    }
    f32x16 sm;
#pragma unroll
    for (int i = 0; i < 16; ++i) sm[i] = ((i & 3) + 8 * (i >> 2)) <= thr ? sb[i] : sc[i];
    max16(m4, sm);
    float mx = fmaxf(fmaxf(m4[0], m4[1]), fmaxf(m4[2], m4[3]));
    {
      const u32 a = __float_as_uint(mx);
      const auto sw = __builtin_amdgcn_permlane32_swap(a, a, false, false);
      const u32 s0 = sw[0], s1 = sw[1];
      mx = fmaxf(__uint_as_float(s0), __uint_as_float(s1));
    }
    CSTAMP(13);
    clear_b(2);
    f32x16 nm;
#pragma unroll
    for (int i = 0; i < 16; ++i) nm[i] = -mx;
    f32x16 sn = mfma32(kf[1], qf, nm);
    pin();
    f32x16 o = z16;
    {
      float pl[16], pf[16];
#pragma unroll
      for (int i = 0; i < 16; ++i) {
        const float p = __builtin_amdgcn_exp2f(sm[i] - mx);
        pl[i] = ((i & 3) + 8 * (i >> 2)) <= thr ? p : 0.f;
        pf[i] = p - pl[i];
      }
      o = mfma32(asfrag(v0), asfrag(pack8(pl)), o);
      o = mfma32(asfrag(v1), asfrag(pack8(pl + 8)), o);
      o = mfma32(asfrag(v2), asfrag(pack8(pf)), o);
      o = mfma32(asfrag(v3), asfrag(pack8(pf + 8)), o);
    }
    pin();
    v4u rfr[3];
    {
      if (grp == 3) vmwait<0>();
      asm volatile("s_waitcnt vmcnt(4)" : "+v"(raw[0]), "+v"(raw[1]), "+v"(raw[2])::"memory");
      clear_b(3);
#pragma unroll
      for (int u = 0; u < 3; ++u) lds_w128(wos + u * 1024 + l16, raw[u]);
      const u32 rd = wos + (4 * g4 + q4) * 96 + 8 * p4;
#pragma unroll
      for (int b = 0; b < 3; ++b) rfr[b] = __builtin_bit_cast(v4u, lds_tr(rd + 32 * b, rd + 32 * b + 16 * 96));
    }
    CSTAMP(14);
    const v4i swo = mksrd(gwo, 512 * 512 * 2);
    const u32 wovo = l8 * 1024 + ((c8 ^ (((l8 >> 1) & 3) << 1)) * 16);
    auto dma_wo = [&](int u) {
      const int uu = (u + loc) & 3, kk = ((u >> 2) + (loc >> 2)) & 1;
      dma16(wos + kk * 4096 + uu * 1024, wovo, swo, (4 * ckq + 2 * ca + kk) * 32768 + (16 * (uu & 1) + 8 * (uu >> 1)) * 1024 + cg * 128);
    };
    const u32 rvo = rfg < 29 ? l16 : OOB;
#pragma unroll
    for (int i = 1; i <= 7; ++i) {
      v0 = lds_r128(vb0 + i * KVT); v1 = lds_r128(vb1 + i * KVT);
      const f32x16 s = sn;
      if (i < 7) sn = mfma32(kf[i + 1], qf, nm);
      pin();
      if (i >= 2 && i <= 5) dma_wo(2 * i - 4);
      if (tb + i >= jmin) {
        float p[16];
#pragma unroll
        for (int j = 0; j < 8; ++j) p[j] = __builtin_amdgcn_exp2f(s[j]);
        o = mfma32(asfrag(v0), asfrag(pack8(p)), o);
        pin();
#pragma unroll
        for (int j = 8; j < 16; ++j) p[j] = __builtin_amdgcn_exp2f(s[j]);
        o = mfma32(asfrag(v1), asfrag(pack8(p + 8)), o);
      }
      pin();
      if (i == 1) asm volatile("s_waitcnt lgkmcnt(0)" : "+v"(rfr[0]), "+v"(rfr[1]), "+v"(rfr[2])::"memory");
      if (i >= 2 && i <= 5) dma_wo(2 * i - 3);
      if (i == 3) {
        vmwait<4>();
        signal(L_CNT, lane, rws, WS_FLAGB + wg * 128, tag);
      }
      if (i >= 3 && i <= 6 && grp == i - 3) {
#pragma unroll
        for (int b = 0; b < 3; ++b) st_sc1(rfr[b], sws, rvo, WS_W1T + ((rfg < 29 ? rfg : 0) * 64 + rks) * 3072 + b * 1024);
      }
      CSTAMP(14 + i);
    }
    const float inv = __builtin_amdgcn_rcpf(o[8]);
    const v4u of = guard4(v4u{cvtpk(o[0] * inv, o[1] * inv), cvtpk(o[2] * inv, o[3] * inv), cvtpk(o[4] * inv, o[5] * inv), cvtpk(o[6] * inv, o[7] * inv)});
    st_sc1(of, sws, (r32 >> 4) * 1024 + ((2 * (hh & 1) + h) * 16 + (r32 & 15)) * 16, WS_O + tt * 65536 + tb * 32768 + (2 * cg + (hh >> 1)) * 2048);
  }
  STAMP(5);

  u32 tra[4];
#pragma unroll
  for (int n = 0; n < 4; ++n) tra[n] = (4 * g4 + q4) * 128 + 8 * (p4 & 1) + (((2 * n + (p4 >> 1)) ^ ((((4 * g4 + q4) >> 1) & 3) << 1)) * 16);
  v4u hres, b1v;
  u32 b2v;
  {
    v4u xres = aload((64 * tt + 8 * w + l8) * 1024 + cg * 128 + c8 * 16, mksrd(gx, 2048 * 512 * 2), 0);
    asm volatile("buffer_load_ushort %0, %1, %2, 0 offen" : "=v"(b2v) : "v"((64 * tt + 8 * w + l8) * 2), "s"(mksrd(gb2, 2048 * 2)) : "memory");
    b1v = aload((don && w < 6) ? (48 * fg + 16 * ((2 * w + h) >> 2) + 8 * blk) * 2 : OOB, mksrd(gb1, 1376 * 2), 0);
    const u32 fbo = (w == 0 && lane < 8) ? WS_FLAGB + wg_of(tt, lane) * 128 : OOB;
    u32 fl = ld_flag(fbo, sws);
    const int a = w >> 2, kq = w & 3;
    asm volatile("s_waitcnt vmcnt(0)" : "+v"(fl), "+v"(xres), "+v"(b1v), "+v"(b2v)::"memory");
    CSTAMP(22);
    signal(L_CNT + 4, lane, rws, WS_FLAGW + wg * 128, tag);
    wait_flag(fl, fbo, sws, tag, spins);
    bar();
    v4u ofr[2][4];
    const u32 oso = WS_O + tt * 65536 + a * 32768 + kq * 8192;
    ld4(ofr[0], l16, sws, oso);
    ld4(ofr[1], l16, sws, oso + 4096);
    u32 fwo = OOB;
    {
      const int f8 = lane - 8, pq = lane < 8 ? 0 : 1 + f8 / 7, pi = lane < 8 ? lane : f8 - 7 * (pq - 1);
      if (w == 0 && don) fwo = WS_FLAGW + ((fg >> 3) * 64 + lane) * 128;
      if (w == 1 && don && lane < 32) fwo = WS_FLAGB + wg_of(lane, dt) * 128;
      if (w == 2 && lane < 29) fwo = WS_FLAGB + (((cg & 3) + 4 * pi) * 8 + 2 * pq + (cg >> 2)) * 128;
      if (w >= 4 && wg == 0) fwo = WS_FLAGA + ((w - 4) * 64 + lane) * 128;
    }
    u32 flw = ld_flag(fwo, sws);
    STAMP(6);
    bf16x8 wf[4][4];
#pragma unroll
    for (int k = 0; k < 4; ++k)
#pragma unroll
      for (int n = 0; n < 4; ++n) { const u32 ad = L_WO + (4 * kq + k) * 4096 + tra[n]; wf[k][n] = lds_tr(ad, ad + 2048); }
    pin();
    f32x4 acc[2][4];
#pragma unroll
    for (int m = 0; m < 2; ++m)
#pragma unroll
      for (int n = 0; n < 4; ++n) acc[m][n] = f32x4{0.f, 0.f, 0.f, 0.f};
#pragma unroll
    for (int k = 0; k < 4; ++k) {
      if (k == 0) take4<5>(ofr[0], l16, sws, oso, spins);
      if (k == 2) take4<1>(ofr[1], l16, sws, oso + 4096, spins);
#pragma unroll
      for (int m = 0; m < 2; ++m)
#pragma unroll
        for (int n = 0; n < 4; ++n) acc[m][n] = mfma16(wf[k][n], asfrag(ofr[k >> 1][2 * (k & 1) + m]), acc[m][n]);
      pin();
    }
    asm volatile("s_waitcnt vmcnt(0)" : "+v"(flw)::"memory");
    wait_flag(flw, fwo, sws, tag, spins);
    STAMP(7);
#pragma unroll
    for (int m = 0; m < 2; ++m)
#pragma unroll
      for (int n = 0; n < 4; ++n) {
        const int tok = 32 * a + 16 * m + (lane & 15);
        lds_wf4(L_PC + kq * 16384 + tok * 256 + (((4 * n + g4) ^ (lane & 15)) * 16), acc[m][n]);
      }
    bar();
    if (wg == 0 && tid == 0) __builtin_amdgcn_raw_buffer_store_b32(tag, rws, WS_EPOCH, 0, 16);
    const int tok = 8 * w + l8;
    f32x4 t[8];
#pragma unroll
    for (int k = 0; k < 4; ++k) {
      t[2 * k] = lds_rf4(L_PC + k * 16384 + tok * 256 + (((2 * c8) ^ (tok & 15)) * 16));
      t[2 * k + 1] = lds_rf4(L_PC + k * 16384 + tok * 256 + (((2 * c8 + 1) ^ (tok & 15)) * 16));
    }
    pin();
    ld_w1(0); ld_w1(1);
    f32x4 s0 = {bflo(xres.x), bfhi(xres.x), bflo(xres.y), bfhi(xres.y)}, s1 = {bflo(xres.z), bfhi(xres.z), bflo(xres.w), bfhi(xres.w)};
    s0 += (t[0] + t[2]) + (t[4] + t[6]); s1 += (t[1] + t[3]) + (t[5] + t[7]);
    hres = v4u{cvtpk(s0.x, s0.y), cvtpk(s0.z, s0.w), cvtpk(s1.x, s1.y), cvtpk(s1.z, s1.w)};
  }
  {
    float hv[8] = {bflo(hres.x), bfhi(hres.x), bflo(hres.y), bfhi(hres.y), bflo(hres.z), bfhi(hres.z), bflo(hres.w), bfhi(hres.w)};
    const float pm = sum8(((hv[0] + hv[1]) + (hv[2] + hv[3])) + ((hv[4] + hv[5]) + (hv[6] + hv[7]))) * (1.f / 64.f);
    float q0 = 0.f, q1 = 0.f;
#pragma unroll
    for (int i = 0; i < 8; i += 2) { q0 = fmaf(hv[i] - pm, hv[i] - pm, q0); q1 = fmaf(hv[i + 1] - pm, hv[i + 1] - pm, q1); }
    const float q = sum8(q0 + q1);
    const v2u stv = {guard(__float_as_uint(pm)), guard(__float_as_uint(q))};
    __builtin_amdgcn_raw_buffer_store_b64(stv, rws, c8 == 0 ? (8 * w + l8) * 8 : OOB, WS_ST + (tt * 8 + cg) * 512, 16);
    STAMP(8);
    ld_w1(2); ld_w1(3);
    vmwait<6>();
    const u32 sto = c8 * 512 + (8 * w + l8) * 8;
    v2u st;
    asm volatile("buffer_load_dwordx2 %0, %1, %2, %3 offen sc1" : "=v"(st) : "v"(sto), "s"(sws), "s"(WS_ST + tt * 4096) : "memory");
    ld_w1(4);
    asm volatile("s_waitcnt vmcnt(3)" : "+v"(st)::"memory");
    if (__builtin_expect(any_sent(st.x > st.y ? st.x : st.y), 0)) {
#pragma clang loop unroll(disable)
      do {
        asm volatile("buffer_load_dwordx2 %0, %1, %2, %3 offen sc1\n\ts_waitcnt vmcnt(0)" : "+v"(st) : "v"(sto), "s"(sws), "s"(WS_ST + tt * 4096) : "memory");
      } while (any_sent(st.x > st.y ? st.x : st.y) && ++spins < SPIN_CAP);
    }
    STAMP(9);
    ld_w1(5);
    const u32 sx_ = st.x, sy_ = st.y;
    const float mc = __uint_as_float(sx_), mean = sum8(mc) * 0.125f;
    const float var = sum8(fmaf(64.f * (mc - mean), mc - mean, __uint_as_float(sy_))) * (1.f / 512.f);
    const float r1 = __builtin_amdgcn_rsqf(var + 1e-5f);
    const float r2 = __builtin_amdgcn_rsqf(var * r1 * r1 + 1e-5f);
    const float sc = r1 * r2;
    const v4u hn = {cvtpk((hv[0] - mean) * sc, (hv[1] - mean) * sc), cvtpk((hv[2] - mean) * sc, (hv[3] - mean) * sc),
                    cvtpk((hv[4] - mean) * sc, (hv[5] - mean) * sc), cvtpk((hv[6] - mean) * sc, (hv[7] - mean) * sc)};
    lds_w128(L_PC + w * 2048 + l8 * 128 + ((c8 ^ (((l8 >> 1) & 3) << 1)) * 16), hn);
    bar();
    const int a = w >> 2, n = w & 3, row = 4 * g4 + q4;
    const u32 ad = L_PC + (4 * a + (row >> 3)) * 2048 + (row & 7) * 128 + (((2 * n + (p4 >> 1)) ^ (((row >> 1) & 3) << 1)) * 16) + 8 * (p4 & 1);
    const v4u fr = guard4(__builtin_bit_cast(v4u, lds_tr(ad, ad + 2 * 2048)));
    st_sc1(fr, sws, l16 + n * 1024, WS_HN + (cg * 64 + 2 * tt + a) * 4096);
    STAMP(10);
    ld_w1(6); ld_w1(7);
    bar();
  }
  const u32 area = w < 3 ? w * 16384 : 12288 + w * 12288;
  const u32 late = L_PD + (w < 3 ? w * 4096 : w * 8192 - 12288);
  auto take_w2 = [&](u32 slot, bf16x8 (&f)[4]) {
#pragma unroll
    for (int n = 0; n < 4; ++n) f[n] = lds_tr(slot + tra[n], slot + tra[n] + 2048);
  };
  auto issue_w2_early = [&]() {
    const int j = (loc >> 3) & 1;
    issue_w2(w + 8 + 8 * j, area + 4096 + j * 4096);
    issue_w2(w + 16 - 8 * j, area + 8192 - j * 4096);
    if (w < 3) issue_w2(w + 24, area + 12288);
  };
  if (don) {
    f32x4 acc[3][4];
#pragma unroll
    for (int b = 0; b < 3; ++b)
#pragma unroll
      for (int d = 0; d < 4; ++d) acc[b][d] = f32x4{0.f, 0.f, 0.f, 0.f};
    auto so = [&](int s) { return WS_HN + (dt * 64 + 8 * w + ((s + drot) & 7)) * 4096; };
    v4u hf[4][4];
    vmwait<6>();
#pragma unroll
    for (int s = 0; s < 4; ++s) ld4(hf[s], l16, sws, so(s));
#pragma unroll
    for (int s = 0; s < 8; ++s) {
      if (s <= 4) take4<12>(hf[s & 3], l16, sws, so(s), spins);
      else if (s == 5) take4<8>(hf[s & 3], l16, sws, so(s), spins);
      else if (s == 6) take4<4>(hf[s & 3], l16, sws, so(s), spins);
      else take4<0>(hf[s & 3], l16, sws, so(s), spins);
      if (s == 0) {
#pragma unroll
        for (int i = 0; i < 8; ++i) landed3(w1f[i]);
      }
      if (s == 7) issue_w2(w, area);
#pragma unroll
      for (int d = 0; d < 4; ++d)
#pragma unroll
        for (int b = 0; b < 3; ++b) acc[b][d] = mfma16(asfrag(w1f[s][b]), asfrag(hf[s & 3][d]), acc[b][d]);
      pin();
      if (s + 4 < 8) ld4(hf[s & 3], l16, sws, so(s + 4));
      pin();
      CSTAMP(26 + s);
    }
    STAMP(11);
    const u32 slot = L_PD + (w & 3) * PD_SLOT;
    if (w >= 4) {
#pragma unroll
      for (int b = 0; b < 3; ++b)
#pragma unroll
        for (int d = 0; d < 4; ++d) lds_wf4(slot + (b * 4 + d) * 1024 + l16, acc[b][d]);
    }
    bar();
    CSTAMP(34);
    if (w >= 4) issue_w2_early();
    if (w < 4) {
      f32x4 t[12];
#pragma unroll
      for (int i = 0; i < 12; ++i) t[i] = lds_rf4(slot + i * 1024 + l16);
      pin();
#pragma unroll
      for (int i = 0; i < 12; ++i) acc[i >> 2][i & 3] += t[i];
      asm volatile("" ::: "memory");
#pragma unroll
      for (int b = 0; b < 3; ++b)
#pragma unroll
        for (int d = 0; d < 4; ++d) lds_wf4(slot + (16 * d + (lane & 15)) * PD_PITCH + (4 * b + g4) * 16, acc[b][d]);
    }
    bar();
    CSTAMP(35);
    u32 go = lane == 0 ? WS_PAD + (wg * 8 + w) * 128 : OOB;
    v4u gv = sent4;
    if (w < 6) {
      const int hf2 = 2 * w + h, b = hf2 >> 2, d = hf2 & 3;
      const u32 ad = L_PD + (16 * d + (lane & 15)) * PD_PITCH + (4 * b + 2 * blk) * 16;
      f32x4 t[8];
#pragma unroll
      for (int k = 0; k < 4; ++k) { t[2 * k] = lds_rf4(ad + k * PD_SLOT); t[2 * k + 1] = lds_rf4(ad + k * PD_SLOT + 16); }
      pin();
      f32x4 s0 = {bflo(b1v.x), bfhi(b1v.x), bflo(b1v.y), bfhi(b1v.y)}, s1 = {bflo(b1v.z), bfhi(b1v.z), bflo(b1v.w), bfhi(b1v.w)};
      s0 += (t[0] + t[2]) + (t[4] + t[6]); s1 += (t[1] + t[3]) + (t[5] + t[7]);
      gv = guard4(v4u{cvtpk(gelu_tanh(s0.x), gelu_tanh(s0.y)), cvtpk(gelu_tanh(s0.z), gelu_tanh(s0.w)),
                      cvtpk(gelu_tanh(s1.x), gelu_tanh(s1.y)), cvtpk(gelu_tanh(s1.z), gelu_tanh(s1.w))});
      go = b < 2 ? g_ab + d * 1024 + ((2 * b + blk) * 16 + (lane & 15)) * 16
                 : (fg < 28 ? g_c + d * 1024 + (blk * 16 + (lane & 15)) * 16 : go);
    }
    st_sc1(gv, sws, go, 0);
    STAMP(12);
    bar();
    if (w < 4) issue_w2_early();
  } else {
    vmwait<0>();
    issue_w2(w, area);
    issue_w2_early();
    __builtin_amdgcn_s_sleep(100);
    STAMP(12);
  }
  {
    if (w < 3) issue_w2(w + 32, late);
    else { issue_w2(w + 24, late); issue_w2(w + 32, late + 4096); }
    f32x4 acc[4][4];
#pragma unroll
    for (int d = 0; d < 4; ++d)
#pragma unroll
      for (int n = 0; n < 4; ++n) acc[d][n] = f32x4{0.f, 0.f, 0.f, 0.f};
    const u32 so = WS_G + (cg * 43 + w) * 4096;
    v4u gf[4][4];
    bf16x8 wc[4];
    if (w >= 4) vmwait<8>();
    vmwait<16>();
    CSTAMP(36);
#pragma unroll
    for (int j = 0; j < 4; ++j) ld4(gf[j], l16, sws, so + j * 32768);
    take_w2(area, wc);
    CSTAMP(37);
#pragma unroll
    for (int j = 0; j < 6; ++j) {
      if (j < 5 || w < 3) {
        if (w >= 3) { if (j == 2) vmwait<8>(); else if (j == 3) vmwait<4>(); else if (j == 4) vmwait<0>(); }
        if (j <= 1) take4<12>(gf[j & 3], l16, sws, so + j * 32768, spins);
        else if (j == 2) take4<16>(gf[j & 3], l16, sws, so + j * 32768, spins);
        else if (j == 3) take4<12>(gf[j & 3], l16, sws, so + j * 32768, spins);
        else if (j == 4) { if (w < 3) vmwait<4>(); take4<8>(gf[j & 3], l16, sws, so + j * 32768, spins); }
        else take4<0>(gf[j & 3], l16, sws, so + j * 32768, spins);
        bf16x8 wn[4];
        if (j < 4 || (j == 4 && w < 3))
          take_w2(j < 2 ? area + 4096 + j * 4096 : j == 2 ? (w < 3 ? area + 12288 : late) : j == 3 ? (w < 3 ? late : late + 4096) : area, wn);
#pragma unroll
        for (int d = 0; d < 4; ++d)
#pragma unroll
          for (int n = 0; n < 4; ++n) acc[d][n] = mfma16(asfrag(gf[j & 3][d]), wc[n], acc[d][n]);
        pin();
        if (j == 0) ld4(gf[0], l16, sws, so + 4 * 32768);
        if (j == 1 && w < 3) { issue_w2(w + 40, area); ld4(gf[1], l16, sws, so + 5 * 32768); }
#pragma unroll
        for (int n = 0; n < 4; ++n) wc[n] = wn[n];
      }
      pin();
      CSTAMP(52 + j);
    }
    STAMP(13);
    asm volatile("s_waitcnt lgkmcnt(0)" ::: "memory");
#pragma unroll
    for (int d = 0; d < 4; ++d)
#pragma unroll
      for (int n = 0; n < 4; ++n) {
        const u32 base = n < 3 ? area + n * 4096 : late;
        lds_wf4(base + (lane & 15) * 256 + (((4 * d + g4) ^ (lane & 15)) * 16), acc[d][n]);
      }
    bar();
    CSTAMP(58);
    const int tok = 8 * w + l8;
    f32x4 t[16];
    const u32 rb = (tok & 15) * 256 + (((2 * c8) ^ (tok & 15)) * 16);
#pragma unroll
    for (int k = 0; k < 8; ++k) {
      const u32 ak = k < 3 ? k * 16384 : 12288 + k * 12288, lk = L_PD + (k < 3 ? k * 4096 : k * 8192 - 12288);
      const u32 ko2 = w < 6 ? ak + (w >> 1) * 4096 : lk;
      t[2 * k] = lds_rf4(rb + ko2);
      t[2 * k + 1] = lds_rf4((rb ^ 16) + ko2);
    }
    pin();
    const float bb = bflo(b2v);
    f32x4 s0 = {bflo(hres.x) + bb, bfhi(hres.x) + bb, bflo(hres.y) + bb, bfhi(hres.y) + bb};
    f32x4 s1 = {bflo(hres.z) + bb, bfhi(hres.z) + bb, bflo(hres.w) + bb, bfhi(hres.w) + bb};
    s0 += ((t[0] + t[2]) + (t[4] + t[6])) + ((t[8] + t[10]) + (t[12] + t[14]));
    s1 += ((t[1] + t[3]) + (t[5] + t[7])) + ((t[9] + t[11]) + (t[13] + t[15]));
    const v4u ov = {cvtpk(s0.x, s0.y), cvtpk(s0.z, s0.w), cvtpk(s1.x, s1.y), cvtpk(s1.z, s1.w)};
    asm volatile(".Lmega_pf_end:");
    *(v4u*)(gout + (64 * tt + tok) * 512 + cg * 64 + c8 * 8) = ov;
  }
  STAMP(14);
}

extern "C" void kernel_launch(void* const* d_in, const int* in_sizes, int n_in, void* d_out, int out_size, void* d_ws, size_t ws_size,
                              hipStream_t stream) {
  (void)hipFuncSetAttribute((const void*)mega, hipFuncAttributeMaxDynamicSharedMemorySize, LDS_BYTES);
  mega<<<256, 512, LDS_BYTES, stream>>>((const u16*)d_in[0], (const u16*)d_in[1], (const u16*)d_in[2], (const u16*)d_in[4], (const u16*)d_in[5],
                                        (const u16*)d_in[6], (const u16*)d_in[7], (u16*)d_out, (char*)d_ws);
}
```

```cpp
#include <hip/hip_runtime.h>
#include <stdint.h>

typedef unsigned u32;
typedef unsigned short u16;
typedef u32 v4u __attribute__((ext_vector_type(4)));
typedef u32 v2u __attribute__((ext_vector_type(2)));
typedef int v4i __attribute__((ext_vector_type(4)));
typedef float f32x2 __attribute__((ext_vector_type(2)));
typedef float f32x4 __attribute__((ext_vector_type(4)));
typedef float f32x16 __attribute__((ext_vector_type(16)));
typedef short bf16x8 __attribute__((ext_vector_type(8)));
typedef short s16x4 __attribute__((ext_vector_type(4)));
typedef __bf16 bf2 __attribute__((ext_vector_type(2)));
#define LAS __attribute__((address_space(3)))
#define DI __device__ __forceinline__

#define CODE_PF 1
template <bool B> struct BoolC { static constexpr bool value = B; };
#define STAMPS 0

constexpr u32 WS_EPOCH = 0, WS_FLAGA = 1u << 19, WS_FLAGB = WS_FLAGA + 32768, WS_FLAGW = WS_FLAGB + 32768, WS_DBG = 65536;
constexpr u32 WS_KV = 1u << 20;
constexpr u32 WS_O = WS_KV + (4u << 20);
constexpr u32 WS_HN = WS_O + (2u << 20);
constexpr u32 WS_G = WS_HN + (2u << 20);
constexpr u32 WS_ST = WS_G + (2u << 20);
constexpr u32 WS_PAD = WS_ST + (1u << 17);
constexpr u32 WS_W1T = WS_PAD + (1u << 18);
constexpr u32 WS_END = WS_W1T + 29 * 64 * 3072;
constexpr u32 SENT = 0xFFFFFFFFu;
constexpr u32 OOB = 0x7FFF0000u;
constexpr int SPIN_CAP = 1 << 16;

constexpr u32 L_XN = 0;
constexpr u32 KVT = 8448;
constexpr u32 L_CNT = 84480;
constexpr u32 L_RING = 86016;
constexpr u32 L_JUNK = L_RING + 65536;
constexpr u32 L_WO = 98304;
constexpr u32 L_PC = 0;
constexpr u32 PD_PITCH = 208, PD_SLOT = 64 * PD_PITCH;
constexpr u32 L_PD = 110592;
constexpr u32 LDS_BYTES = 163840;

DI u32 cvtpk(float a, float b) { f32x2 v = {a, b}; bf2 r = __builtin_convertvector(v, bf2); return __builtin_bit_cast(u32, r); }
DI float bflo(u32 x) { return __uint_as_float(x << 16); }
DI float bfhi(u32 x) { return __uint_as_float(x & 0xffff0000u); }
DI u32 guard(u32 x) { return x < 0xFFFFFFFEu ? x : 0xFFFFFFFEu; }
DI v4u guard4(v4u v) { v4u r = {guard(v.x), guard(v.y), guard(v.z), guard(v.w)}; return r; }
DI u32 umax3(u32 a, u32 b, u32 c) { u32 m = a > b ? a : b; return m > c ? m : c; }
DI u32 smax4(u32 m, v4u v) { return umax3(umax3(m, v.x, v.y), v.z, v.w); }
DI bool any_sent(u32 m) { return __builtin_amdgcn_ballot_w64(m == SENT) != 0; }
DI bf16x8 asfrag(v4u v) { return __builtin_bit_cast(bf16x8, v); }
DI f32x16 mfma32(bf16x8 a, bf16x8 b, f32x16 c) { return __builtin_amdgcn_mfma_f32_32x32x16_bf16(a, b, c, 0, 0, 0); }
DI f32x4 mfma16(bf16x8 a, bf16x8 b, f32x4 c) { return __builtin_amdgcn_mfma_f32_16x16x32_bf16(a, b, c, 0, 0, 0); }
DI v4u lds_r128(u32 a) { return *(const LAS v4u*)(uintptr_t)a; }
DI f32x4 lds_rf4(u32 a) { return *(const LAS f32x4*)(uintptr_t)a; }
DI void lds_w128(u32 a, v4u v) { *(LAS v4u*)(uintptr_t)a = v; }
DI void lds_wf4(u32 a, f32x4 v) { *(LAS f32x4*)(uintptr_t)a = v; }
DI bf16x8 lds_tr(u32 a0, u32 a1) {
  s16x4 lo = __builtin_amdgcn_ds_read_tr16_b64_v4i16((LAS s16x4*)(uintptr_t)a0);
  s16x4 hi = __builtin_amdgcn_ds_read_tr16_b64_v4i16((LAS s16x4*)(uintptr_t)a1);
  return __builtin_shufflevector(lo, hi, 0, 1, 2, 3, 4, 5, 6, 7);
}
template <int CTRL> DI float dpp(float v) { return __int_as_float(__builtin_amdgcn_update_dpp(0, __float_as_int(v), CTRL, 0xf, 0xf, true)); }
DI float sum8(float v) { v += dpp<0xB1>(v); v += dpp<0x4E>(v); v += dpp<0x141>(v); return v; }
DI v4i mksrd(const void* p, u32 bytes) { v4i s = {(int)(u32)(uintptr_t)p, (int)(u32)((uintptr_t)p >> 32), (int)bytes, 0x00020000}; return s; }
DI __amdgpu_buffer_rsrc_t mkrs(const void* p, u32 bytes) { return __builtin_amdgcn_make_buffer_rsrc((void*)p, 0, bytes, 0x00020000); }
DI void dma16(u32 lds, u32 voff, v4i srd, u32 soff) {
  asm volatile("s_mov_b32 m0, %0\n\ts_nop 0\n\tbuffer_load_dwordx4 %1, %2, %3 offen lds" ::"s"(lds), "v"(voff), "s"(srd), "s"(soff) : "memory");
}
DI v4u aload_sc1(u32 voff, v4i srd, u32 soff) {
  v4u r;
  asm volatile("buffer_load_dwordx4 %0, %1, %2, %3 offen sc1" : "=v"(r) : "v"(voff), "s"(srd), "s"(soff) : "memory");
  return r;
}
DI v4u aload(u32 voff, v4i srd, u32 soff) {
  v4u r;
  asm volatile("buffer_load_dwordx4 %0, %1, %2, %3 offen" : "=v"(r) : "v"(voff), "s"(srd), "s"(soff) : "memory");
  return r;
}
DI void ld4(v4u (&f)[4], u32 voff, v4i srd, u32 soff) {
  asm volatile("buffer_load_dwordx4 %0, %4, %5, %6 offen sc1\n\tbuffer_load_dwordx4 %1, %4, %5, %6 offen offset:1024 sc1\n\t"
               "buffer_load_dwordx4 %2, %4, %5, %6 offen offset:2048 sc1\n\tbuffer_load_dwordx4 %3, %4, %5, %6 offen offset:3072 sc1"
               : "=&v"(f[0]), "=&v"(f[1]), "=&v"(f[2]), "=&v"(f[3]) : "v"(voff), "s"(srd), "s"(soff) : "memory");
}
template <int N> DI void take4(v4u (&f)[4], u32 voff, v4i srd, u32 soff, int& spins) {
  asm volatile("s_waitcnt vmcnt(%4)" : "+v"(f[0]), "+v"(f[1]), "+v"(f[2]), "+v"(f[3]) : "n"(N) : "memory");
  if (__builtin_expect(any_sent(smax4(smax4(smax4(smax4(0, f[0]), f[1]), f[2]), f[3])), 0)) {
#pragma clang loop unroll(disable)
    do {
      asm volatile("s_sleep 1\n\tbuffer_load_dwordx4 %0, %4, %5, %6 offen sc1\n\tbuffer_load_dwordx4 %1, %4, %5, %6 offen offset:1024 sc1\n\t"
                   "buffer_load_dwordx4 %2, %4, %5, %6 offen offset:2048 sc1\n\tbuffer_load_dwordx4 %3, %4, %5, %6 offen offset:3072 sc1\n\t"
                   "s_waitcnt vmcnt(0)"
                   : "+v"(f[0]), "+v"(f[1]), "+v"(f[2]), "+v"(f[3]) : "v"(voff), "s"(srd), "s"(soff) : "memory");
    } while (any_sent(smax4(smax4(smax4(smax4(0, f[0]), f[1]), f[2]), f[3])) && ++spins < SPIN_CAP);
  }
}
template <int N> DI void vmwait() { asm volatile("s_waitcnt vmcnt(%0)" ::"n"(N) : "memory"); }
DI void bar() { asm volatile("s_waitcnt lgkmcnt(0)\n\ts_barrier" ::: "memory"); }
DI v4u ld_sc1(__amdgpu_buffer_rsrc_t rs, u32 voff, u32 soff) { return __builtin_amdgcn_raw_buffer_load_b128(rs, voff, soff, 16); }
DI void st_sc1(v4u v, v4i srd, u32 voff, u32 soff) {
  asm volatile("buffer_store_dwordx4 %0, %1, %2, %3 offen sc1\n\ts_nop 1" ::"v"(v), "v"(voff), "s"(srd), "s"(soff) : "memory");
}
DI void pin() { __builtin_amdgcn_sched_barrier(0); }

#define STAMP(i) do {} while (0)
#define CSTAMP(i) do {} while (0)

DI void max16(float (&m)[4], const f32x16& s) {
#pragma unroll
  for (int j = 0; j < 8; ++j) m[j & 3] = fmaxf(fmaxf(m[j & 3], s[2 * j]), s[2 * j + 1]);
}
DI v4u pack8(const float* p) { return v4u{cvtpk(p[0], p[1]), cvtpk(p[2], p[3]), cvtpk(p[4], p[5]), cvtpk(p[6], p[7])}; }
DI float gelu_tanh(float x) {
  const float c1 = -2.0f * 0.7978845608028654f * 1.4426950408889634f, c2 = c1 * 0.044715f;
  const float e = __builtin_amdgcn_exp2f(x * (c1 + c2 * x * x));
  return x * __builtin_amdgcn_rcpf(1.0f + e);
}
struct Frag4 { bf16x8 x[4], w[4]; };
template <bool XFIRST>
DI void gemm4(f32x16& acc, const Frag4& c, Frag4& n, const u32 (&xa)[4], u32 xo, u32 wo) {
#pragma unroll
  for (int s = 0; s < 4; ++s) {
    acc = XFIRST ? mfma32(c.x[s], c.w[s], acc) : mfma32(c.w[s], c.x[s], acc);
    n.x[s] = asfrag(lds_r128(xa[s] + xo));
    n.w[s] = lds_tr(wo + s * 2048, wo + s * 2048 + 512);
    pin();
  }
}
DI void read4(Frag4& n, const u32 (&xa)[4], u32 xo, u32 wo) {
#pragma unroll
  for (int s = 0; s < 4; ++s) { n.x[s] = asfrag(lds_r128(xa[s] + xo)); n.w[s] = lds_tr(wo + s * 2048, wo + s * 2048 + 512); }
}
DI void ld3(v4u (&f)[3], u32 voff, v4i srd, u32 soff) {
  asm volatile("buffer_load_dwordx4 %0, %3, %4, %5 offen sc1\n\tbuffer_load_dwordx4 %1, %3, %4, %5 offen offset:1024 sc1\n\t"
               "buffer_load_dwordx4 %2, %3, %4, %5 offen offset:2048 sc1"
               : "=&v"(f[0]), "=&v"(f[1]), "=&v"(f[2]) : "v"(voff), "s"(srd), "s"(soff) : "memory");
}
DI void landed3(v4u (&f)[3]) { asm volatile("" : "+v"(f[0]), "+v"(f[1]), "+v"(f[2])); }
DI void signal(u32 cnt, int lane, __amdgpu_buffer_rsrc_t rws, u32 flag, u32 tag) {
  u32 old = 0;
  if (lane == 0) old = __hip_atomic_fetch_add((LAS u32*)(uintptr_t)cnt, 1u, __ATOMIC_RELAXED, __HIP_MEMORY_SCOPE_WORKGROUP);
  if (__builtin_amdgcn_readfirstlane(old) == 7 && lane == 0) __builtin_amdgcn_raw_buffer_store_b32(tag, rws, flag, 0, 16);
}
DI int wg_of(int tt, int cg) { return ((tt & 7) + 8 * (cg & 3)) * 8 + 2 * (tt >> 3) + (cg >> 2); }
DI u32 ld_flag(u32 off, v4i sws) {
  u32 f;
  asm volatile("buffer_load_dword %0, %1, %2, 0 offen sc1" : "=v"(f) : "v"(off), "s"(sws) : "memory");
  return f;
}
DI void wait_flag(u32& f, u32 off, v4i sws, u32 tag, int& spins) {
  if (__builtin_expect(__builtin_amdgcn_ballot_w64(off != OOB && f != tag) != 0, 0)) {
#pragma clang loop unroll(disable)
    do {
      asm volatile("s_sleep 2\n\tbuffer_load_dword %0, %1, %2, 0 offen sc1\n\ts_waitcnt vmcnt(0)" : "+v"(f) : "v"(off), "s"(sws) : "memory");
    } while (__builtin_amdgcn_ballot_w64(off != OOB && f != tag) != 0 && ++spins < SPIN_CAP);
  }
}

__global__ void __launch_bounds__(512, 2)
mega(const u16* __restrict__ gx, const u16* __restrict__ gwqkv, const u16* __restrict__ gwo, const u16* __restrict__ gw1,
     const u16* __restrict__ gb1, const u16* __restrict__ gw2, const u16* __restrict__ gb2, u16* __restrict__ gout, char* ws) {
  const int tid = threadIdx.x, lane = tid & 63;
  const int w = __builtin_amdgcn_readfirstlane(tid >> 6);
  const int wg = blockIdx.x, xcd = wg & 7, loc = wg >> 3;
  const int tt = 8 * (xcd >> 1) + (loc & 7), cg = 4 * (xcd & 1) + (loc >> 3);
  const int dt = 4 * (xcd & 1) + (loc & 3), fq = xcd >> 1, fi = loc >> 2;
  const int fg = fq == 0 ? fi : 1 + 7 * fq + fi;
  const bool don = fq == 0 || fi < 7;
  const int rot = ((loc & 7) + 2 * (loc >> 3)) & 7, drot = (fi + 2 * (loc & 3)) & 7;
  STAMP(0);
  const u32 l16 = lane * 16;
  const int l8 = lane >> 3, c8 = lane & 7;
  const v4i sx = mksrd(gx, 2048 * 512 * 2), swqkv = mksrd(gwqkv, 512 * 1536 * 2);
  const __amdgpu_buffer_rsrc_t rws = mkrs(ws, WS_END);
  const v4i sws = mksrd(ws, WS_END);
  const v4u sent4 = {SENT, SENT, SENT, SENT};
  int spins = 0;

  v4u xr[8];
  {
    const u32 vo = (64 * tt + 8 * w + l8) * 1024 + c8 * 16;
#pragma unroll
    for (int i = 0; i < 4; ++i) xr[i] = aload(vo, sx, ((i + rot) & 7) * 128);
    {
      unsigned long long pc;
      int len;
      asm volatile("s_getpc_b64 %0\n.Lmega_pf_start:\n\ts_mov_b32 %1, .Lmega_pf_end-.Lmega_pf_start" : "=s"(pc), "=s"(len));
      dma16(L_JUNK + (w & 1) * 1024, w < 2 ? l16 + (loc * 2 + w) * 1024 : OOB, mksrd((const void*)(pc & ~63ull), len > 0 ? len : 0), 0);
    }
#pragma unroll
    for (int i = 4; i < 8; ++i) xr[i] = aload(vo, sx, ((i + rot) & 7) * 128);
  }
  const int grp = loc & 3;
  auto clear_kv = [&](int g) {
    const u32 vo = grp == g ? tid * 16 : OOB;
    st_sc1(sent4, sws, vo, WS_KV + (tt * 8 + cg) * 16384);
    st_sc1(sent4, sws, vo, WS_KV + (tt * 8 + cg) * 16384 + 8192);
  };
  u32 epoch;
  asm volatile("s_load_dword %0, %1, 0x0" : "=s"(epoch) : "s"(ws) : "memory");
  const u32 wvq = (8 * w + l8) * 3072 + ((c8 ^ (((l8 >> 1) & 1) << 2)) * 16) + cg * 128, wvk = wvq + 1024, wvv = wvq + 2048;
  u32 ko[8];
#pragma unroll
  for (int i = 0; i < 8; ++i) ko[i] = ((i + rot) & 7) * (64 * 3072);
  auto issue_w = [&](int st) {
    const u32 slot = L_RING + (st & 3) * 16384 + w * 1024;
    dma16(slot, st < 8 ? wvk : wvq, swqkv, st < 8 ? ko[st & 7] : ko[(2 * st) & 7]);
    dma16(slot + 8192, st < 8 ? wvv : wvq, swqkv, st < 8 ? ko[st & 7] : ko[(2 * st + 1) & 7]);
  };
  const int r32 = lane & 31, h = lane >> 5, blk = (lane >> 4) & 1, q4 = (lane & 15) >> 2, p4 = lane & 3;
  const int hh = w & 3, tb = w >> 2;
  const int isV = w & 1, hp = (w >> 1) & 1;
  u32 xa[4];
#pragma unroll
  for (int s = 0; s < 4; ++s) xa[s] = L_XN + (32 * tb + r32) * 128 + (((2 * s + h) ^ ((r32 >> 1) & 7)) * 16);
  u32 wa = L_RING + isV * 8192 + (8 * h + q4) * 128 + (((4 * hp + 2 * blk + (p4 >> 1)) ^ (((q4 >> 1) & 1) << 2)) * 16) + 8 * (p4 & 1);
  asm volatile("" : "+v"(xa[0]), "+v"(xa[1]), "+v"(xa[2]), "+v"(xa[3]), "+v"(wa));
  constexpr int PF = CODE_PF ? 1 : 0;
  {
    f32x2 xv[32];
    f32x2 s1a = {0.f, 0.f}, s1b = s1a, s2a = s1a, s2b = s1a, shift;
    auto acc4 = [&](int i) {
      const u32 d[4] = {xr[i].x, xr[i].y, xr[i].z, xr[i].w};
#pragma unroll
      for (int j = 0; j < 4; ++j) {
        const f32x2 a = f32x2{bflo(d[j]), bfhi(d[j])} - shift;
        xv[4 * i + j] = a;
        if (j & 1) { s1b += a; s2b = __builtin_elementwise_fma(a, a, s2b); } else { s1a += a; s2a = __builtin_elementwise_fma(a, a, s2a); }
      }
    };
    CSTAMP(40);
    asm volatile("s_waitcnt vmcnt(%2)" : "+v"(xr[0]), "+v"(xr[1]) : "n"(6 + PF) : "memory");
    CSTAMP(41);
    {
      const float sh0 = dpp<0x00>(bflo(xr[0].x)), sh1 = dpp<0x141>(sh0), sh = (lane & 4) ? sh1 : sh0;
      shift = f32x2{sh, sh};
    }
    acc4(0); acc4(1); pin();
    issue_w(0);
    asm volatile("s_waitcnt vmcnt(%2)" : "+v"(xr[2]), "+v"(xr[3]) : "n"(6 + PF) : "memory");
    CSTAMP(43);
    acc4(2); acc4(3); pin();
    issue_w(1); clear_kv(0);
    asm volatile("s_waitcnt vmcnt(8)" : "+v"(xr[4]), "+v"(xr[5])::"memory");
    CSTAMP(45);
    acc4(4); acc4(5); pin();
    issue_w(2);
    asm volatile("s_waitcnt vmcnt(8)" : "+v"(xr[6]), "+v"(xr[7])::"memory");
    CSTAMP(47);
    acc4(6); acc4(7);
    s1a += s1b; s2a += s2b;
    const float s1 = sum8(s1a.x + s1a.y) * (1.f / 512.f), s2 = sum8(s2a.x + s2a.y) * (1.f / 512.f);
    const float rs = __builtin_amdgcn_rsqf(fmaxf(s2 - s1 * s1, 0.f) + 1e-5f), nb = -s1 * rs;
    const f32x2 rs2 = {rs, rs}, nb2 = {nb, nb};
    const int row = 8 * w + l8;
    const u32 xw = L_XN + row * 128 + ((c8 ^ ((row >> 1) & 7)) * 16);
#pragma unroll
    for (int i = 0; i < 8; ++i) {
      u32 o[4];
#pragma unroll
      for (int j = 0; j < 4; ++j) { const f32x2 y = __builtin_elementwise_fma(xv[4 * i + j], rs2, nb2); o[j] = cvtpk(y.x, y.y); }
      lds_w128(xw + i * 8192, v4u{o[0], o[1], o[2], o[3]});
    }
    if (tid < 3) *(LAS u32*)(uintptr_t)(L_CNT + tid * 4) = 0;
    pin();
    issue_w(3); clear_kv(1);
    CSTAMP(48);
  }
  asm volatile("s_waitcnt lgkmcnt(0)" : "+s"(epoch)::"memory");
  const u32 tag = epoch + 1;
  STAMP(1);

  Frag4 fa, fb;
  const u32 qwa = L_RING + (8 * h + q4) * 128 + (((2 * hh + (p4 >> 1)) ^ (((q4 >> 1) & 1) << 2)) * 16) + 8 * (p4 & 1);
  const int rks = wg & 63, rfg = 8 * (wg >> 6) + w;
  {
    f32x16 acc;
#pragma unroll
    for (int i = 0; i < 16; ++i) acc[i] = 0.f;
    vmwait<10>(); bar();
    CSTAMP(51);
    auto loop = [&](auto xfirst) {
      constexpr bool XF = decltype(xfirst)::value;
      read4(fa, xa, 0, wa);
      vmwait<8>(); bar(); issue_w(4); clear_kv(2);
      gemm4<XF>(acc, fa, fb, xa, 1 * 8192, wa + 1 * 16384);
      vmwait<8>(); bar(); issue_w(5); clear_kv(3);
      gemm4<XF>(acc, fb, fa, xa, 2 * 8192, wa + 2 * 16384);
      CSTAMP(0);
      vmwait<10>(); bar(); issue_w(6);
      gemm4<XF>(acc, fa, fb, xa, 3 * 8192, wa + 3 * 16384);
      vmwait<8>(); bar(); issue_w(7);
      gemm4<XF>(acc, fb, fa, xa, 4 * 8192, wa);
      CSTAMP(2);
      vmwait<6>(); bar(); issue_w(8);
      gemm4<XF>(acc, fa, fb, xa, 5 * 8192, wa + 1 * 16384);
      vmwait<4>(); bar(); issue_w(9);
      if (tid == 0) __builtin_amdgcn_raw_buffer_store_b32(tag, rws, WS_FLAGA + wg * 128, 0, 16);
      gemm4<XF>(acc, fb, fa, xa, 6 * 8192, wa + 2 * 16384);
      CSTAMP(4);
      vmwait<4>(); bar();
      gemm4<XF>(acc, fa, fb, xa, 7 * 8192, wa + 3 * 16384);
      vmwait<2>(); bar();
      gemm4<XF>(acc, fb, fa, xa, 0, qwa);
      CSTAMP(6);
      const v4u a = guard4(v4u{cvtpk(acc[0], acc[1]), cvtpk(acc[2], acc[3]), cvtpk(acc[4], acc[5]), cvtpk(acc[6], acc[7])});
      const v4u b = guard4(v4u{cvtpk(acc[8], acc[9]), cvtpk(acc[10], acc[11]), cvtpk(acc[12], acc[13]), cvtpk(acc[14], acc[15])});
      const u32 oa = decltype(xfirst)::value ? (2 * hp + (r32 >> 4)) * 2048 + 1024 + h * 256 + (r32 & 15) * 16 : 2 * hp * 2048 + l16;
      const u32 ob = oa + (decltype(xfirst)::value ? 512 : 2048);
      const u32 gso = WS_KV + (tt * 8 + cg) * 16384 + tb * 8192;
      st_sc1(a, sws, oa, gso);
      st_sc1(b, sws, ob, gso);
      issue_w(10); issue_w(11);
      lds_w128((8 + tb) * KVT + oa, a);
      lds_w128((8 + tb) * KVT + ob, b);
    };
    if (isV) loop(BoolC<true>{}); else loop(BoolC<false>{});
    STAMP(2);
  }
  bf16x8 qf;
  v4u halo[2][4];
  u32 hso[2], hvo[2];
  {
    const int ptt = tt - 4 + (lane & 3);
    const u32 fao = (ptt >= 0 && lane < 4) ? WS_FLAGA + wg_of(ptt, cg) * 128 : OOB;
    u32 fl = ld_flag(fao, sws);
    f32x16 acc;
#pragma unroll
    for (int i = 0; i < 16; ++i) acc[i] = 0.f;
    gemm4<false>(acc, fa, fb, xa, 1 * 8192, qwa + 1 * 8192);
    vmwait<7>(); bar();
    gemm4<false>(acc, fb, fa, xa, 2 * 8192, qwa + 2 * 8192);
    gemm4<false>(acc, fa, fb, xa, 3 * 8192, qwa + 3 * 8192);
    CSTAMP(7);
    vmwait<3>(); bar();
    CSTAMP(8);
    {
      asm volatile("s_waitcnt vmcnt(0)" : "+v"(fl)::"memory");
      wait_flag(fl, fao, sws, tag, spins);
#pragma unroll
      for (int i = 0; i < 2; ++i) {
        const int pt = tt - 4 + 2 * (w >> 2) + i;
        hso[i] = pt >= 0 ? WS_KV + (pt * 8 + cg) * 16384 + (w & 3) * 4096 : 0;
        hvo[i] = pt >= 0 ? l16 : OOB;
        ld4(halo[i], hvo[i], sws, hso[i]);
      }
    }
    CSTAMP(9);
    gemm4<false>(acc, fb, fa, xa, 4 * 8192, qwa + 4 * 8192);
    gemm4<false>(acc, fa, fb, xa, 5 * 8192, qwa + 5 * 8192);
    bar();
    gemm4<false>(acc, fb, fa, xa, 6 * 8192, qwa + 6 * 8192);
    gemm4<false>(acc, fa, fb, xa, 7 * 8192, qwa + 7 * 8192);
#pragma unroll
    for (int s = 0; s < 4; ++s) acc = mfma32(fb.w[s], fb.x[s], acc);
    const float qs = 0.25f * 1.4426950408889634f;
    qf = asfrag(v4u{cvtpk(acc[0] * qs, acc[1] * qs), cvtpk(acc[2] * qs, acc[3] * qs), cvtpk(acc[4] * qs, acc[5] * qs), cvtpk(acc[6] * qs, acc[7] * qs)});
  }
  bar();
  STAMP(3);
  const int g4 = lane >> 4;
  {
    if (tid < 160) { const v4u one4 = {0x3F803F80u, 0x3F803F80u, 0x3F803F80u, 0x3F803F80u}; lds_w128((tid >> 4) * KVT + 8192 + (tid & 15) * 16, one4); }
    CSTAMP(10);
    take4<4>(halo[0], hvo[0], sws, hso[0], spins);
    CSTAMP(11);
#pragma unroll
    for (int u = 0; u < 4; ++u) lds_w128((4 * (w >> 2) + ((w & 3) >> 1)) * KVT + (4 * (w & 1) + u) * 1024 + l16, halo[0][u]);
    take4<0>(halo[1], hvo[1], sws, hso[1], spins);
    CSTAMP(12);
#pragma unroll
    for (int u = 0; u < 4; ++u) lds_w128((4 * (w >> 2) + 2 + ((w & 3) >> 1)) * KVT + (4 * (w & 1) + u) * 1024 + l16, halo[1][u]);
  }
  const u32 g_ab = WS_G + (dt * 43 + fg) * 4096, g_c = WS_G + (dt * 43 + 29 + (fg >> 1)) * 4096 + (fg & 1) * 512;
  const v4i sw2 = mksrd(gw2, 1376 * 2048 * 2);
  const u32 w2vo = (8 * (l8 >> 2) + (l8 & 3)) * 4096 + ((c8 ^ (((l8 >> 1) & 3) << 1)) * 16);
  auto dma_w2 = [&](int step, u32 slot, int u) {
    const u32 r0 = step < 29 ? step * 48 : (step - 29) * 96 + 32, r1 = r0 + (step < 29 ? 16 : 48);
    dma16(slot + u * 1024, w2vo, sw2, (((u & 1) ? r1 : r0) + 4 * (u >> 1)) * 4096 + tt * 128);
  };
  auto issue_w2 = [&](int step, u32 slot) {
#pragma unroll
    for (int u = 0; u < 4; ++u) dma_w2(step, slot, u);
  };
  v4u w1f[8][3];
  const u32 w1vo = don ? l16 : OOB;
  auto ld_w1 = [&](int s) { ld3(w1f[s], w1vo, sws, WS_W1T + ((don ? fg : 0) * 64 + 8 * w + ((s + drot) & 7)) * 3072); };

  {
    const int jmin = tt >= 4 ? 0 : 8 - 2 * tt;
    const u32 kb = tb * KVT + hh * 2048 + l16;
    const u32 ones = tb * KVT + 8192 + ((r32 >= 20 && r32 < 28) ? 64 : 0);
    u32 vb0 = r32 < 16 ? tb * KVT + hh * 2048 + 1024 + h * 256 + r32 * 16 : ones;
    u32 vb1 = r32 < 16 ? vb0 + 512 : ones;
    u32 gclr = !don ? OOB : tid < 256 ? g_ab + tid * 16 : (tid < 384 && fg < 28) ? g_c + ((tid - 256) >> 5) * 1024 + (tid & 31) * 16 : OOB;
    u32 sclr = tid < 32 ? tid * 16 : OOB;
    asm volatile("" : "+v"(vb0), "+v"(vb1), "+v"(gclr), "+v"(sclr));
    f32x16 z16;
#pragma unroll
    for (int i = 0; i < 16; ++i) z16[i] = 0.f;
    const int ca = w >> 2, ckq = w & 3;
    const u32 wos = L_WO + (4 * ckq + 2 * ca) * 4096;
    v4u raw[3];
    {
      const v4i sw1 = mksrd(gw1, 2048 * 1376 * 2);
#pragma unroll
      for (int u = 0; u < 3; ++u) {
        const int q = 64 * u + lane, row = q / 6;
        int c = q - 6 * row;
        if (rfg == 28 && c >= 4) c -= 2;
        raw[u] = aload(rfg < 29 ? row * 2752 + c * 16 : OOB, sw1, rks * (32 * 2752) + (rfg < 29 ? rfg : 0) * 96);
      }
    }
    auto clear_b = [&](int g) {
      if (grp == g) {
        st_sc1(sent4, sws, l16, WS_O + tt * 65536 + cg * 4096 + (w >> 2) * 32768 + (w & 3) * 1024);
        st_sc1(sent4, sws, l16, WS_HN + (cg * 64 + 2 * tt) * 4096 + w * 1024);
        st_sc1(sent4, sws, gclr, 0);
        st_sc1(sent4, sws, sclr, WS_ST + (tt * 8 + cg) * 512);
      }
    };
    bar();
    STAMP(4);
    bf16x8 kf[9];
#pragma unroll
    for (int i = 1; i <= 9; ++i) kf[i % 9] = asfrag(lds_r128(kb + (i % 9) * KVT));
    v4u v0 = lds_r128(vb0 + 8 * KVT), v1 = lds_r128(vb1 + 8 * KVT), v2 = lds_r128(vb0), v3 = lds_r128(vb1);
    pin();
    clear_b(0);
    float m4[4] = {-3.0e38f, -3.0e38f, -3.0e38f, -3.0e38f};
    const int thr = r32 - 4 * h;
    f32x16 sa = mfma32(kf[1], qf, z16), sb = mfma32(kf[2], qf, z16), sc = mfma32(kf[3], qf, z16);
    pin();
    if (tb + 1 >= jmin) max16(m4, sa);
    pin(); sa = mfma32(kf[4], qf, z16); pin();
    if (tb + 2 >= jmin) max16(m4, sb);
    pin(); sb = mfma32(kf[5], qf, z16); pin();
    if (tb + 3 >= jmin) max16(m4, sc);
    pin(); sc = mfma32(kf[6], qf, z16); pin();
    if (tb + 4 >= jmin) max16(m4, sa);
    pin(); sa = mfma32(kf[7], qf, z16); pin();
    clear_b(1);
    if (tb + 5 >= jmin) max16(m4, sb);
    pin(); sb = mfma32(kf[8], qf, z16); pin();
    if (tb + 6 >= jmin) max16(m4, sc);
    pin(); sc = mfma32(kf[0], qf, z16); pin();
    if (tb + 7 >= jmin) max16(m4, sa);
    pin();
    if (tb < jmin) {
#pragma unroll
      for (int i = 0; i < 16; ++i) sc[i] = -3.0e38f;
    }
    f32x16 sm;
#pragma unroll
    for (int i = 0; i < 16; ++i) sm[i] = ((i & 3) + 8 * (i >> 2)) <= thr ? sb[i] : sc[i];
    max16(m4, sm);
    float mx = fmaxf(fmaxf(m4[0], m4[1]), fmaxf(m4[2], m4[3]));
    {
      const u32 a = __float_as_uint(mx);
      const auto sw = __builtin_amdgcn_permlane32_swap(a, a, false, false);
      const u32 s0 = sw[0], s1 = sw[1];
      mx = fmaxf(__uint_as_float(s0), __uint_as_float(s1));
    }
    CSTAMP(13);
    clear_b(2);
    f32x16 nm;
#pragma unroll
    for (int i = 0; i < 16; ++i) nm[i] = -mx;
    f32x16 sn = mfma32(kf[1], qf, nm);
    pin();
    f32x16 o = z16;
    {
      float pl[16], pf[16];
#pragma unroll
      for (int i = 0; i < 16; ++i) {
        const float p = __builtin_amdgcn_exp2f(sm[i] - mx);
        pl[i] = ((i & 3) + 8 * (i >> 2)) <= thr ? p : 0.f;
        pf[i] = p - pl[i];
      }
      o = mfma32(asfrag(v0), asfrag(pack8(pl)), o);
      o = mfma32(asfrag(v1), asfrag(pack8(pl + 8)), o);
      o = mfma32(asfrag(v2), asfrag(pack8(pf)), o);
      o = mfma32(asfrag(v3), asfrag(pack8(pf + 8)), o);
    }
    pin();
    v4u rfr[3];
    {
      if (grp == 3) vmwait<0>();
      asm volatile("s_waitcnt vmcnt(4)" : "+v"(raw[0]), "+v"(raw[1]), "+v"(raw[2])::"memory");
      clear_b(3);
#pragma unroll
      for (int u = 0; u < 3; ++u) lds_w128(wos + u * 1024 + l16, raw[u]);
      const u32 rd = wos + (4 * g4 + q4) * 96 + 8 * p4;
#pragma unroll
      for (int b = 0; b < 3; ++b) rfr[b] = __builtin_bit_cast(v4u, lds_tr(rd + 32 * b, rd + 32 * b + 16 * 96));
    }
    CSTAMP(14);
    const v4i swo = mksrd(gwo, 512 * 512 * 2);
    const u32 wovo = l8 * 1024 + ((c8 ^ (((l8 >> 1) & 3) << 1)) * 16);
    auto dma_wo = [&](int u) {
      const int uu = (u + loc) & 3, kk = ((u >> 2) + (loc >> 2)) & 1;
      dma16(wos + kk * 4096 + uu * 1024, wovo, swo, (4 * ckq + 2 * ca + kk) * 32768 + (16 * (uu & 1) + 8 * (uu >> 1)) * 1024 + cg * 128);
    };
    const u32 rvo = rfg < 29 ? l16 : OOB;
#pragma unroll
    for (int i = 1; i <= 7; ++i) {
      v0 = lds_r128(vb0 + i * KVT); v1 = lds_r128(vb1 + i * KVT);
      const f32x16 s = sn;
      if (i < 7) sn = mfma32(kf[i + 1], qf, nm);
      pin();
      if (i >= 2 && i <= 5) dma_wo(2 * i - 4);
      if (tb + i >= jmin) {
        float p[16];
#pragma unroll
        for (int j = 0; j < 8; ++j) p[j] = __builtin_amdgcn_exp2f(s[j]);
        o = mfma32(asfrag(v0), asfrag(pack8(p)), o);
        pin();
#pragma unroll
        for (int j = 8; j < 16; ++j) p[j] = __builtin_amdgcn_exp2f(s[j]);
        o = mfma32(asfrag(v1), asfrag(pack8(p + 8)), o);
      }
      pin();
      if (i == 1) asm volatile("s_waitcnt lgkmcnt(0)" : "+v"(rfr[0]), "+v"(rfr[1]), "+v"(rfr[2])::"memory");
      if (i >= 2 && i <= 5) dma_wo(2 * i - 3);
      if (i == 3) {
        vmwait<4>();
        signal(L_CNT, lane, rws, WS_FLAGB + wg * 128, tag);
      }
      if (i >= 3 && i <= 6 && grp == i - 3) {
#pragma unroll
        for (int b = 0; b < 3; ++b) st_sc1(rfr[b], sws, rvo, WS_W1T + ((rfg < 29 ? rfg : 0) * 64 + rks) * 3072 + b * 1024);
      }
      CSTAMP(14 + i);
    }
    const float inv = __builtin_amdgcn_rcpf(o[8]);
    const v4u of = guard4(v4u{cvtpk(o[0] * inv, o[1] * inv), cvtpk(o[2] * inv, o[3] * inv), cvtpk(o[4] * inv, o[5] * inv), cvtpk(o[6] * inv, o[7] * inv)});
    st_sc1(of, sws, (r32 >> 4) * 1024 + ((2 * (hh & 1) + h) * 16 + (r32 & 15)) * 16, WS_O + tt * 65536 + tb * 32768 + (2 * cg + (hh >> 1)) * 2048);
  }
  STAMP(5);

  u32 tra[4];
#pragma unroll
  for (int n = 0; n < 4; ++n) tra[n] = (4 * g4 + q4) * 128 + 8 * (p4 & 1) + (((2 * n + (p4 >> 1)) ^ ((((4 * g4 + q4) >> 1) & 3) << 1)) * 16);
  v4u hres, b1v;
  u32 b2v;
  {
    v4u xres = aload((64 * tt + 8 * w + l8) * 1024 + cg * 128 + c8 * 16, mksrd(gx, 2048 * 512 * 2), 0);
    asm volatile("buffer_load_ushort %0, %1, %2, 0 offen" : "=v"(b2v) : "v"((64 * tt + 8 * w + l8) * 2), "s"(mksrd(gb2, 2048 * 2)) : "memory");
    b1v = aload((don && w < 6) ? (48 * fg + 16 * ((2 * w + h) >> 2) + 8 * blk) * 2 : OOB, mksrd(gb1, 1376 * 2), 0);
    const u32 fbo = (w == 0 && lane < 8) ? WS_FLAGB + wg_of(tt, lane) * 128 : OOB;
    u32 fl = ld_flag(fbo, sws);
    const int a = w >> 2, kq = w & 3;
    asm volatile("s_waitcnt vmcnt(0)" : "+v"(fl), "+v"(xres), "+v"(b1v), "+v"(b2v)::"memory");
    CSTAMP(22);
    signal(L_CNT + 4, lane, rws, WS_FLAGW + wg * 128, tag);
    wait_flag(fl, fbo, sws, tag, spins);
    bar();
    v4u ofr[2][4];
    const u32 oso = WS_O + tt * 65536 + a * 32768 + kq * 8192;
    ld4(ofr[0], l16, sws, oso);
    ld4(ofr[1], l16, sws, oso + 4096);
    u32 fwo = OOB;
    {
      const int f8 = lane - 8, pq = lane < 8 ? 0 : 1 + f8 / 7, pi = lane < 8 ? lane : f8 - 7 * (pq - 1);
      if (w == 0 && don) fwo = WS_FLAGW + ((fg >> 3) * 64 + lane) * 128;
      if (w == 1 && don && lane < 32) fwo = WS_FLAGB + wg_of(lane, dt) * 128;
      if (w == 2 && lane < 29) fwo = WS_FLAGB + (((cg & 3) + 4 * pi) * 8 + 2 * pq + (cg >> 2)) * 128;
      if (w >= 4 && wg == 0) fwo = WS_FLAGA + ((w - 4) * 64 + lane) * 128;
    }
    u32 flw = ld_flag(fwo, sws);
    STAMP(6);
    bf16x8 wf[4][4];
#pragma unroll
    for (int k = 0; k < 4; ++k)
#pragma unroll
      for (int n = 0; n < 4; ++n) { const u32 ad = L_WO + (4 * kq + k) * 4096 + tra[n]; wf[k][n] = lds_tr(ad, ad + 2048); }
    pin();
    f32x4 acc[2][4];
#pragma unroll
    for (int m = 0; m < 2; ++m)
#pragma unroll
      for (int n = 0; n < 4; ++n) acc[m][n] = f32x4{0.f, 0.f, 0.f, 0.f};
#pragma unroll
    for (int k = 0; k < 4; ++k) {
      if (k == 0) take4<5>(ofr[0], l16, sws, oso, spins);
      if (k == 2) take4<1>(ofr[1], l16, sws, oso + 4096, spins);
#pragma unroll
      for (int m = 0; m < 2; ++m)
#pragma unroll
        for (int n = 0; n < 4; ++n) acc[m][n] = mfma16(wf[k][n], asfrag(ofr[k >> 1][2 * (k & 1) + m]), acc[m][n]);
      pin();
    }
    asm volatile("s_waitcnt vmcnt(0)" : "+v"(flw)::"memory");
    wait_flag(flw, fwo, sws, tag, spins);
    STAMP(7);
#pragma unroll
    for (int m = 0; m < 2; ++m)
#pragma unroll
      for (int n = 0; n < 4; ++n) {
        const int tok = 32 * a + 16 * m + (lane & 15);
        lds_wf4(L_PC + kq * 16384 + tok * 256 + (((4 * n + g4) ^ (lane & 15)) * 16), acc[m][n]);
      }
    bar();
    if (wg == 0 && tid == 0) __builtin_amdgcn_raw_buffer_store_b32(tag, rws, WS_EPOCH, 0, 16);
    const int tok = 8 * w + l8;
    f32x4 t[8];
#pragma unroll
    for (int k = 0; k < 4; ++k) {
      t[2 * k] = lds_rf4(L_PC + k * 16384 + tok * 256 + (((2 * c8) ^ (tok & 15)) * 16));
      t[2 * k + 1] = lds_rf4(L_PC + k * 16384 + tok * 256 + (((2 * c8 + 1) ^ (tok & 15)) * 16));
    }
    pin();
    ld_w1(0); ld_w1(1);
    f32x4 s0 = {bflo(xres.x), bfhi(xres.x), bflo(xres.y), bfhi(xres.y)}, s1 = {bflo(xres.z), bfhi(xres.z), bflo(xres.w), bfhi(xres.w)};
    s0 += (t[0] + t[2]) + (t[4] + t[6]); s1 += (t[1] + t[3]) + (t[5] + t[7]);
    hres = v4u{cvtpk(s0.x, s0.y), cvtpk(s0.z, s0.w), cvtpk(s1.x, s1.y), cvtpk(s1.z, s1.w)};
  }
  {
    float hv[8] = {bflo(hres.x), bfhi(hres.x), bflo(hres.y), bfhi(hres.y), bflo(hres.z), bfhi(hres.z), bflo(hres.w), bfhi(hres.w)};
    const float pm = sum8(((hv[0] + hv[1]) + (hv[2] + hv[3])) + ((hv[4] + hv[5]) + (hv[6] + hv[7]))) * (1.f / 64.f);
    float q0 = 0.f, q1 = 0.f;
#pragma unroll
    for (int i = 0; i < 8; i += 2) { q0 = fmaf(hv[i] - pm, hv[i] - pm, q0); q1 = fmaf(hv[i + 1] - pm, hv[i + 1] - pm, q1); }
    const float q = sum8(q0 + q1);
    const v2u stv = {guard(__float_as_uint(pm)), guard(__float_as_uint(q))};
    __builtin_amdgcn_raw_buffer_store_b64(stv, rws, c8 == 0 ? (8 * w + l8) * 8 : OOB, WS_ST + (tt * 8 + cg) * 512, 16);
    STAMP(8);
    ld_w1(2); ld_w1(3);
    vmwait<6>();
    const u32 sto = c8 * 512 + (8 * w + l8) * 8;
    v2u st;
    asm volatile("buffer_load_dwordx2 %0, %1, %2, %3 offen sc1" : "=v"(st) : "v"(sto), "s"(sws), "s"(WS_ST + tt * 4096) : "memory");
    ld_w1(4);
    asm volatile("s_waitcnt vmcnt(3)" : "+v"(st)::"memory");
    if (__builtin_expect(any_sent(st.x > st.y ? st.x : st.y), 0)) {
#pragma clang loop unroll(disable)
      do {
        asm volatile("buffer_load_dwordx2 %0, %1, %2, %3 offen sc1\n\ts_waitcnt vmcnt(0)" : "+v"(st) : "v"(sto), "s"(sws), "s"(WS_ST + tt * 4096) : "memory");
      } while (any_sent(st.x > st.y ? st.x : st.y) && ++spins < SPIN_CAP);
    }
    STAMP(9);
    ld_w1(5);
    const u32 sx_ = st.x, sy_ = st.y;
    const float mc = __uint_as_float(sx_), mean = sum8(mc) * 0.125f;
    const float var = sum8(fmaf(64.f * (mc - mean), mc - mean, __uint_as_float(sy_))) * (1.f / 512.f);
    const float r1 = __builtin_amdgcn_rsqf(var + 1e-5f);
    const float r2 = __builtin_amdgcn_rsqf(var * r1 * r1 + 1e-5f);
    const float sc = r1 * r2;
    const v4u hn = {cvtpk((hv[0] - mean) * sc, (hv[1] - mean) * sc), cvtpk((hv[2] - mean) * sc, (hv[3] - mean) * sc),
                    cvtpk((hv[4] - mean) * sc, (hv[5] - mean) * sc), cvtpk((hv[6] - mean) * sc, (hv[7] - mean) * sc)};
    lds_w128(L_PC + w * 2048 + l8 * 128 + ((c8 ^ (((l8 >> 1) & 3) << 1)) * 16), hn);
    bar();
    const int a = w >> 2, n = w & 3, row = 4 * g4 + q4;
    const u32 ad = L_PC + (4 * a + (row >> 3)) * 2048 + (row & 7) * 128 + (((2 * n + (p4 >> 1)) ^ (((row >> 1) & 3) << 1)) * 16) + 8 * (p4 & 1);
    const v4u fr = guard4(__builtin_bit_cast(v4u, lds_tr(ad, ad + 2 * 2048)));
    st_sc1(fr, sws, l16 + n * 1024, WS_HN + (cg * 64 + 2 * tt + a) * 4096);
    STAMP(10);
    ld_w1(6); ld_w1(7);
    bar();
  }
  const u32 area = w < 3 ? w * 16384 : 12288 + w * 12288;
  const u32 late = L_PD + (w < 3 ? w * 4096 : w * 8192 - 12288);
  auto take_w2 = [&](u32 slot, bf16x8 (&f)[4]) {
#pragma unroll
    for (int n = 0; n < 4; ++n) f[n] = lds_tr(slot + tra[n], slot + tra[n] + 2048);
  };
  auto issue_w2_early = [&]() {
    const int j = (loc >> 3) & 1;
    issue_w2(w + 8 + 8 * j, area + 4096 + j * 4096);
    issue_w2(w + 16 - 8 * j, area + 8192 - j * 4096);
    if (w < 3) issue_w2(w + 24, area + 12288);
  };
  if (don) {
    f32x4 acc[3][4];
#pragma unroll
    for (int b = 0; b < 3; ++b)
#pragma unroll
      for (int d = 0; d < 4; ++d) acc[b][d] = f32x4{0.f, 0.f, 0.f, 0.f};
    auto so = [&](int s) { return WS_HN + (dt * 64 + 8 * w + ((s + drot) & 7)) * 4096; };
    v4u hf[4][4];
    vmwait<6>();
#pragma unroll
    for (int s = 0; s < 4; ++s) ld4(hf[s], l16, sws, so(s));
#pragma unroll
    for (int s = 0; s < 8; ++s) {
      if (s <= 4) take4<12>(hf[s & 3], l16, sws, so(s), spins);
      else if (s == 5) take4<8>(hf[s & 3], l16, sws, so(s), spins);
      else if (s == 6) take4<4>(hf[s & 3], l16, sws, so(s), spins);
      else take4<0>(hf[s & 3], l16, sws, so(s), spins);
      if (s == 0) {
#pragma unroll
        for (int i = 0; i < 8; ++i) landed3(w1f[i]);
      }
      if (s == 7) issue_w2(w, area);
#pragma unroll
      for (int d = 0; d < 4; ++d)
#pragma unroll
        for (int b = 0; b < 3; ++b) acc[b][d] = mfma16(asfrag(w1f[s][b]), asfrag(hf[s & 3][d]), acc[b][d]);
      pin();
      if (s + 4 < 8) ld4(hf[s & 3], l16, sws, so(s + 4));
      pin();
      CSTAMP(26 + s);
    }
    STAMP(11);
    const u32 slot = L_PD + (w & 3) * PD_SLOT;
    if (w >= 4) {
#pragma unroll
      for (int b = 0; b < 3; ++b)
#pragma unroll
        for (int d = 0; d < 4; ++d) lds_wf4(slot + (b * 4 + d) * 1024 + l16, acc[b][d]);
    }
    bar();
    CSTAMP(34);
    if (w >= 4) issue_w2_early();
    if (w < 4) {
      f32x4 t[12];
#pragma unroll
      for (int i = 0; i < 12; ++i) t[i] = lds_rf4(slot + i * 1024 + l16);
      pin();
#pragma unroll
      for (int i = 0; i < 12; ++i) acc[i >> 2][i & 3] += t[i];
      asm volatile("" ::: "memory");
#pragma unroll
      for (int b = 0; b < 3; ++b)
#pragma unroll
        for (int d = 0; d < 4; ++d) lds_wf4(slot + (16 * d + (lane & 15)) * PD_PITCH + (4 * b + g4) * 16, acc[b][d]);
    }
    bar();
    CSTAMP(35);
    u32 go = lane == 0 ? WS_PAD + (wg * 8 + w) * 128 : OOB;
    v4u gv = sent4;
    if (w < 6) {
      const int hf2 = 2 * w + h, b = hf2 >> 2, d = hf2 & 3;
      const u32 ad = L_PD + (16 * d + (lane & 15)) * PD_PITCH + (4 * b + 2 * blk) * 16;
      f32x4 t[8];
#pragma unroll
      for (int k = 0; k < 4; ++k) { t[2 * k] = lds_rf4(ad + k * PD_SLOT); t[2 * k + 1] = lds_rf4(ad + k * PD_SLOT + 16); }
      pin();
      f32x4 s0 = {bflo(b1v.x), bfhi(b1v.x), bflo(b1v.y), bfhi(b1v.y)}, s1 = {bflo(b1v.z), bfhi(b1v.z), bflo(b1v.w), bfhi(b1v.w)};
      s0 += (t[0] + t[2]) + (t[4] + t[6]); s1 += (t[1] + t[3]) + (t[5] + t[7]);
      gv = guard4(v4u{cvtpk(gelu_tanh(s0.x), gelu_tanh(s0.y)), cvtpk(gelu_tanh(s0.z), gelu_tanh(s0.w)),
                      cvtpk(gelu_tanh(s1.x), gelu_tanh(s1.y)), cvtpk(gelu_tanh(s1.z), gelu_tanh(s1.w))});
      go = b < 2 ? g_ab + d * 1024 + ((2 * b + blk) * 16 + (lane & 15)) * 16
                 : (fg < 28 ? g_c + d * 1024 + (blk * 16 + (lane & 15)) * 16 : go);
    }
    st_sc1(gv, sws, go, 0);
    STAMP(12);
    bar();
    if (w < 4) issue_w2_early();
  } else {
    vmwait<0>();
    issue_w2(w, area);
    issue_w2_early();
    __builtin_amdgcn_s_sleep(100);
    STAMP(12);
  }
  {
    if (w < 3) issue_w2(w + 32, late);
    else { issue_w2(w + 24, late); issue_w2(w + 32, late + 4096); }
    f32x4 acc[4][4];
#pragma unroll
    for (int d = 0; d < 4; ++d)
#pragma unroll
      for (int n = 0; n < 4; ++n) acc[d][n] = f32x4{0.f, 0.f, 0.f, 0.f};
    const u32 so = WS_G + (cg * 43 + w) * 4096;
    v4u gf[4][4];
    bf16x8 wc[4];
    if (w >= 4) vmwait<8>();
    vmwait<16>();
    CSTAMP(36);
#pragma unroll
    for (int j = 0; j < 4; ++j) ld4(gf[j], l16, sws, so + j * 32768);
    take_w2(area, wc);
    CSTAMP(37);
#pragma unroll
    for (int j = 0; j < 6; ++j) {
      if (j < 5 || w < 3) {
        if (w >= 3) { if (j == 2) vmwait<8>(); else if (j == 3) vmwait<4>(); else if (j == 4) vmwait<0>(); }
        if (j <= 1) take4<12>(gf[j & 3], l16, sws, so + j * 32768, spins);
        else if (j == 2) take4<16>(gf[j & 3], l16, sws, so + j * 32768, spins);
        else if (j == 3) take4<12>(gf[j & 3], l16, sws, so + j * 32768, spins);
        else if (j == 4) { if (w < 3) vmwait<4>(); take4<8>(gf[j & 3], l16, sws, so + j * 32768, spins); }
        else take4<0>(gf[j & 3], l16, sws, so + j * 32768, spins);
        bf16x8 wn[4];
        if (j < 4 || (j == 4 && w < 3))
          take_w2(j < 2 ? area + 4096 + j * 4096 : j == 2 ? (w < 3 ? area + 12288 : late) : j == 3 ? (w < 3 ? late : late + 4096) : area, wn);
#pragma unroll
        for (int d = 0; d < 4; ++d)
#pragma unroll
          for (int n = 0; n < 4; ++n) acc[d][n] = mfma16(asfrag(gf[j & 3][d]), wc[n], acc[d][n]);
        pin();
        if (j == 0) ld4(gf[0], l16, sws, so + 4 * 32768);
        if (j == 1 && w < 3) { issue_w2(w + 40, area); ld4(gf[1], l16, sws, so + 5 * 32768); }
#pragma unroll
        for (int n = 0; n < 4; ++n) wc[n] = wn[n];
      }
      pin();
      CSTAMP(52 + j);
    }
    STAMP(13);
    asm volatile("s_waitcnt lgkmcnt(0)" ::: "memory");
#pragma unroll
    for (int d = 0; d < 4; ++d)
#pragma unroll
      for (int n = 0; n < 4; ++n) {
        const u32 base = n < 3 ? area + n * 4096 : late;
        lds_wf4(base + (lane & 15) * 256 + (((4 * d + g4) ^ (lane & 15)) * 16), acc[d][n]);
      }
    bar();
    CSTAMP(58);
    const int tok = 8 * w + l8;
    f32x4 t[16];
    const u32 rb = (tok & 15) * 256 + (((2 * c8) ^ (tok & 15)) * 16);
#pragma unroll
    for (int k = 0; k < 8; ++k) {
      const u32 ak = k < 3 ? k * 16384 : 12288 + k * 12288, lk = L_PD + (k < 3 ? k * 4096 : k * 8192 - 12288);
      const u32 ko2 = w < 6 ? ak + (w >> 1) * 4096 : lk;
      t[2 * k] = lds_rf4(rb + ko2);
      t[2 * k + 1] = lds_rf4((rb ^ 16) + ko2);
    }
    pin();
    const float bb = bflo(b2v);
    f32x4 s0 = {bflo(hres.x) + bb, bfhi(hres.x) + bb, bflo(hres.y) + bb, bfhi(hres.y) + bb};
    f32x4 s1 = {bflo(hres.z) + bb, bfhi(hres.z) + bb, bflo(hres.w) + bb, bfhi(hres.w) + bb};
    s0 += ((t[0] + t[2]) + (t[4] + t[6])) + ((t[8] + t[10]) + (t[12] + t[14]));
    s1 += ((t[1] + t[3]) + (t[5] + t[7])) + ((t[9] + t[11]) + (t[13] + t[15]));
    const v4u ov = {cvtpk(s0.x, s0.y), cvtpk(s0.z, s0.w), cvtpk(s1.x, s1.y), cvtpk(s1.z, s1.w)};
    asm volatile(".Lmega_pf_end:");
    asm volatile("buffer_store_dwordx4 %0, %1, %2, 0 offen sc0 sc1 nt\n\ts_nop 1" ::"v"(ov), "v"((64 * tt + tok) * 1024 + cg * 128 + c8 * 16), "s"(mksrd(gout, 2048 * 512 * 2)) : "memory");
  }
  STAMP(14);
}

extern "C" void kernel_launch(void* const* d_in, const int* in_sizes, int n_in, void* d_out, int out_size, void* d_ws, size_t ws_size,
                              hipStream_t stream) {
  (void)hipFuncSetAttribute((const void*)mega, hipFuncAttributeMaxDynamicSharedMemorySize, LDS_BYTES);
  mega<<<256, 512, LDS_BYTES, stream>>>((const u16*)d_in[0], (const u16*)d_in[1], (const u16*)d_in[2], (const u16*)d_in[4], (const u16*)d_in[5],
                                        (const u16*)d_in[6], (const u16*)d_in[7], (u16*)d_out, (char*)d_ws);
}
```

```cpp
#include <hip/hip_runtime.h>
#include <stdint.h>

typedef unsigned u32;
typedef unsigned short u16;
typedef u32 v4u __attribute__((ext_vector_type(4)));
typedef u32 v2u __attribute__((ext_vector_type(2)));
typedef int v4i __attribute__((ext_vector_type(4)));
typedef float f32x2 __attribute__((ext_vector_type(2)));
typedef float f32x4 __attribute__((ext_vector_type(4)));
typedef float f32x16 __attribute__((ext_vector_type(16)));
typedef short bf16x8 __attribute__((ext_vector_type(8)));
typedef short s16x4 __attribute__((ext_vector_type(4)));
typedef __bf16 bf2 __attribute__((ext_vector_type(2)));
#define LAS __attribute__((address_space(3)))
#define DI __device__ __forceinline__

#define CODE_PF 1
template <bool B> struct BoolC { static constexpr bool value = B; };
#define STAMPS 0

constexpr u32 WS_EPOCH = 0, WS_FLAGA = 1u << 19, WS_FLAGB = WS_FLAGA + 32768, WS_FLAGW = WS_FLAGB + 32768, WS_DBG = 65536;
constexpr u32 WS_KV = 1u << 20;
constexpr u32 WS_O = WS_KV + (4u << 20);
constexpr u32 WS_HN = WS_O + (2u << 20);
constexpr u32 WS_G = WS_HN + (2u << 20);
constexpr u32 WS_ST = WS_G + (2u << 20);
constexpr u32 WS_PAD = WS_ST + (1u << 17);
constexpr u32 WS_W1T = WS_PAD + (1u << 18);
constexpr u32 WS_END = WS_W1T + 29 * 64 * 3072;
constexpr u32 SENT = 0xFFFFFFFFu;
constexpr u32 OOB = 0x7FFF0000u;
constexpr int SPIN_CAP = 1 << 16;

constexpr u32 L_XN = 0;
constexpr u32 KVT = 8448;
constexpr u32 L_CNT = 84480;
constexpr u32 L_RING = 86016;
constexpr u32 L_JUNK = L_RING + 65536;
constexpr u32 L_WO = 98304;
constexpr u32 L_PC = 0;
constexpr u32 PD_PITCH = 208, PD_SLOT = 64 * PD_PITCH;
constexpr u32 L_PD = 110592;
constexpr u32 LDS_BYTES = 163840;

DI u32 cvtpk(float a, float b) { f32x2 v = {a, b}; bf2 r = __builtin_convertvector(v, bf2); return __builtin_bit_cast(u32, r); }
DI float bflo(u32 x) { return __uint_as_float(x << 16); }
DI float bfhi(u32 x) { return __uint_as_float(x & 0xffff0000u); }
DI u32 guard(u32 x) { return x < 0xFFFFFFFEu ? x : 0xFFFFFFFEu; }
DI v4u guard4(v4u v) { v4u r = {guard(v.x), guard(v.y), guard(v.z), guard(v.w)}; return r; }
DI u32 umax3(u32 a, u32 b, u32 c) { u32 m = a > b ? a : b; return m > c ? m : c; }
DI u32 smax4(u32 m, v4u v) { return umax3(umax3(m, v.x, v.y), v.z, v.w); }
DI bool any_sent(u32 m) { return __builtin_amdgcn_ballot_w64(m == SENT) != 0; }
DI bf16x8 asfrag(v4u v) { return __builtin_bit_cast(bf16x8, v); }
DI f32x16 mfma32(bf16x8 a, bf16x8 b, f32x16 c) { return __builtin_amdgcn_mfma_f32_32x32x16_bf16(a, b, c, 0, 0, 0); }
DI f32x4 mfma16(bf16x8 a, bf16x8 b, f32x4 c) { return __builtin_amdgcn_mfma_f32_16x16x32_bf16(a, b, c, 0, 0, 0); }
DI v4u lds_r128(u32 a) { return *(const LAS v4u*)(uintptr_t)a; }
DI f32x4 lds_rf4(u32 a) { return *(const LAS f32x4*)(uintptr_t)a; }
DI void lds_w128(u32 a, v4u v) { *(LAS v4u*)(uintptr_t)a = v; }
DI void lds_wf4(u32 a, f32x4 v) { *(LAS f32x4*)(uintptr_t)a = v; }
DI bf16x8 lds_tr(u32 a0, u32 a1) {
  s16x4 lo = __builtin_amdgcn_ds_read_tr16_b64_v4i16((LAS s16x4*)(uintptr_t)a0);
  s16x4 hi = __builtin_amdgcn_ds_read_tr16_b64_v4i16((LAS s16x4*)(uintptr_t)a1);
  return __builtin_shufflevector(lo, hi, 0, 1, 2, 3, 4, 5, 6, 7);
}
template <int CTRL> DI float dpp(float v) { return __int_as_float(__builtin_amdgcn_update_dpp(0, __float_as_int(v), CTRL, 0xf, 0xf, true)); }
DI float sum8(float v) { v += dpp<0xB1>(v); v += dpp<0x4E>(v); v += dpp<0x141>(v); return v; }
DI v4i mksrd(const void* p, u32 bytes) { v4i s = {(int)(u32)(uintptr_t)p, (int)(u32)((uintptr_t)p >> 32), (int)bytes, 0x00020000}; return s; }
DI __amdgpu_buffer_rsrc_t mkrs(const void* p, u32 bytes) { return __builtin_amdgcn_make_buffer_rsrc((void*)p, 0, bytes, 0x00020000); }
DI void dma16(u32 lds, u32 voff, v4i srd, u32 soff) {
  asm volatile("s_mov_b32 m0, %0\n\ts_nop 0\n\tbuffer_load_dwordx4 %1, %2, %3 offen lds" ::"s"(lds), "v"(voff), "s"(srd), "s"(soff) : "memory");
}
DI v4u aload_sc1(u32 voff, v4i srd, u32 soff) {
  v4u r;
  asm volatile("buffer_load_dwordx4 %0, %1, %2, %3 offen sc1" : "=v"(r) : "v"(voff), "s"(srd), "s"(soff) : "memory");
  return r;
}
DI v4u aload(u32 voff, v4i srd, u32 soff) {
  v4u r;
  asm volatile("buffer_load_dwordx4 %0, %1, %2, %3 offen" : "=v"(r) : "v"(voff), "s"(srd), "s"(soff) : "memory");
  return r;
}
DI void ld4(v4u (&f)[4], u32 voff, v4i srd, u32 soff) {
  asm volatile("buffer_load_dwordx4 %0, %4, %5, %6 offen sc1\n\tbuffer_load_dwordx4 %1, %4, %5, %6 offen offset:1024 sc1\n\t"
               "buffer_load_dwordx4 %2, %4, %5, %6 offen offset:2048 sc1\n\tbuffer_load_dwordx4 %3, %4, %5, %6 offen offset:3072 sc1"
               : "=&v"(f[0]), "=&v"(f[1]), "=&v"(f[2]), "=&v"(f[3]) : "v"(voff), "s"(srd), "s"(soff) : "memory");
}
template <int N> DI void take4(v4u (&f)[4], u32 voff, v4i srd, u32 soff, int& spins) {
  asm volatile("s_waitcnt vmcnt(%4)" : "+v"(f[0]), "+v"(f[1]), "+v"(f[2]), "+v"(f[3]) : "n"(N) : "memory");
  if (__builtin_expect(any_sent(smax4(smax4(smax4(smax4(0, f[0]), f[1]), f[2]), f[3])), 0)) {
#pragma clang loop unroll(disable)
    do {
      asm volatile("s_sleep 1\n\tbuffer_load_dwordx4 %0, %4, %5, %6 offen sc1\n\tbuffer_load_dwordx4 %1, %4, %5, %6 offen offset:1024 sc1\n\t"
                   "buffer_load_dwordx4 %2, %4, %5, %6 offen offset:2048 sc1\n\tbuffer_load_dwordx4 %3, %4, %5, %6 offen offset:3072 sc1\n\t"
                   "s_waitcnt vmcnt(0)"
                   : "+v"(f[0]), "+v"(f[1]), "+v"(f[2]), "+v"(f[3]) : "v"(voff), "s"(srd), "s"(soff) : "memory");
    } while (any_sent(smax4(smax4(smax4(smax4(0, f[0]), f[1]), f[2]), f[3])) && ++spins < SPIN_CAP);
  }
}
template <int N> DI void vmwait() { asm volatile("s_waitcnt vmcnt(%0)" ::"n"(N) : "memory"); }
DI void bar() { asm volatile("s_waitcnt lgkmcnt(0)\n\ts_barrier" ::: "memory"); }
DI v4u ld_sc1(__amdgpu_buffer_rsrc_t rs, u32 voff, u32 soff) { return __builtin_amdgcn_raw_buffer_load_b128(rs, voff, soff, 16); }
DI void st_sc1(v4u v, v4i srd, u32 voff, u32 soff) {
  asm volatile("buffer_store_dwordx4 %0, %1, %2, %3 offen sc1\n\ts_nop 1" ::"v"(v), "v"(voff), "s"(srd), "s"(soff) : "memory");
}
DI void pin() { __builtin_amdgcn_sched_barrier(0); }

#define STAMP(i) do {} while (0)
#define CSTAMP(i) do {} while (0)

DI void max16(float (&m)[4], const f32x16& s) {
#pragma unroll
  for (int j = 0; j < 8; ++j) m[j & 3] = fmaxf(fmaxf(m[j & 3], s[2 * j]), s[2 * j + 1]);
}
DI v4u pack8(const float* p) { return v4u{cvtpk(p[0], p[1]), cvtpk(p[2], p[3]), cvtpk(p[4], p[5]), cvtpk(p[6], p[7])}; }
DI float gelu_tanh(float x) {
  const float c1 = -2.0f * 0.7978845608028654f * 1.4426950408889634f, c2 = c1 * 0.044715f;
  const float e = __builtin_amdgcn_exp2f(x * (c1 + c2 * x * x));
  return x * __builtin_amdgcn_rcpf(1.0f + e);
}
struct Frag4 { bf16x8 x[4], w[4]; };
template <bool XFIRST>
DI void gemm4(f32x16& acc, const Frag4& c, Frag4& n, const u32 (&xa)[4], u32 xo, u32 wo) {
#pragma unroll
  for (int s = 0; s < 4; ++s) {
    acc = XFIRST ? mfma32(c.x[s], c.w[s], acc) : mfma32(c.w[s], c.x[s], acc);
    n.x[s] = asfrag(lds_r128(xa[s] + xo));
    n.w[s] = lds_tr(wo + s * 2048, wo + s * 2048 + 512);
    pin();
  }
}
DI void read4(Frag4& n, const u32 (&xa)[4], u32 xo, u32 wo) {
#pragma unroll
  for (int s = 0; s < 4; ++s) { n.x[s] = asfrag(lds_r128(xa[s] + xo)); n.w[s] = lds_tr(wo + s * 2048, wo + s * 2048 + 512); }
}
DI void ld3(v4u (&f)[3], u32 voff, v4i srd, u32 soff) {
  asm volatile("buffer_load_dwordx4 %0, %3, %4, %5 offen sc1\n\tbuffer_load_dwordx4 %1, %3, %4, %5 offen offset:1024 sc1\n\t"
               "buffer_load_dwordx4 %2, %3, %4, %5 offen offset:2048 sc1"
               : "=&v"(f[0]), "=&v"(f[1]), "=&v"(f[2]) : "v"(voff), "s"(srd), "s"(soff) : "memory");
}
DI void landed3(v4u (&f)[3]) { asm volatile("" : "+v"(f[0]), "+v"(f[1]), "+v"(f[2])); }
DI void signal(u32 cnt, int lane, __amdgpu_buffer_rsrc_t rws, u32 flag, u32 tag) {
  u32 old = 0;
  if (lane == 0) old = __hip_atomic_fetch_add((LAS u32*)(uintptr_t)cnt, 1u, __ATOMIC_RELAXED, __HIP_MEMORY_SCOPE_WORKGROUP);
  if (__builtin_amdgcn_readfirstlane(old) == 7 && lane == 0) __builtin_amdgcn_raw_buffer_store_b32(tag, rws, flag, 0, 16);
}
DI int wg_of(int tt, int cg) { return ((tt & 7) + 8 * (cg & 3)) * 8 + 2 * (tt >> 3) + (cg >> 2); }
DI u32 ld_flag(u32 off, v4i sws) {
  u32 f;
  asm volatile("buffer_load_dword %0, %1, %2, 0 offen sc1" : "=v"(f) : "v"(off), "s"(sws) : "memory");
  return f;
}
DI void wait_flag(u32& f, u32 off, v4i sws, u32 tag, int& spins) {
  if (__builtin_expect(__builtin_amdgcn_ballot_w64(off != OOB && f != tag) != 0, 0)) {
#pragma clang loop unroll(disable)
    do {
      asm volatile("s_sleep 2\n\tbuffer_load_dword %0, %1, %2, 0 offen sc1\n\ts_waitcnt vmcnt(0)" : "+v"(f) : "v"(off), "s"(sws) : "memory");
    } while (__builtin_amdgcn_ballot_w64(off != OOB && f != tag) != 0 && ++spins < SPIN_CAP);
  }
}

__global__ void __launch_bounds__(512,2) k_pre(unsigned* p){ if(threadIdx.x==0) { unsigned t=(unsigned)__builtin_amdgcn_s_memrealtime(); __hip_atomic_store(p + 64 + blockIdx.x, t, __ATOMIC_RELAXED, __HIP_MEMORY_SCOPE_AGENT);} }
__global__ void __launch_bounds__(512,2) k_post(unsigned* p, int amp){ const unsigned t=(unsigned)__builtin_amdgcn_s_memrealtime();
  if (blockIdx.x == 0 && threadIdx.x < 64) { unsigned m = 0xffffffffu; for (int i = threadIdx.x; i < 256; i += 64) { unsigned d = t - p[64 + i]; m = d < m ? d : m; }
    for (int o = 32; o; o >>= 1) { unsigned x = __shfl_xor(m, o); m = x < m ? x : m; }
    const unsigned d = amp * m, t0 = (unsigned)__builtin_amdgcn_s_memrealtime(); while ((unsigned)__builtin_amdgcn_s_memrealtime() - t0 < d && d < 100000) __builtin_amdgcn_s_sleep(1); } }
__global__ void __launch_bounds__(512, 2)
mega(const u16* __restrict__ gx, const u16* __restrict__ gwqkv, const u16* __restrict__ gwo, const u16* __restrict__ gw1,
     const u16* __restrict__ gb1, const u16* __restrict__ gw2, const u16* __restrict__ gb2, u16* __restrict__ gout, char* ws) {
  const int tid = threadIdx.x, lane = tid & 63;
  const int w = __builtin_amdgcn_readfirstlane(tid >> 6);
  const int wg = blockIdx.x, xcd = wg & 7, loc = wg >> 3;
  const int tt = 8 * (xcd >> 1) + (loc & 7), cg = 4 * (xcd & 1) + (loc >> 3);
  const int dt = 4 * (xcd & 1) + (loc & 3), fq = xcd >> 1, fi = loc >> 2;
  const int fg = fq == 0 ? fi : 1 + 7 * fq + fi;
  const bool don = fq == 0 || fi < 7;
  const int rot = ((loc & 7) + 2 * (loc >> 3)) & 7, drot = (fi + 2 * (loc & 3)) & 7;
  STAMP(0);
  const u32 l16 = lane * 16;
  const int l8 = lane >> 3, c8 = lane & 7;
  const v4i sx = mksrd(gx, 2048 * 512 * 2), swqkv = mksrd(gwqkv, 512 * 1536 * 2);
  const __amdgpu_buffer_rsrc_t rws = mkrs(ws, WS_END);
  const v4i sws = mksrd(ws, WS_END);
  const v4u sent4 = {SENT, SENT, SENT, SENT};
  int spins = 0;

  v4u xr[8];
  {
    const u32 vo = (64 * tt + 8 * w + l8) * 1024 + c8 * 16;
#pragma unroll
    for (int i = 0; i < 4; ++i) xr[i] = aload(vo, sx, ((i + rot) & 7) * 128);
    {
      unsigned long long pc;
      int len;
      asm volatile("s_getpc_b64 %0\n.Lmega_pf_start:\n\ts_mov_b32 %1, .Lmega_pf_end-.Lmega_pf_start" : "=s"(pc), "=s"(len));
      dma16(L_JUNK + (w & 1) * 1024, w < 2 ? l16 + (loc * 2 + w) * 1024 : OOB, mksrd((const void*)(pc & ~63ull), len > 0 ? len : 0), 0);
    }
#pragma unroll
    for (int i = 4; i < 8; ++i) xr[i] = aload(vo, sx, ((i + rot) & 7) * 128);
  }
  const int grp = loc & 3;
  auto clear_kv = [&](int g) {
    const u32 vo = grp == g ? tid * 16 : OOB;
    st_sc1(sent4, sws, vo, WS_KV + (tt * 8 + cg) * 16384);
    st_sc1(sent4, sws, vo, WS_KV + (tt * 8 + cg) * 16384 + 8192);
  };
  u32 epoch;
  asm volatile("s_load_dword %0, %1, 0x0" : "=s"(epoch) : "s"(ws) : "memory");
  const u32 wvq = (8 * w + l8) * 3072 + ((c8 ^ (((l8 >> 1) & 1) << 2)) * 16) + cg * 128, wvk = wvq + 1024, wvv = wvq + 2048;
  u32 ko[8];
#pragma unroll
  for (int i = 0; i < 8; ++i) ko[i] = ((i + rot) & 7) * (64 * 3072);
  auto issue_w = [&](int st) {
    const u32 slot = L_RING + (st & 3) * 16384 + w * 1024;
    dma16(slot, st < 8 ? wvk : wvq, swqkv, st < 8 ? ko[st & 7] : ko[(2 * st) & 7]);
    dma16(slot + 8192, st < 8 ? wvv : wvq, swqkv, st < 8 ? ko[st & 7] : ko[(2 * st + 1) & 7]);
  };
  const int r32 = lane & 31, h = lane >> 5, blk = (lane >> 4) & 1, q4 = (lane & 15) >> 2, p4 = lane & 3;
  const int hh = w & 3, tb = w >> 2;
  const int isV = w & 1, hp = (w >> 1) & 1;
  u32 xa[4];
#pragma unroll
  for (int s = 0; s < 4; ++s) xa[s] = L_XN + (32 * tb + r32) * 128 + (((2 * s + h) ^ ((r32 >> 1) & 7)) * 16);
  u32 wa = L_RING + isV * 8192 + (8 * h + q4) * 128 + (((4 * hp + 2 * blk + (p4 >> 1)) ^ (((q4 >> 1) & 1) << 2)) * 16) + 8 * (p4 & 1);
  asm volatile("" : "+v"(xa[0]), "+v"(xa[1]), "+v"(xa[2]), "+v"(xa[3]), "+v"(wa));
  constexpr int PF = CODE_PF ? 1 : 0;
  {
    f32x2 xv[32];
    f32x2 s1a = {0.f, 0.f}, s1b = s1a, s2a = s1a, s2b = s1a, shift;
    auto acc4 = [&](int i) {
      const u32 d[4] = {xr[i].x, xr[i].y, xr[i].z, xr[i].w};
#pragma unroll
      for (int j = 0; j < 4; ++j) {
        const f32x2 a = f32x2{bflo(d[j]), bfhi(d[j])} - shift;
        xv[4 * i + j] = a;
        if (j & 1) { s1b += a; s2b = __builtin_elementwise_fma(a, a, s2b); } else { s1a += a; s2a = __builtin_elementwise_fma(a, a, s2a); }
      }
    };
    CSTAMP(40);
    asm volatile("s_waitcnt vmcnt(%2)" : "+v"(xr[0]), "+v"(xr[1]) : "n"(6 + PF) : "memory");
    CSTAMP(41);
    {
      const float sh0 = dpp<0x00>(bflo(xr[0].x)), sh1 = dpp<0x141>(sh0), sh = (lane & 4) ? sh1 : sh0;
      shift = f32x2{sh, sh};
    }
    acc4(0); acc4(1); pin();
    issue_w(0);
    asm volatile("s_waitcnt vmcnt(%2)" : "+v"(xr[2]), "+v"(xr[3]) : "n"(6 + PF) : "memory");
    CSTAMP(43);
    acc4(2); acc4(3); pin();
    issue_w(1); clear_kv(0);
    asm volatile("s_waitcnt vmcnt(8)" : "+v"(xr[4]), "+v"(xr[5])::"memory");
    CSTAMP(45);
    acc4(4); acc4(5); pin();
    issue_w(2);
    asm volatile("s_waitcnt vmcnt(8)" : "+v"(xr[6]), "+v"(xr[7])::"memory");
    CSTAMP(47);
    acc4(6); acc4(7);
    s1a += s1b; s2a += s2b;
    const float s1 = sum8(s1a.x + s1a.y) * (1.f / 512.f), s2 = sum8(s2a.x + s2a.y) * (1.f / 512.f);
    const float rs = __builtin_amdgcn_rsqf(fmaxf(s2 - s1 * s1, 0.f) + 1e-5f), nb = -s1 * rs;
    const f32x2 rs2 = {rs, rs}, nb2 = {nb, nb};
    const int row = 8 * w + l8;
    const u32 xw = L_XN + row * 128 + ((c8 ^ ((row >> 1) & 7)) * 16);
#pragma unroll
    for (int i = 0; i < 8; ++i) {
      u32 o[4];
#pragma unroll
      for (int j = 0; j < 4; ++j) { const f32x2 y = __builtin_elementwise_fma(xv[4 * i + j], rs2, nb2); o[j] = cvtpk(y.x, y.y); }
      lds_w128(xw + i * 8192, v4u{o[0], o[1], o[2], o[3]});
    }
    if (tid < 3) *(LAS u32*)(uintptr_t)(L_CNT + tid * 4) = 0;
    pin();
    issue_w(3); clear_kv(1);
    CSTAMP(48);
  }
  asm volatile("s_waitcnt lgkmcnt(0)" : "+s"(epoch)::"memory");
  const u32 tag = epoch + 1;
  STAMP(1);

  Frag4 fa, fb;
  const u32 qwa = L_RING + (8 * h + q4) * 128 + (((2 * hh + (p4 >> 1)) ^ (((q4 >> 1) & 1) << 2)) * 16) + 8 * (p4 & 1);
  const int rks = wg & 63, rfg = 8 * (wg >> 6) + w;
  {
    f32x16 acc;
#pragma unroll
    for (int i = 0; i < 16; ++i) acc[i] = 0.f;
    vmwait<10>(); bar();
    CSTAMP(51);
    auto loop = [&](auto xfirst) {
      constexpr bool XF = decltype(xfirst)::value;
      read4(fa, xa, 0, wa);
      vmwait<8>(); bar(); issue_w(4); clear_kv(2);
      gemm4<XF>(acc, fa, fb, xa, 1 * 8192, wa + 1 * 16384);
      vmwait<8>(); bar(); issue_w(5); clear_kv(3);
      gemm4<XF>(acc, fb, fa, xa, 2 * 8192, wa + 2 * 16384);
      CSTAMP(0);
      vmwait<10>(); bar(); issue_w(6);
      gemm4<XF>(acc, fa, fb, xa, 3 * 8192, wa + 3 * 16384);
      vmwait<8>(); bar(); issue_w(7);
      gemm4<XF>(acc, fb, fa, xa, 4 * 8192, wa);
      CSTAMP(2);
      vmwait<6>(); bar(); issue_w(8);
      gemm4<XF>(acc, fa, fb, xa, 5 * 8192, wa + 1 * 16384);
      vmwait<4>(); bar(); issue_w(9);
      if (tid == 0) __builtin_amdgcn_raw_buffer_store_b32(tag, rws, WS_FLAGA + wg * 128, 0, 16);
      gemm4<XF>(acc, fb, fa, xa, 6 * 8192, wa + 2 * 16384);
      CSTAMP(4);
      vmwait<4>(); bar();
      gemm4<XF>(acc, fa, fb, xa, 7 * 8192, wa + 3 * 16384);
      vmwait<2>(); bar();
      gemm4<XF>(acc, fb, fa, xa, 0, qwa);
      CSTAMP(6);
      const v4u a = guard4(v4u{cvtpk(acc[0], acc[1]), cvtpk(acc[2], acc[3]), cvtpk(acc[4], acc[5]), cvtpk(acc[6], acc[7])});
      const v4u b = guard4(v4u{cvtpk(acc[8], acc[9]), cvtpk(acc[10], acc[11]), cvtpk(acc[12], acc[13]), cvtpk(acc[14], acc[15])});
      const u32 oa = decltype(xfirst)::value ? (2 * hp + (r32 >> 4)) * 2048 + 1024 + h * 256 + (r32 & 15) * 16 : 2 * hp * 2048 + l16;
      const u32 ob = oa + (decltype(xfirst)::value ? 512 : 2048);
      const u32 gso = WS_KV + (tt * 8 + cg) * 16384 + tb * 8192;
      st_sc1(a, sws, oa, gso);
      st_sc1(b, sws, ob, gso);
      issue_w(10); issue_w(11);
      lds_w128((8 + tb) * KVT + oa, a);
      lds_w128((8 + tb) * KVT + ob, b);
    };
    if (isV) loop(BoolC<true>{}); else loop(BoolC<false>{});
    STAMP(2);
  }
  bf16x8 qf;
  v4u halo[2][4];
  u32 hso[2], hvo[2];
  {
    const int ptt = tt - 4 + (lane & 3);
    const u32 fao = (ptt >= 0 && lane < 4) ? WS_FLAGA + wg_of(ptt, cg) * 128 : OOB;
    u32 fl = ld_flag(fao, sws);
    f32x16 acc;
#pragma unroll
    for (int i = 0; i < 16; ++i) acc[i] = 0.f;
    gemm4<false>(acc, fa, fb, xa, 1 * 8192, qwa + 1 * 8192);
    vmwait<7>(); bar();
    gemm4<false>(acc, fb, fa, xa, 2 * 8192, qwa + 2 * 8192);
    gemm4<false>(acc, fa, fb, xa, 3 * 8192, qwa + 3 * 8192);
    CSTAMP(7);
    vmwait<3>(); bar();
    CSTAMP(8);
    {
      asm volatile("s_waitcnt vmcnt(0)" : "+v"(fl)::"memory");
      wait_flag(fl, fao, sws, tag, spins);
#pragma unroll
      for (int i = 0; i < 2; ++i) {
        const int pt = tt - 4 + 2 * (w >> 2) + i;
        hso[i] = pt >= 0 ? WS_KV + (pt * 8 + cg) * 16384 + (w & 3) * 4096 : 0;
        hvo[i] = pt >= 0 ? l16 : OOB;
        ld4(halo[i], hvo[i], sws, hso[i]);
      }
    }
    CSTAMP(9);
    gemm4<false>(acc, fb, fa, xa, 4 * 8192, qwa + 4 * 8192);
    gemm4<false>(acc, fa, fb, xa, 5 * 8192, qwa + 5 * 8192);
    bar();
    gemm4<false>(acc, fb, fa, xa, 6 * 8192, qwa + 6 * 8192);
    gemm4<false>(acc, fa, fb, xa, 7 * 8192, qwa + 7 * 8192);
#pragma unroll
    for (int s = 0; s < 4; ++s) acc = mfma32(fb.w[s], fb.x[s], acc);
    const float qs = 0.25f * 1.4426950408889634f;
    qf = asfrag(v4u{cvtpk(acc[0] * qs, acc[1] * qs), cvtpk(acc[2] * qs, acc[3] * qs), cvtpk(acc[4] * qs, acc[5] * qs), cvtpk(acc[6] * qs, acc[7] * qs)});
  }
  bar();
  STAMP(3);
  const int g4 = lane >> 4;
  {
    if (tid < 160) { const v4u one4 = {0x3F803F80u, 0x3F803F80u, 0x3F803F80u, 0x3F803F80u}; lds_w128((tid >> 4) * KVT + 8192 + (tid & 15) * 16, one4); }
    CSTAMP(10);
    take4<4>(halo[0], hvo[0], sws, hso[0], spins);
    CSTAMP(11);
#pragma unroll
    for (int u = 0; u < 4; ++u) lds_w128((4 * (w >> 2) + ((w & 3) >> 1)) * KVT + (4 * (w & 1) + u) * 1024 + l16, halo[0][u]);
    take4<0>(halo[1], hvo[1], sws, hso[1], spins);
    CSTAMP(12);
#pragma unroll
    for (int u = 0; u < 4; ++u) lds_w128((4 * (w >> 2) + 2 + ((w & 3) >> 1)) * KVT + (4 * (w & 1) + u) * 1024 + l16, halo[1][u]);
  }
  const u32 g_ab = WS_G + (dt * 43 + fg) * 4096, g_c = WS_G + (dt * 43 + 29 + (fg >> 1)) * 4096 + (fg & 1) * 512;
  const v4i sw2 = mksrd(gw2, 1376 * 2048 * 2);
  const u32 w2vo = (8 * (l8 >> 2) + (l8 & 3)) * 4096 + ((c8 ^ (((l8 >> 1) & 3) << 1)) * 16);
  auto dma_w2 = [&](int step, u32 slot, int u) {
    const u32 r0 = step < 29 ? step * 48 : (step - 29) * 96 + 32, r1 = r0 + (step < 29 ? 16 : 48);
    dma16(slot + u * 1024, w2vo, sw2, (((u & 1) ? r1 : r0) + 4 * (u >> 1)) * 4096 + tt * 128);
  };
  auto issue_w2 = [&](int step, u32 slot) {
#pragma unroll
    for (int u = 0; u < 4; ++u) dma_w2(step, slot, u);
  };
  v4u w1f[8][3];
  const u32 w1vo = don ? l16 : OOB;
  auto ld_w1 = [&](int s) { ld3(w1f[s], w1vo, sws, WS_W1T + ((don ? fg : 0) * 64 + 8 * w + ((s + drot) & 7)) * 3072); };

  {
    const int jmin = tt >= 4 ? 0 : 8 - 2 * tt;
    const u32 kb = tb * KVT + hh * 2048 + l16;
    const u32 ones = tb * KVT + 8192 + ((r32 >= 20 && r32 < 28) ? 64 : 0);
    u32 vb0 = r32 < 16 ? tb * KVT + hh * 2048 + 1024 + h * 256 + r32 * 16 : ones;
    u32 vb1 = r32 < 16 ? vb0 + 512 : ones;
    u32 gclr = !don ? OOB : tid < 256 ? g_ab + tid * 16 : (tid < 384 && fg < 28) ? g_c + ((tid - 256) >> 5) * 1024 + (tid & 31) * 16 : OOB;
    u32 sclr = tid < 32 ? tid * 16 : OOB;
    asm volatile("" : "+v"(vb0), "+v"(vb1), "+v"(gclr), "+v"(sclr));
    f32x16 z16;
#pragma unroll
    for (int i = 0; i < 16; ++i) z16[i] = 0.f;
    const int ca = w >> 2, ckq = w & 3;
    const u32 wos = L_WO + (4 * ckq + 2 * ca) * 4096;
    v4u raw[3];
    {
      const v4i sw1 = mksrd(gw1, 2048 * 1376 * 2);
#pragma unroll
      for (int u = 0; u < 3; ++u) {
        const int q = 64 * u + lane, row = q / 6;
        int c = q - 6 * row;
        if (rfg == 28 && c >= 4) c -= 2;
        raw[u] = aload(rfg < 29 ? row * 2752 + c * 16 : OOB, sw1, rks * (32 * 2752) + (rfg < 29 ? rfg : 0) * 96);
      }
    }
    auto clear_b = [&](int g) {
      if (grp == g) {
        st_sc1(sent4, sws, l16, WS_O + tt * 65536 + cg * 4096 + (w >> 2) * 32768 + (w & 3) * 1024);
        st_sc1(sent4, sws, l16, WS_HN + (cg * 64 + 2 * tt) * 4096 + w * 1024);
        st_sc1(sent4, sws, gclr, 0);
        st_sc1(sent4, sws, sclr, WS_ST + (tt * 8 + cg) * 512);
      }
    };
    bar();
    STAMP(4);
    bf16x8 kf[9];
#pragma unroll
    for (int i = 1; i <= 9; ++i) kf[i % 9] = asfrag(lds_r128(kb + (i % 9) * KVT));
    v4u v0 = lds_r128(vb0 + 8 * KVT), v1 = lds_r128(vb1 + 8 * KVT), v2 = lds_r128(vb0), v3 = lds_r128(vb1);
    pin();
    clear_b(0);
    float m4[4] = {-3.0e38f, -3.0e38f, -3.0e38f, -3.0e38f};
    const int thr = r32 - 4 * h;
    f32x16 sa = mfma32(kf[1], qf, z16), sb = mfma32(kf[2], qf, z16), sc = mfma32(kf[3], qf, z16);
    pin();
    if (tb + 1 >= jmin) max16(m4, sa);
    pin(); sa = mfma32(kf[4], qf, z16); pin();
    if (tb + 2 >= jmin) max16(m4, sb);
    pin(); sb = mfma32(kf[5], qf, z16); pin();
    if (tb + 3 >= jmin) max16(m4, sc);
    pin(); sc = mfma32(kf[6], qf, z16); pin();
    if (tb + 4 >= jmin) max16(m4, sa);
    pin(); sa = mfma32(kf[7], qf, z16); pin();
    clear_b(1);
    if (tb + 5 >= jmin) max16(m4, sb);
    pin(); sb = mfma32(kf[8], qf, z16); pin();
    if (tb + 6 >= jmin) max16(m4, sc);
    pin(); sc = mfma32(kf[0], qf, z16); pin();
    if (tb + 7 >= jmin) max16(m4, sa);
    pin();
    if (tb < jmin) {
#pragma unroll
      for (int i = 0; i < 16; ++i) sc[i] = -3.0e38f;
    }
    f32x16 sm;
#pragma unroll
    for (int i = 0; i < 16; ++i) sm[i] = ((i & 3) + 8 * (i >> 2)) <= thr ? sb[i] : sc[i];
    max16(m4, sm);
    float mx = fmaxf(fmaxf(m4[0], m4[1]), fmaxf(m4[2], m4[3]));
    {
      const u32 a = __float_as_uint(mx);
      const auto sw = __builtin_amdgcn_permlane32_swap(a, a, false, false);
      const u32 s0 = sw[0], s1 = sw[1];
      mx = fmaxf(__uint_as_float(s0), __uint_as_float(s1));
    }
    CSTAMP(13);
    clear_b(2);
    f32x16 nm;
#pragma unroll
    for (int i = 0; i < 16; ++i) nm[i] = -mx;
    f32x16 sn = mfma32(kf[1], qf, nm);
    pin();
    f32x16 o = z16;
    {
      float pl[16], pf[16];
#pragma unroll
      for (int i = 0; i < 16; ++i) {
        const float p = __builtin_amdgcn_exp2f(sm[i] - mx);
        pl[i] = ((i & 3) + 8 * (i >> 2)) <= thr ? p : 0.f;
        pf[i] = p - pl[i];
      }
      o = mfma32(asfrag(v0), asfrag(pack8(pl)), o);
      o = mfma32(asfrag(v1), asfrag(pack8(pl + 8)), o);
      o = mfma32(asfrag(v2), asfrag(pack8(pf)), o);
      o = mfma32(asfrag(v3), asfrag(pack8(pf + 8)), o);
    }
    pin();
    v4u rfr[3];
    {
      if (grp == 3) vmwait<0>();
      asm volatile("s_waitcnt vmcnt(4)" : "+v"(raw[0]), "+v"(raw[1]), "+v"(raw[2])::"memory");
      clear_b(3);
#pragma unroll
      for (int u = 0; u < 3; ++u) lds_w128(wos + u * 1024 + l16, raw[u]);
      const u32 rd = wos + (4 * g4 + q4) * 96 + 8 * p4;
#pragma unroll
      for (int b = 0; b < 3; ++b) rfr[b] = __builtin_bit_cast(v4u, lds_tr(rd + 32 * b, rd + 32 * b + 16 * 96));
    }
    CSTAMP(14);
    const v4i swo = mksrd(gwo, 512 * 512 * 2);
    const u32 wovo = l8 * 1024 + ((c8 ^ (((l8 >> 1) & 3) << 1)) * 16);
    auto dma_wo = [&](int u) {
      const int uu = (u + loc) & 3, kk = ((u >> 2) + (loc >> 2)) & 1;
      dma16(wos + kk * 4096 + uu * 1024, wovo, swo, (4 * ckq + 2 * ca + kk) * 32768 + (16 * (uu & 1) + 8 * (uu >> 1)) * 1024 + cg * 128);
    };
    const u32 rvo = rfg < 29 ? l16 : OOB;
#pragma unroll
    for (int i = 1; i <= 7; ++i) {
      v0 = lds_r128(vb0 + i * KVT); v1 = lds_r128(vb1 + i * KVT);
      const f32x16 s = sn;
      if (i < 7) sn = mfma32(kf[i + 1], qf, nm);
      pin();
      if (i >= 2 && i <= 5) dma_wo(2 * i - 4);
      if (tb + i >= jmin) {
        float p[16];
#pragma unroll
        for (int j = 0; j < 8; ++j) p[j] = __builtin_amdgcn_exp2f(s[j]);
        o = mfma32(asfrag(v0), asfrag(pack8(p)), o);
        pin();
#pragma unroll
        for (int j = 8; j < 16; ++j) p[j] = __builtin_amdgcn_exp2f(s[j]);
        o = mfma32(asfrag(v1), asfrag(pack8(p + 8)), o);
      }
      pin();
      if (i == 1) asm volatile("s_waitcnt lgkmcnt(0)" : "+v"(rfr[0]), "+v"(rfr[1]), "+v"(rfr[2])::"memory");
      if (i >= 2 && i <= 5) dma_wo(2 * i - 3);
      if (i == 3) {
        vmwait<4>();
        signal(L_CNT, lane, rws, WS_FLAGB + wg * 128, tag);
      }
      if (i >= 3 && i <= 6 && grp == i - 3) {
#pragma unroll
        for (int b = 0; b < 3; ++b) st_sc1(rfr[b], sws, rvo, WS_W1T + ((rfg < 29 ? rfg : 0) * 64 + rks) * 3072 + b * 1024);
      }
      CSTAMP(14 + i);
    }
    const float inv = __builtin_amdgcn_rcpf(o[8]);
    const v4u of = guard4(v4u{cvtpk(o[0] * inv, o[1] * inv), cvtpk(o[2] * inv, o[3] * inv), cvtpk(o[4] * inv, o[5] * inv), cvtpk(o[6] * inv, o[7] * inv)});
    st_sc1(of, sws, (r32 >> 4) * 1024 + ((2 * (hh & 1) + h) * 16 + (r32 & 15)) * 16, WS_O + tt * 65536 + tb * 32768 + (2 * cg + (hh >> 1)) * 2048);
  }
  STAMP(5);

  u32 tra[4];
#pragma unroll
  for (int n = 0; n < 4; ++n) tra[n] = (4 * g4 + q4) * 128 + 8 * (p4 & 1) + (((2 * n + (p4 >> 1)) ^ ((((4 * g4 + q4) >> 1) & 3) << 1)) * 16);
  v4u hres, b1v;
  u32 b2v;
  {
    v4u xres = aload((64 * tt + 8 * w + l8) * 1024 + cg * 128 + c8 * 16, mksrd(gx, 2048 * 512 * 2), 0);
    asm volatile("buffer_load_ushort %0, %1, %2, 0 offen" : "=v"(b2v) : "v"((64 * tt + 8 * w + l8) * 2), "s"(mksrd(gb2, 2048 * 2)) : "memory");
    b1v = aload((don && w < 6) ? (48 * fg + 16 * ((2 * w + h) >> 2) + 8 * blk) * 2 : OOB, mksrd(gb1, 1376 * 2), 0);
    const u32 fbo = (w == 0 && lane < 8) ? WS_FLAGB + wg_of(tt, lane) * 128 : OOB;
    u32 fl = ld_flag(fbo, sws);
    const int a = w >> 2, kq = w & 3;
    asm volatile("s_waitcnt vmcnt(0)" : "+v"(fl), "+v"(xres), "+v"(b1v), "+v"(b2v)::"memory");
    CSTAMP(22);
    signal(L_CNT + 4, lane, rws, WS_FLAGW + wg * 128, tag);
    wait_flag(fl, fbo, sws, tag, spins);
    bar();
    v4u ofr[2][4];
    const u32 oso = WS_O + tt * 65536 + a * 32768 + kq * 8192;
    ld4(ofr[0], l16, sws, oso);
    ld4(ofr[1], l16, sws, oso + 4096);
    u32 fwo = OOB;
    {
      const int f8 = lane - 8, pq = lane < 8 ? 0 : 1 + f8 / 7, pi = lane < 8 ? lane : f8 - 7 * (pq - 1);
      if (w == 0 && don) fwo = WS_FLAGW + ((fg >> 3) * 64 + lane) * 128;
      if (w == 1 && don && lane < 32) fwo = WS_FLAGB + wg_of(lane, dt) * 128;
      if (w == 2 && lane < 29) fwo = WS_FLAGB + (((cg & 3) + 4 * pi) * 8 + 2 * pq + (cg >> 2)) * 128;
      if (w >= 4 && wg == 0) fwo = WS_FLAGA + ((w - 4) * 64 + lane) * 128;
    }
    u32 flw = ld_flag(fwo, sws);
    STAMP(6);
    bf16x8 wf[4][4];
#pragma unroll
    for (int k = 0; k < 4; ++k)
#pragma unroll
      for (int n = 0; n < 4; ++n) { const u32 ad = L_WO + (4 * kq + k) * 4096 + tra[n]; wf[k][n] = lds_tr(ad, ad + 2048); }
    pin();
    f32x4 acc[2][4];
#pragma unroll
    for (int m = 0; m < 2; ++m)
#pragma unroll
      for (int n = 0; n < 4; ++n) acc[m][n] = f32x4{0.f, 0.f, 0.f, 0.f};
#pragma unroll
    for (int k = 0; k < 4; ++k) {
      if (k == 0) take4<5>(ofr[0], l16, sws, oso, spins);
      if (k == 2) take4<1>(ofr[1], l16, sws, oso + 4096, spins);
#pragma unroll
      for (int m = 0; m < 2; ++m)
#pragma unroll
        for (int n = 0; n < 4; ++n) acc[m][n] = mfma16(wf[k][n], asfrag(ofr[k >> 1][2 * (k & 1) + m]), acc[m][n]);
      pin();
    }
    asm volatile("s_waitcnt vmcnt(0)" : "+v"(flw)::"memory");
    wait_flag(flw, fwo, sws, tag, spins);
    STAMP(7);
#pragma unroll
    for (int m = 0; m < 2; ++m)
#pragma unroll
      for (int n = 0; n < 4; ++n) {
        const int tok = 32 * a + 16 * m + (lane & 15);
        lds_wf4(L_PC + kq * 16384 + tok * 256 + (((4 * n + g4) ^ (lane & 15)) * 16), acc[m][n]);
      }
    bar();
    if (wg == 0 && tid == 0) __builtin_amdgcn_raw_buffer_store_b32(tag, rws, WS_EPOCH, 0, 16);
    const int tok = 8 * w + l8;
    f32x4 t[8];
#pragma unroll
    for (int k = 0; k < 4; ++k) {
      t[2 * k] = lds_rf4(L_PC + k * 16384 + tok * 256 + (((2 * c8) ^ (tok & 15)) * 16));
      t[2 * k + 1] = lds_rf4(L_PC + k * 16384 + tok * 256 + (((2 * c8 + 1) ^ (tok & 15)) * 16));
    }
    pin();
    ld_w1(0); ld_w1(1);
    f32x4 s0 = {bflo(xres.x), bfhi(xres.x), bflo(xres.y), bfhi(xres.y)}, s1 = {bflo(xres.z), bfhi(xres.z), bflo(xres.w), bfhi(xres.w)};
    s0 += (t[0] + t[2]) + (t[4] + t[6]); s1 += (t[1] + t[3]) + (t[5] + t[7]);
    hres = v4u{cvtpk(s0.x, s0.y), cvtpk(s0.z, s0.w), cvtpk(s1.x, s1.y), cvtpk(s1.z, s1.w)};
  }
  {
    float hv[8] = {bflo(hres.x), bfhi(hres.x), bflo(hres.y), bfhi(hres.y), bflo(hres.z), bfhi(hres.z), bflo(hres.w), bfhi(hres.w)};
    const float pm = sum8(((hv[0] + hv[1]) + (hv[2] + hv[3])) + ((hv[4] + hv[5]) + (hv[6] + hv[7]))) * (1.f / 64.f);
    float q0 = 0.f, q1 = 0.f;
#pragma unroll
    for (int i = 0; i < 8; i += 2) { q0 = fmaf(hv[i] - pm, hv[i] - pm, q0); q1 = fmaf(hv[i + 1] - pm, hv[i + 1] - pm, q1); }
    const float q = sum8(q0 + q1);
    const v2u stv = {guard(__float_as_uint(pm)), guard(__float_as_uint(q))};
    __builtin_amdgcn_raw_buffer_store_b64(stv, rws, c8 == 0 ? (8 * w + l8) * 8 : OOB, WS_ST + (tt * 8 + cg) * 512, 16);
    STAMP(8);
    ld_w1(2); ld_w1(3);
    vmwait<6>();
    const u32 sto = c8 * 512 + (8 * w + l8) * 8;
    v2u st;
    asm volatile("buffer_load_dwordx2 %0, %1, %2, %3 offen sc1" : "=v"(st) : "v"(sto), "s"(sws), "s"(WS_ST + tt * 4096) : "memory");
    ld_w1(4);
    asm volatile("s_waitcnt vmcnt(3)" : "+v"(st)::"memory");
    if (__builtin_expect(any_sent(st.x > st.y ? st.x : st.y), 0)) {
#pragma clang loop unroll(disable)
      do {
        asm volatile("buffer_load_dwordx2 %0, %1, %2, %3 offen sc1\n\ts_waitcnt vmcnt(0)" : "+v"(st) : "v"(sto), "s"(sws), "s"(WS_ST + tt * 4096) : "memory");
      } while (any_sent(st.x > st.y ? st.x : st.y) && ++spins < SPIN_CAP);
    }
    STAMP(9);
    ld_w1(5);
    const u32 sx_ = st.x, sy_ = st.y;
    const float mc = __uint_as_float(sx_), mean = sum8(mc) * 0.125f;
    const float var = sum8(fmaf(64.f * (mc - mean), mc - mean, __uint_as_float(sy_))) * (1.f / 512.f);
    const float r1 = __builtin_amdgcn_rsqf(var + 1e-5f);
    const float r2 = __builtin_amdgcn_rsqf(var * r1 * r1 + 1e-5f);
    const float sc = r1 * r2;
    const v4u hn = {cvtpk((hv[0] - mean) * sc, (hv[1] - mean) * sc), cvtpk((hv[2] - mean) * sc, (hv[3] - mean) * sc),
                    cvtpk((hv[4] - mean) * sc, (hv[5] - mean) * sc), cvtpk((hv[6] - mean) * sc, (hv[7] - mean) * sc)};
    lds_w128(L_PC + w * 2048 + l8 * 128 + ((c8 ^ (((l8 >> 1) & 3) << 1)) * 16), hn);
    bar();
    const int a = w >> 2, n = w & 3, row = 4 * g4 + q4;
    const u32 ad = L_PC + (4 * a + (row >> 3)) * 2048 + (row & 7) * 128 + (((2 * n + (p4 >> 1)) ^ (((row >> 1) & 3) << 1)) * 16) + 8 * (p4 & 1);
    const v4u fr = guard4(__builtin_bit_cast(v4u, lds_tr(ad, ad + 2 * 2048)));
    st_sc1(fr, sws, l16 + n * 1024, WS_HN + (cg * 64 + 2 * tt + a) * 4096);
    STAMP(10);
    ld_w1(6); ld_w1(7);
    bar();
  }
  const u32 area = w < 3 ? w * 16384 : 12288 + w * 12288;
  const u32 late = L_PD + (w < 3 ? w * 4096 : w * 8192 - 12288);
  auto take_w2 = [&](u32 slot, bf16x8 (&f)[4]) {
#pragma unroll
    for (int n = 0; n < 4; ++n) f[n] = lds_tr(slot + tra[n], slot + tra[n] + 2048);
  };
  auto issue_w2_early = [&]() {
    const int j = (loc >> 3) & 1;
    issue_w2(w + 8 + 8 * j, area + 4096 + j * 4096);
    issue_w2(w + 16 - 8 * j, area + 8192 - j * 4096);
    if (w < 3) issue_w2(w + 24, area + 12288);
  };
  if (don) {
    f32x4 acc[3][4];
#pragma unroll
    for (int b = 0; b < 3; ++b)
#pragma unroll
      for (int d = 0; d < 4; ++d) acc[b][d] = f32x4{0.f, 0.f, 0.f, 0.f};
    auto so = [&](int s) { return WS_HN + (dt * 64 + 8 * w + ((s + drot) & 7)) * 4096; };
    v4u hf[4][4];
    vmwait<6>();
#pragma unroll
    for (int s = 0; s < 4; ++s) ld4(hf[s], l16, sws, so(s));
#pragma unroll
    for (int s = 0; s < 8; ++s) {
      if (s <= 4) take4<12>(hf[s & 3], l16, sws, so(s), spins);
      else if (s == 5) take4<8>(hf[s & 3], l16, sws, so(s), spins);
      else if (s == 6) take4<4>(hf[s & 3], l16, sws, so(s), spins);
      else take4<0>(hf[s & 3], l16, sws, so(s), spins);
      if (s == 0) {
#pragma unroll
        for (int i = 0; i < 8; ++i) landed3(w1f[i]);
      }
      if (s == 7) issue_w2(w, area);
#pragma unroll
      for (int d = 0; d < 4; ++d)
#pragma unroll
        for (int b = 0; b < 3; ++b) acc[b][d] = mfma16(asfrag(w1f[s][b]), asfrag(hf[s & 3][d]), acc[b][d]);
      pin();
      if (s + 4 < 8) ld4(hf[s & 3], l16, sws, so(s + 4));
      pin();
      CSTAMP(26 + s);
    }
    STAMP(11);
    const u32 slot = L_PD + (w & 3) * PD_SLOT;
    if (w >= 4) {
#pragma unroll
      for (int b = 0; b < 3; ++b)
#pragma unroll
        for (int d = 0; d < 4; ++d) lds_wf4(slot + (b * 4 + d) * 1024 + l16, acc[b][d]);
    }
    bar();
    CSTAMP(34);
    if (w >= 4) issue_w2_early();
    if (w < 4) {
      f32x4 t[12];
#pragma unroll
      for (int i = 0; i < 12; ++i) t[i] = lds_rf4(slot + i * 1024 + l16);
      pin();
#pragma unroll
      for (int i = 0; i < 12; ++i) acc[i >> 2][i & 3] += t[i];
      asm volatile("" ::: "memory");
#pragma unroll
      for (int b = 0; b < 3; ++b)
#pragma unroll
        for (int d = 0; d < 4; ++d) lds_wf4(slot + (16 * d + (lane & 15)) * PD_PITCH + (4 * b + g4) * 16, acc[b][d]);
    }
    bar();
    CSTAMP(35);
    u32 go = lane == 0 ? WS_PAD + (wg * 8 + w) * 128 : OOB;
    v4u gv = sent4;
    if (w < 6) {
      const int hf2 = 2 * w + h, b = hf2 >> 2, d = hf2 & 3;
      const u32 ad = L_PD + (16 * d + (lane & 15)) * PD_PITCH + (4 * b + 2 * blk) * 16;
      f32x4 t[8];
#pragma unroll
      for (int k = 0; k < 4; ++k) { t[2 * k] = lds_rf4(ad + k * PD_SLOT); t[2 * k + 1] = lds_rf4(ad + k * PD_SLOT + 16); }
      pin();
      f32x4 s0 = {bflo(b1v.x), bfhi(b1v.x), bflo(b1v.y), bfhi(b1v.y)}, s1 = {bflo(b1v.z), bfhi(b1v.z), bflo(b1v.w), bfhi(b1v.w)};
      s0 += (t[0] + t[2]) + (t[4] + t[6]); s1 += (t[1] + t[3]) + (t[5] + t[7]);
      gv = guard4(v4u{cvtpk(gelu_tanh(s0.x), gelu_tanh(s0.y)), cvtpk(gelu_tanh(s0.z), gelu_tanh(s0.w)),
                      cvtpk(gelu_tanh(s1.x), gelu_tanh(s1.y)), cvtpk(gelu_tanh(s1.z), gelu_tanh(s1.w))});
      go = b < 2 ? g_ab + d * 1024 + ((2 * b + blk) * 16 + (lane & 15)) * 16
                 : (fg < 28 ? g_c + d * 1024 + (blk * 16 + (lane & 15)) * 16 : go);
    }
    st_sc1(gv, sws, go, 0);
    STAMP(12);
    bar();
    if (w < 4) issue_w2_early();
  } else {
    vmwait<0>();
    issue_w2(w, area);
    issue_w2_early();
    __builtin_amdgcn_s_sleep(100);
    STAMP(12);
  }
  {
    if (w < 3) issue_w2(w + 32, late);
    else { issue_w2(w + 24, late); issue_w2(w + 32, late + 4096); }
    f32x4 acc[4][4];
#pragma unroll
    for (int d = 0; d < 4; ++d)
#pragma unroll
      for (int n = 0; n < 4; ++n) acc[d][n] = f32x4{0.f, 0.f, 0.f, 0.f};
    const u32 so = WS_G + (cg * 43 + w) * 4096;
    v4u gf[4][4];
    bf16x8 wc[4];
    if (w >= 4) vmwait<8>();
    vmwait<16>();
    CSTAMP(36);
#pragma unroll
    for (int j = 0; j < 4; ++j) ld4(gf[j], l16, sws, so + j * 32768);
    take_w2(area, wc);
    CSTAMP(37);
#pragma unroll
    for (int j = 0; j < 6; ++j) {
      if (j < 5 || w < 3) {
        if (w >= 3) { if (j == 2) vmwait<8>(); else if (j == 3) vmwait<4>(); else if (j == 4) vmwait<0>(); }
        if (j <= 1) take4<12>(gf[j & 3], l16, sws, so + j * 32768, spins);
        else if (j == 2) take4<16>(gf[j & 3], l16, sws, so + j * 32768, spins);
        else if (j == 3) take4<12>(gf[j & 3], l16, sws, so + j * 32768, spins);
        else if (j == 4) { if (w < 3) vmwait<4>(); take4<8>(gf[j & 3], l16, sws, so + j * 32768, spins); }
        else take4<0>(gf[j & 3], l16, sws, so + j * 32768, spins);
        bf16x8 wn[4];
        if (j < 4 || (j == 4 && w < 3))
          take_w2(j < 2 ? area + 4096 + j * 4096 : j == 2 ? (w < 3 ? area + 12288 : late) : j == 3 ? (w < 3 ? late : late + 4096) : area, wn);
#pragma unroll
        for (int d = 0; d < 4; ++d)
#pragma unroll
          for (int n = 0; n < 4; ++n) acc[d][n] = mfma16(asfrag(gf[j & 3][d]), wc[n], acc[d][n]);
        pin();
        if (j == 0) ld4(gf[0], l16, sws, so + 4 * 32768);
        if (j == 1 && w < 3) { issue_w2(w + 40, area); ld4(gf[1], l16, sws, so + 5 * 32768); }
#pragma unroll
        for (int n = 0; n < 4; ++n) wc[n] = wn[n];
      }
      pin();
      CSTAMP(52 + j);
    }
    STAMP(13);
    asm volatile("s_waitcnt lgkmcnt(0)" ::: "memory");
#pragma unroll
    for (int d = 0; d < 4; ++d)
#pragma unroll
      for (int n = 0; n < 4; ++n) {
        const u32 base = n < 3 ? area + n * 4096 : late;
        lds_wf4(base + (lane & 15) * 256 + (((4 * d + g4) ^ (lane & 15)) * 16), acc[d][n]);
      }
    bar();
    CSTAMP(58);
    const int tok = 8 * w + l8;
    f32x4 t[16];
    const u32 rb = (tok & 15) * 256 + (((2 * c8) ^ (tok & 15)) * 16);
#pragma unroll
    for (int k = 0; k < 8; ++k) {
      const u32 ak = k < 3 ? k * 16384 : 12288 + k * 12288, lk = L_PD + (k < 3 ? k * 4096 : k * 8192 - 12288);
      const u32 ko2 = w < 6 ? ak + (w >> 1) * 4096 : lk;
      t[2 * k] = lds_rf4(rb + ko2);
      t[2 * k + 1] = lds_rf4((rb ^ 16) + ko2);
    }
    pin();
    const float bb = bflo(b2v);
    f32x4 s0 = {bflo(hres.x) + bb, bfhi(hres.x) + bb, bflo(hres.y) + bb, bfhi(hres.y) + bb};
    f32x4 s1 = {bflo(hres.z) + bb, bfhi(hres.z) + bb, bflo(hres.w) + bb, bfhi(hres.w) + bb};
    s0 += ((t[0] + t[2]) + (t[4] + t[6])) + ((t[8] + t[10]) + (t[12] + t[14]));
    s1 += ((t[1] + t[3]) + (t[5] + t[7])) + ((t[9] + t[11]) + (t[13] + t[15]));
    const v4u ov = {cvtpk(s0.x, s0.y), cvtpk(s0.z, s0.w), cvtpk(s1.x, s1.y), cvtpk(s1.z, s1.w)};
    asm volatile(".Lmega_pf_end:");
    *(v4u*)(gout + (64 * tt + tok) * 512 + cg * 64 + c8 * 8) = ov;
  }
  if (tid == 0) __hip_atomic_store((u32*)ws + 64 + wg, (u32)__builtin_amdgcn_s_memrealtime(), __ATOMIC_RELAXED, __HIP_MEMORY_SCOPE_AGENT);
}

extern "C" void kernel_launch(void* const* d_in, const int* in_sizes, int n_in, void* d_out, int out_size, void* d_ws, size_t ws_size,
                              hipStream_t stream) {
  (void)hipFuncSetAttribute((const void*)mega, hipFuncAttributeMaxDynamicSharedMemorySize, LDS_BYTES);
  mega<<<256, 512, LDS_BYTES, stream>>>((const u16*)d_in[0], (const u16*)d_in[1], (const u16*)d_in[2], (const u16*)d_in[4], (const u16*)d_in[5],
                                        (const u16*)d_in[6], (const u16*)d_in[7], (u16*)d_out, (char*)d_ws);
  k_post<<<256,512,0,stream>>>((unsigned*)d_ws, 0);
}
```

```cpp
#include <hip/hip_runtime.h>
#include <stdint.h>

typedef unsigned u32;
typedef unsigned short u16;
typedef u32 v4u __attribute__((ext_vector_type(4)));
typedef u32 v2u __attribute__((ext_vector_type(2)));
typedef int v4i __attribute__((ext_vector_type(4)));
typedef float f32x2 __attribute__((ext_vector_type(2)));
typedef float f32x4 __attribute__((ext_vector_type(4)));
typedef float f32x16 __attribute__((ext_vector_type(16)));
typedef short bf16x8 __attribute__((ext_vector_type(8)));
typedef short s16x4 __attribute__((ext_vector_type(4)));
typedef __bf16 bf2 __attribute__((ext_vector_type(2)));
#define LAS __attribute__((address_space(3)))
#define DI __device__ __forceinline__

#define ABL 0
#define CODE_PF 1
#define STAMPS 0
#define D_KV 0
#define HK_AT 3
#define W1_A 4
#define W1_B 8
#define W1_C 13
#define W2PF 20
#define D_O 4
#define D_ST -1
#define D_HN -1
#define D_XP -1
#define D_G -1
#define BG_TABLE 0, 0, 0, 0, 1, 1, 2, 2, 3, 3, 4, 4, 5,   6, 7, 9, 10
#define BG_SIG 11
#define BG_BAR 13
#define BG_FLAG 14
constexpr int NBG = 17, NBS = 13;
constexpr int BG_AT[NBG] = {BG_TABLE};
constexpr int bg_younger() { int n = 0; for (int op = NBS; op < NBG; ++op) n += BG_AT[op] <= BG_SIG ? 4 : 0; return n; }
constexpr int bg_at0() { int n = 0; for (int op = 0; op < NBS; ++op) n += BG_AT[op] == 0; return n; }
constexpr bool bg_ok() { for (int op = 0; op < NBS; ++op) for (int o2 = NBS; o2 < NBG; ++o2) if (BG_AT[op] > BG_AT[o2] || BG_AT[op] > BG_SIG || BG_AT[o2] > BG_BAR || BG_BAR > BG_FLAG) return false; return true; }
static_assert(bg_ok(), "stores, then wo, then the flag load; the signal follows the stores");
template <bool B> struct BoolC { static constexpr bool value = B; };

constexpr u32 WS_EPOCH = 0, WS_FLAGA = 1u << 19, WS_FLAGB = WS_FLAGA + 32768, WS_DBG = 65536;
constexpr u32 WS_KV = 1u << 20;
constexpr u32 WS_O = WS_KV + (4u << 20);
constexpr u32 WS_HN = WS_O + (2u << 20);
constexpr u32 WS_G = WS_HN + (2u << 20);
constexpr u32 WS_ST = WS_G + (2u << 20);
constexpr u32 WS_XP = WS_ST + (1u << 17);
constexpr u32 WS_END = WS_XP + 240 * 18432;
constexpr u32 SENT = 0xFFFFFFFFu;
constexpr u32 OOB = 0x7FFF0000u;
constexpr int SPIN_CAP = 1 << 16;

constexpr u32 L_XN = 0;
constexpr u32 KVT = 8448;
constexpr u32 L_CNT = 84480;
constexpr u32 L_RING = 86016;
constexpr u32 L_JUNK = L_RING + 65536;
constexpr u32 L_WO = 98304;
constexpr u32 L_PC = 0;
constexpr u32 L_W1 = 65536;
constexpr u32 L_PD = 0;
constexpr u32 LDS_BYTES = 163840;

DI u32 cvtpk(float a, float b) { f32x2 v = {a, b}; bf2 r = __builtin_convertvector(v, bf2); return __builtin_bit_cast(u32, r); }
DI float bflo(u32 x) { return __uint_as_float(x << 16); }
DI float bfhi(u32 x) { return __uint_as_float(x & 0xffff0000u); }
DI u32 guard(u32 x) { return x < 0xFFFFFFFEu ? x : 0xFFFFFFFEu; }
DI v4u guard4(v4u v) { v4u r = {guard(v.x), guard(v.y), guard(v.z), guard(v.w)}; return r; }
DI u32 umax3(u32 a, u32 b, u32 c) { u32 m = a > b ? a : b; return m > c ? m : c; }
DI u32 smax4(u32 m, v4u v) { return umax3(umax3(m, v.x, v.y), v.z, v.w); }
DI u32 vmax(v4u v) { return umax3(v.x > v.y ? v.x : v.y, v.z, v.w); }
DI u32 chk4(const v4u (&f)[4]) { return umax3(smax4(vmax(f[0]), f[1]), vmax(f[2]), vmax(f[3])); }
DI bool any_sent(u32 m) { return __builtin_amdgcn_ballot_w64(m == SENT) != 0; }
DI bf16x8 asfrag(v4u v) { return __builtin_bit_cast(bf16x8, v); }
DI f32x16 mfma32(bf16x8 a, bf16x8 b, f32x16 c) { return __builtin_amdgcn_mfma_f32_32x32x16_bf16(a, b, c, 0, 0, 0); }
DI f32x4 mfma16(bf16x8 a, bf16x8 b, f32x4 c) { return __builtin_amdgcn_mfma_f32_16x16x32_bf16(a, b, c, 0, 0, 0); }
DI v4u lds_r128(u32 a) { return *(const LAS v4u*)(uintptr_t)a; }
DI f32x4 lds_rf4(u32 a) { return *(const LAS f32x4*)(uintptr_t)a; }
DI void lds_w128(u32 a, v4u v) { *(LAS v4u*)(uintptr_t)a = v; }
DI void lds_wf4(u32 a, f32x4 v) { *(LAS f32x4*)(uintptr_t)a = v; }
DI bf16x8 lds_tr(u32 a0, u32 a1) {
  s16x4 lo = __builtin_amdgcn_ds_read_tr16_b64_v4i16((LAS s16x4*)(uintptr_t)a0);
  s16x4 hi = __builtin_amdgcn_ds_read_tr16_b64_v4i16((LAS s16x4*)(uintptr_t)a1);
  return __builtin_shufflevector(lo, hi, 0, 1, 2, 3, 4, 5, 6, 7);
}
template <int CTRL> DI float dpp(float v) { return __int_as_float(__builtin_amdgcn_update_dpp(0, __float_as_int(v), CTRL, 0xf, 0xf, true)); }
DI float sum8(float v) { v += dpp<0xB1>(v); v += dpp<0x4E>(v); v += dpp<0x141>(v); return v; }
DI v4i mksrd(const void* p, u32 bytes) { v4i s = {(int)(u32)(uintptr_t)p, (int)(u32)((uintptr_t)p >> 32), (int)bytes, 0x00020000}; return s; }
DI __amdgpu_buffer_rsrc_t mkrs(const void* p, u32 bytes) { return __builtin_amdgcn_make_buffer_rsrc((void*)p, 0, bytes, 0x00020000); }
DI void dma16(u32 lds, u32 voff, v4i srd, u32 soff) {
  asm volatile("s_mov_b32 m0, %0\n\ts_nop 0\n\tbuffer_load_dwordx4 %1, %2, %3 offen lds" ::"s"(lds), "v"(voff), "s"(srd), "s"(soff) : "memory");
}
DI v4u aload_sc1(u32 voff, v4i srd, u32 soff) {
  v4u r;
  asm volatile("buffer_load_dwordx4 %0, %1, %2, %3 offen sc1" : "=v"(r) : "v"(voff), "s"(srd), "s"(soff) : "memory");
  return r;
}
DI v4u aload(u32 voff, v4i srd, u32 soff) {
  v4u r;
  asm volatile("buffer_load_dwordx4 %0, %1, %2, %3 offen" : "=v"(r) : "v"(voff), "s"(srd), "s"(soff) : "memory");
  return r;
}
DI void ld4(v4u (&f)[4], u32 voff, v4i srd, u32 soff) {
  asm volatile("buffer_load_dwordx4 %0, %4, %5, %6 offen sc1\n\tbuffer_load_dwordx4 %1, %4, %5, %6 offen offset:1024 sc1\n\t"
               "buffer_load_dwordx4 %2, %4, %5, %6 offen offset:2048 sc1\n\tbuffer_load_dwordx4 %3, %4, %5, %6 offen offset:3072 sc1"
               : "=&v"(f[0]), "=&v"(f[1]), "=&v"(f[2]), "=&v"(f[3]) : "v"(voff), "s"(srd), "s"(soff) : "memory");
}
DI void ld4s(v4u (&f)[4], const u32 (&voff)[4], v4i srd, const u32 (&so)[4]) {
  asm volatile("buffer_load_dwordx4 %0, %4, %8, %9 offen sc1\n\tbuffer_load_dwordx4 %1, %5, %8, %10 offen sc1\n\t"
               "buffer_load_dwordx4 %2, %6, %8, %11 offen sc1\n\tbuffer_load_dwordx4 %3, %7, %8, %12 offen sc1"
               : "=&v"(f[0]), "=&v"(f[1]), "=&v"(f[2]), "=&v"(f[3])
               : "v"(voff[0]), "v"(voff[1]), "v"(voff[2]), "v"(voff[3]), "s"(srd), "s"(so[0]), "s"(so[1]), "s"(so[2]), "s"(so[3]) : "memory");
}
template <int N> DI void take4s(v4u (&f)[4], const u32 (&voff)[4], v4i srd, const u32 (&so)[4], int& spins) {
  asm volatile("s_waitcnt vmcnt(%4)" : "+v"(f[0]), "+v"(f[1]), "+v"(f[2]), "+v"(f[3]) : "n"(N) : "memory");
  if (__builtin_expect(any_sent(chk4(f)), 0)) {
#pragma clang loop unroll(disable)
    do {
      asm volatile("s_sleep 1\n\tbuffer_load_dwordx4 %0, %4, %8, %9 offen sc1\n\tbuffer_load_dwordx4 %1, %5, %8, %10 offen sc1\n\t"
                   "buffer_load_dwordx4 %2, %6, %8, %11 offen sc1\n\tbuffer_load_dwordx4 %3, %7, %8, %12 offen sc1\n\ts_waitcnt vmcnt(0)"
                   : "+v"(f[0]), "+v"(f[1]), "+v"(f[2]), "+v"(f[3])
                   : "v"(voff[0]), "v"(voff[1]), "v"(voff[2]), "v"(voff[3]), "s"(srd), "s"(so[0]), "s"(so[1]), "s"(so[2]), "s"(so[3]) : "memory");
    } while (any_sent(chk4(f)) && ++spins < SPIN_CAP);
  }
}
template <int N> DI void take4(v4u (&f)[4], u32 voff, v4i srd, u32 soff, int& spins) {
  asm volatile("s_waitcnt vmcnt(%4)" : "+v"(f[0]), "+v"(f[1]), "+v"(f[2]), "+v"(f[3]) : "n"(N) : "memory");
  if (__builtin_expect(any_sent(chk4(f)), 0)) {
#pragma clang loop unroll(disable)
    do {
      asm volatile("s_sleep 1\n\tbuffer_load_dwordx4 %0, %4, %5, %6 offen sc1\n\tbuffer_load_dwordx4 %1, %4, %5, %6 offen offset:1024 sc1\n\t"
                   "buffer_load_dwordx4 %2, %4, %5, %6 offen offset:2048 sc1\n\tbuffer_load_dwordx4 %3, %4, %5, %6 offen offset:3072 sc1\n\t"
                   "s_waitcnt vmcnt(0)"
                   : "+v"(f[0]), "+v"(f[1]), "+v"(f[2]), "+v"(f[3]) : "v"(voff), "s"(srd), "s"(soff) : "memory");
    } while (any_sent(chk4(f)) && ++spins < SPIN_CAP);
  }
}
template <int M1, int M2, int M3, int S1, int S2, int S3>
DI void dma4(u32 lds, u32 voff, v4i srd, u32 soff) {
  u32 t;
  soff = __builtin_amdgcn_readfirstlane(soff);
  lds = __builtin_amdgcn_readfirstlane(lds);
  asm volatile("s_mov_b32 m0, %1\n\ts_mov_b32 %0, %4\n\tbuffer_load_dwordx4 %2, %3, %0 offen lds\n\t"
               "s_add_i32 m0, m0, %5\n\ts_add_i32 %0, %0, %8\n\tbuffer_load_dwordx4 %2, %3, %0 offen lds\n\t"
               "s_add_i32 m0, m0, %6\n\ts_add_i32 %0, %0, %9\n\tbuffer_load_dwordx4 %2, %3, %0 offen lds\n\t"
               "s_add_i32 m0, m0, %7\n\ts_add_i32 %0, %0, %10\n\tbuffer_load_dwordx4 %2, %3, %0 offen lds"
               : "=&s"(t) : "s"(lds), "v"(voff), "s"(srd), "s"(soff), "n"(M1), "n"(M2), "n"(M3), "n"(S1), "n"(S2), "n"(S3) : "memory", "scc");
}
template <int N> DI void vmwait() { asm volatile("s_waitcnt vmcnt(%0)" ::"n"(N) : "memory"); }
DI void bar() { asm volatile("s_waitcnt lgkmcnt(0)\n\ts_barrier" ::: "memory"); }
DI v4u ld_sc1(__amdgpu_buffer_rsrc_t rs, u32 voff, u32 soff) { return __builtin_amdgcn_raw_buffer_load_b128(rs, voff, soff, 16); }
DI void st_sc1(v4u v, v4i srd, u32 voff, u32 soff) {
  asm volatile("buffer_store_dwordx4 %0, %1, %2, %3 offen sc1\n\ts_nop 1" ::"v"(v), "v"(voff), "s"(srd), "s"(soff) : "memory");
}
template <int D, int YOUNGER> DI void pace() { if (D < 0) vmwait<YOUNGER>(); else if (D > 0) __builtin_amdgcn_s_sleep(D < 0 ? 0 : D); }
DI void pin() { __builtin_amdgcn_sched_barrier(0); }

#define STAMP(i) do {} while (0)
#define CSTAMP(i) do {} while (0)

DI void max16(float (&m)[4], const f32x16& s) {
#pragma unroll
  for (int j = 0; j < 8; ++j) m[j & 3] = fmaxf(fmaxf(m[j & 3], s[2 * j]), s[2 * j + 1]);
}
DI v4u pack8(const float* p) { return v4u{cvtpk(p[0], p[1]), cvtpk(p[2], p[3]), cvtpk(p[4], p[5]), cvtpk(p[6], p[7])}; }
DI v4u pack44(f32x4 a, f32x4 b) { return v4u{cvtpk(a.x, a.y), cvtpk(a.z, a.w), cvtpk(b.x, b.y), cvtpk(b.z, b.w)}; }
DI f32x4 unlo(v4u v) { return f32x4{bflo(v.x), bfhi(v.x), bflo(v.y), bfhi(v.y)}; }
DI f32x4 unhi(v4u v) { return f32x4{bflo(v.z), bfhi(v.z), bflo(v.w), bfhi(v.w)}; }
DI float gelu_tanh(float x) {
  const float c1 = -2.0f * 0.7978845608028654f * 1.4426950408889634f, c2 = c1 * 0.044715f;
  const float e = __builtin_amdgcn_exp2f(x * (c1 + c2 * x * x));
  return x * __builtin_amdgcn_rcpf(1.0f + e);
}
struct Frag4 { bf16x8 f[4]; };
template <bool XFIRST, bool RDX>
DI void gemm4(f32x16& acc, const Frag4& xc, const Frag4& wc, Frag4& xn, Frag4& wn, const u32 (&xa)[4], u32 xo, u32 wo) {
#pragma unroll
  for (int s = 0; s < 4; ++s) {
    acc = XFIRST ? mfma32(xc.f[s], wc.f[s], acc) : mfma32(wc.f[s], xc.f[s], acc);
    if (RDX) xn.f[s] = asfrag(lds_r128(xa[s] + xo));
    wn.f[s] = lds_tr(wo + s * 2048, wo + s * 2048 + 512);
    pin();
  }
}
DI void ld3(v4u (&f)[3], u32 voff, v4i srd, u32 soff) {
  asm volatile("buffer_load_dwordx4 %0, %3, %4, %5 offen sc1\n\tbuffer_load_dwordx4 %1, %3, %4, %5 offen offset:1024 sc1\n\t"
               "buffer_load_dwordx4 %2, %3, %4, %5 offen offset:2048 sc1"
               : "=&v"(f[0]), "=&v"(f[1]), "=&v"(f[2]) : "v"(voff), "s"(srd), "s"(soff) : "memory");
}
template <int N> DI void take3(v4u (&f)[3], u32 voff, v4i srd, u32 soff, int& spins) {
  asm volatile("s_waitcnt vmcnt(%3)" : "+v"(f[0]), "+v"(f[1]), "+v"(f[2]) : "n"(N) : "memory");
  if (__builtin_expect(any_sent(umax3(vmax(f[0]), vmax(f[1]), vmax(f[2]))), 0)) {
#pragma clang loop unroll(disable)
    do {
      asm volatile("s_sleep 1\n\tbuffer_load_dwordx4 %0, %3, %4, %5 offen sc1\n\tbuffer_load_dwordx4 %1, %3, %4, %5 offen offset:1024 sc1\n\t"
                   "buffer_load_dwordx4 %2, %3, %4, %5 offen offset:2048 sc1\n\ts_waitcnt vmcnt(0)"
                   : "+v"(f[0]), "+v"(f[1]), "+v"(f[2]) : "v"(voff), "s"(srd), "s"(soff) : "memory");
    } while (any_sent(umax3(vmax(f[0]), vmax(f[1]), vmax(f[2]))) && ++spins < SPIN_CAP);
  }
}
template <int N> DI void signal(u32 cnt, int lane, __amdgpu_buffer_rsrc_t rws, u32 flag, u32 tag) {
  u32 old = 0;
  if (lane == 0) old = __hip_atomic_fetch_add((LAS u32*)(uintptr_t)cnt, 1u, __ATOMIC_RELAXED, __HIP_MEMORY_SCOPE_WORKGROUP);
  if (__builtin_amdgcn_readfirstlane(old) == N - 1 && lane == 0) __builtin_amdgcn_raw_buffer_store_b32(tag, rws, flag, 0, 16);
}
DI int wg_of(int tt, int cg) { return ((tt & 7) + 8 * (cg & 3)) * 8 + 2 * (tt >> 3) + (cg >> 2); }
DI int wg_ofd(int d2, int ft, int kc) { return (d2 + 4 * (ft & 7)) * 8 + 4 * (ft >> 3) + kc; }
DI u32 ld_flag(u32 off, v4i sws) {
  u32 f;
  asm volatile("buffer_load_dword %0, %1, %2, 0 offen sc1" : "=v"(f) : "v"(off), "s"(sws) : "memory");
  return f;
}
DI void wait_flag(u32& f, u32 off, v4i sws, u32 tag, int& spins) {
  if (__builtin_expect(__builtin_amdgcn_ballot_w64(off != OOB && f != tag) != 0, 0)) {
#pragma clang loop unroll(disable)
    do {
      asm volatile("s_sleep 2\n\tbuffer_load_dword %0, %1, %2, 0 offen sc1\n\ts_waitcnt vmcnt(0)" : "+v"(f) : "v"(off), "s"(sws) : "memory");
    } while (__builtin_amdgcn_ballot_w64(off != OOB && f != tag) != 0 && ++spins < SPIN_CAP);
  }
}

__global__ void __launch_bounds__(512, 2)
mega(const u16* __restrict__ gx, const u16* __restrict__ gwqkv, const u16* __restrict__ gwo, const u16* __restrict__ gw1,
     const u16* __restrict__ gb1, const u16* __restrict__ gw2, const u16* __restrict__ gb2, u16* __restrict__ gout, char* ws) {
  const int tid = threadIdx.x, lane = tid & 63;
  const int w = __builtin_amdgcn_readfirstlane(tid >> 6);
  const int wg = blockIdx.x, xcd = wg & 7, loc = wg >> 3;
  const int tt = 8 * (xcd >> 1) + (loc & 7), cg = 4 * (xcd & 1) + (loc >> 3);
  const int kc = xcd & 3, d2 = loc & 3, ft = 8 * (xcd >> 2) + (loc >> 2);
  const bool don = ft < 15;
  const int rot = ((loc & 7) + 2 * (loc >> 3)) & 7;
  STAMP(0);
  const u32 l16 = lane * 16;
  const int l8 = lane >> 3, c8 = lane & 7;
  int spins = 0;

  v4u xr[8];
  {
    __builtin_amdgcn_s_setprio(3);
    const v4i sx = mksrd(gx, 2048 * 512 * 2);
    const u32 vo = (64 * tt + 8 * w + l8) * 1024 + c8 * 16;
#pragma unroll
    for (int i = 0; i < 8; ++i) xr[i] = aload(vo, sx, ((i + rot) & 7) * 128);
    {
      unsigned long long pc;
      int len;
      asm volatile("s_getpc_b64 %0\n.Lmega_pf_start:\n\ts_mov_b32 %1, .Lmega_pf_end-.Lmega_pf_start" : "=s"(pc), "=s"(len));
      dma16(L_JUNK + (w & 1) * 1024, w < 2 ? l16 + (loc * 2 + w) * 1024 : OOB, mksrd((const void*)(pc & ~63ull), len > 0 ? len : 0), 0);
    }
  }
  __builtin_amdgcn_s_setprio(0);
  constexpr int PF = CODE_PF ? 1 : 0;
  u32 epoch;
  asm volatile("s_load_dword %0, %1, 0x0" : "=s"(epoch) : "s"(ws) : "memory");
  const v4i swqkv = mksrd(gwqkv, 512 * 1536 * 2);
  const __amdgpu_buffer_rsrc_t rws = mkrs(ws, WS_END);
  const v4i sws = mksrd(ws, WS_END);
  v4u sent4 = {SENT, SENT, SENT, SENT};
  asm volatile("" : "+v"(sent4));
  u32 kvs = WS_KV + (tt * 8 + cg) * 16384;
  auto clear_kv = [&](int g) { if ((loc & 3) == g && !(ABL & 32)) { st_sc1(sent4, sws, l16, kvs + w * 1024); st_sc1(sent4, sws, l16, kvs + w * 1024 + 8192); } };
  u32 wvq = (8 * w + l8) * 3072 + ((c8 ^ (((l8 >> 1) & 1) << 2)) * 16) + cg * 128, wvk = wvq + 1024, wvv = wvq + 2048;
  u32 ko[8];
#pragma unroll
  for (int i = 0; i < 8; ++i) ko[i] = ((i + rot) & 7) * (64 * 3072);
  u32 ring = L_RING + w * 1024;
  auto issue_w = [&](int st) {
    dma16(ring + (st & 3) * 16384, st < 8 ? wvk : wvq, swqkv, st < 8 ? ko[st & 7] : ko[(2 * st) & 7]);
    dma16(ring + (st & 3) * 16384 + 8192, st < 8 ? wvv : wvq, swqkv, st < 8 ? ko[st & 7] : ko[(2 * st + 1) & 7]);
  };
  const int r32 = lane & 31, h = lane >> 5, blk = (lane >> 4) & 1, q4 = (lane & 15) >> 2, p4 = lane & 3, g4 = lane >> 4;
  const int hh = w & 3, tb = w >> 2;
  const int isV = w & 1, hp = (w >> 1) & 1;
  u32 xa[4];
#pragma unroll
  for (int s = 0; s < 4; ++s) xa[s] = L_XN + (32 * tb + r32) * 128 + (((2 * s + h) ^ ((r32 >> 1) & 7)) * 16);
  u32 wa = L_RING + isV * 8192 + (8 * h + q4) * 128 + (((4 * hp + 2 * blk + (p4 >> 1)) ^ (((q4 >> 1) & 1) << 2)) * 16) + 8 * (p4 & 1);
  u32 qwa = L_RING + (8 * h + q4) * 128 + (((2 * hh + (p4 >> 1)) ^ (((q4 >> 1) & 1) << 2)) * 16) + 8 * (p4 & 1);
  u32 xw = L_XN + (8 * w + l8) * 128 + ((c8 ^ (((8 * w + l8) >> 1) & 7)) * 16);
  u32 fao = (tt - 4 + lane >= 0 && lane < 4) ? WS_FLAGA + wg_of(tt - 4 + lane, cg) * 128 : OOB;
  u32 kvo = isV ? (2 * hp + (r32 >> 4)) * 1024 + h * 256 + (r32 & 15) * 16 : 2 * hp * 1024 + l16;
  u32 kvl = (8 + tb) * KVT + (isV ? (2 * hp + (r32 >> 4)) * 2048 + 1024 + h * 256 + (r32 & 15) * 16 : 2 * hp * 2048 + l16);
  const int hpt = tt - 4 + (w >> 1);
  u32 hso = WS_KV + ((hpt >= 0 ? hpt : 0) * 8 + cg) * 16384 + (w & 1) * 4096 + 8192, hvo = hpt >= 0 ? l16 : OOB;
  u32 hks[4], hko[4];
#pragma unroll
  for (int j = 0; j < 4; ++j) {
    const bool ok = tt - 4 + j >= 0;
    hks[j] = WS_KV + ((ok ? tt - 4 + j : 0) * 8 + cg) * 16384 + (ok ? tb * 4096 : 0) + hh * 1024;
    hko[j] = ok ? l16 : h * 512;
  }
  asm volatile("" : "+v"(xa[0]), "+v"(xa[1]), "+v"(xa[2]), "+v"(xa[3]), "+v"(wa), "+v"(qwa), "+v"(xw), "+v"(fao), "+v"(kvo), "+v"(kvl), "+v"(hvo),
               "+v"(wvq), "+v"(wvk), "+v"(wvv));
  asm volatile("" : "+s"(ko[0]), "+s"(ko[1]), "+s"(ko[2]), "+s"(ko[3]), "+s"(ko[4]), "+s"(ko[5]), "+s"(ko[6]), "+s"(ko[7]), "+s"(kvs), "+s"(ring), "+s"(hso));
  asm volatile("" : "+s"(hks[0]), "+s"(hks[1]), "+s"(hks[2]), "+s"(hks[3]));
  {
    f32x2 xv[32];
    f32x2 s1a = {0.f, 0.f}, s1b = s1a, s2a = s1a, s2b = s1a, shift;
    auto acc4 = [&](int i) {
      const u32 d[4] = {xr[i].x, xr[i].y, xr[i].z, xr[i].w};
#pragma unroll
      for (int j = 0; j < 4; ++j) {
        const f32x2 a = f32x2{bflo(d[j]), bfhi(d[j])} - shift;
        xv[4 * i + j] = a;
        if (j & 1) { s1b += a; s2b = __builtin_elementwise_fma(a, a, s2b); } else { s1a += a; s2a = __builtin_elementwise_fma(a, a, s2a); }
      }
    };
    CSTAMP(40);
    asm volatile("s_waitcnt vmcnt(%2)" : "+v"(xr[0]), "+v"(xr[1]) : "n"(6 + PF) : "memory");
    CSTAMP(41);
    {
      const float sh0 = dpp<0x00>(bflo(xr[0].x)), sh1 = dpp<0x141>(sh0), sh = (lane & 4) ? sh1 : sh0;
      shift = f32x2{sh, sh};
    }
    acc4(0); acc4(1); pin();
    asm volatile("s_waitcnt vmcnt(%2)" : "+v"(xr[2]), "+v"(xr[3]) : "n"(4 + PF) : "memory");
    acc4(2); acc4(3); pin();
    asm volatile("s_waitcnt vmcnt(%2)" : "+v"(xr[4]), "+v"(xr[5]) : "n"(2 + PF) : "memory");
    acc4(4); acc4(5); pin();
    issue_w(0);
    asm volatile("s_waitcnt vmcnt(%2)" : "+v"(xr[6]), "+v"(xr[7]) : "n"(2 + PF) : "memory");
    CSTAMP(47);
    issue_w(1);
    acc4(6); acc4(7);
    s1a += s1b; s2a += s2b;
    const float s1 = sum8(s1a.x + s1a.y) * (1.f / 512.f), s2 = sum8(s2a.x + s2a.y) * (1.f / 512.f);
    const float rs = __builtin_amdgcn_rsqf(fmaxf(s2 - s1 * s1, 0.f) + 1e-5f), nb = -s1 * rs;
    const f32x2 rs2 = {rs, rs}, nb2 = {nb, nb};
#pragma unroll
    for (int i = 0; i < 8; ++i) {
      u32 o[4];
#pragma unroll
      for (int j = 0; j < 4; ++j) { const f32x2 y = __builtin_elementwise_fma(xv[4 * i + j], rs2, nb2); o[j] = cvtpk(y.x, y.y); }
      lds_w128(xw + i * 8192, v4u{o[0], o[1], o[2], o[3]});
      if (i == 2) { pin(); issue_w(2); pin(); }
      if (i == 5) { pin(); clear_kv(0); pin(); }
    }
    if (tid == 0) *(LAS u32*)(uintptr_t)L_CNT = 0;
    pin();
    issue_w(3); clear_kv(1);
    CSTAMP(48);
  }
  asm volatile("s_waitcnt lgkmcnt(0)" : "+s"(epoch)::"memory");
  const u32 tag = epoch + 1;
  STAMP(1);

  Frag4 xk[8], wa_, wb_;
  u32 fla;
  {
    f32x16 acc;
#pragma unroll
    for (int i = 0; i < 16; ++i) acc[i] = 0.f;
    vmwait<6>(); bar();
    CSTAMP(51);
    auto loop = [&](auto xfirst) {
      constexpr bool XF = decltype(xfirst)::value;
#pragma unroll
      for (int s = 0; s < 4; ++s) { xk[0].f[s] = asfrag(lds_r128(xa[s])); wa_.f[s] = lds_tr(wa + s * 2048, wa + s * 2048 + 512); }
      vmwait<4>(); bar(); issue_w(4); clear_kv(2);
      gemm4<XF, true>(acc, xk[0], wa_, xk[1], wb_, xa, 1 * 8192, wa + 1 * 16384);
      vmwait<4>(); bar(); issue_w(5); clear_kv(3);
      gemm4<XF, true>(acc, xk[1], wb_, xk[2], wa_, xa, 2 * 8192, wa + 2 * 16384);
      CSTAMP(0);
      vmwait<4>(); bar(); issue_w(6);
      gemm4<XF, true>(acc, xk[2], wa_, xk[3], wb_, xa, 3 * 8192, wa + 3 * 16384);
      vmwait<4>(); bar(); issue_w(7);
      gemm4<XF, true>(acc, xk[3], wb_, xk[4], wa_, xa, 4 * 8192, wa);
      CSTAMP(2);
      vmwait<4>(); bar(); issue_w(8);
      gemm4<XF, true>(acc, xk[4], wa_, xk[5], wb_, xa, 5 * 8192, wa + 1 * 16384);
      vmwait<4>(); bar(); issue_w(9);
      if (tid == 0) __builtin_amdgcn_raw_buffer_store_b32(tag, rws, WS_FLAGA + wg * 128, 0, 16);
      gemm4<XF, true>(acc, xk[5], wb_, xk[6], wa_, xa, 6 * 8192, wa + 2 * 16384);
      CSTAMP(4);
      vmwait<4>(); bar(); issue_w(10);
      gemm4<XF, true>(acc, xk[6], wa_, xk[7], wb_, xa, 7 * 8192, wa + 3 * 16384);
      vmwait<4>(); bar(); issue_w(11);
      fla = ld_flag(fao, sws);
      gemm4<XF, false>(acc, xk[7], wb_, xk[7], wa_, xa, 0, qwa);
      CSTAMP(6);
      const v4u a = guard4(v4u{cvtpk(acc[0], acc[1]), cvtpk(acc[2], acc[3]), cvtpk(acc[4], acc[5]), cvtpk(acc[6], acc[7])});
      st_sc1(a, sws, kvo, kvs + (XF ? 8192 : 0) + tb * 4096);
      const v4u b = guard4(v4u{cvtpk(acc[8], acc[9]), cvtpk(acc[10], acc[11]), cvtpk(acc[12], acc[13]), cvtpk(acc[14], acc[15])});
      st_sc1(b, sws, kvo, kvs + (XF ? 8192 + 512 : 1024) + tb * 4096);
      lds_w128(kvl, a);
      lds_w128(kvl + (XF ? 512 : 2048), b);
    };
    if (isV) loop(BoolC<true>{}); else loop(BoolC<false>{});
    STAMP(2);
  }
  bf16x8 qf;
  v4u hk[4], hv[4];
  u32 kb, vb0, vb1, fm[16], pm[8], ovo, wovo, cst, cgv, cxv, fbo;
  u32 so_o, so_hn, so_st, so_g, so_x, so_xc;
  u32 tra[4], pcw[4], pcr, w1vo, w1so, oso, xro, b1o, feo;
  const v4i sw1 = mksrd(gw1, 2048 * 1376 * 2);
  const int s6 = w < 2 * kc ? w : w - 2;
  {
    f32x16 acc;
#pragma unroll
    for (int i = 0; i < 16; ++i) acc[i] = 0.f;
    if (tid < 160) { const u32 one = (tid >> 5) + tt >= 4 ? 0x3F803F80u : 0u; lds_w128((tid >> 4) * KVT + 8192 + (tid & 15) * 16, v4u{one, one, one, one}); }
    gemm4<false, false>(acc, xk[0], wa_, xk[0], wb_, xa, 0, qwa + 1 * 8192);
    vmwait<7>(); bar();
    gemm4<false, false>(acc, xk[1], wb_, xk[0], wa_, xa, 0, qwa + 2 * 8192);
    gemm4<false, false>(acc, xk[2], wa_, xk[0], wb_, xa, 0, qwa + 3 * 8192);
    CSTAMP(7);
    asm volatile("s_waitcnt vmcnt(2)" : "+v"(fla)::"memory");
    wait_flag(fla, fao, sws, tag, spins);
    bar();
    CSTAMP(8);
    if (HK_AT == 3) { pace<D_KV, 0>(); ld4s(hk, hko, sws, hks); }
    gemm4<false, false>(acc, xk[3], wb_, xk[0], wa_, xa, 0, qwa + 4 * 8192);
    if (HK_AT == 4) { pace<D_KV, 0>(); ld4s(hk, hko, sws, hks); }
    gemm4<false, false>(acc, xk[4], wa_, xk[0], wb_, xa, 0, qwa + 5 * 8192);
    if (HK_AT == 5) { pace<D_KV, 0>(); ld4s(hk, hko, sws, hks); }
    {
      kb = tb * KVT + hh * 2048 + l16;
      const u32 ones = tb * KVT + 8192 + ((r32 >= 20 && r32 < 28) ? 64 : 0);
      vb0 = r32 < 16 ? tb * KVT + hh * 2048 + 1024 + h * 256 + r32 * 16 : ones;
      vb1 = r32 < 16 ? vb0 + 512 : ones;
#pragma unroll
      for (int i = 0; i < 16; ++i) fm[i] = (u32)(((i & 3) + 8 * (i >> 2) - 1 - (r32 - 4 * h)) >> 31);
#pragma unroll
      for (int k = 0; k < 8; ++k) pm[k] = (fm[2 * k] & 0xFFFFu) | (fm[2 * k + 1] & 0xFFFF0000u);
      ovo = (r32 >> 4) * 1024 + ((2 * (hh & 1) + h) * 16 + (r32 & 15)) * 16 + tb * 32768 + (hh >> 1) * 2048;
      asm volatile("" : "+v"(kb), "+v"(vb0), "+v"(vb1), "+v"(pm[0]), "+v"(pm[1]), "+v"(pm[2]), "+v"(pm[3]), "+v"(pm[4]), "+v"(pm[5]), "+v"(pm[6]),
                   "+v"(pm[7]), "+v"(ovo));
      asm volatile("" : "+v"(fm[0]), "+v"(fm[1]), "+v"(fm[2]), "+v"(fm[3]), "+v"(fm[4]), "+v"(fm[5]), "+v"(fm[6]), "+v"(fm[7]), "+v"(fm[8]), "+v"(fm[9]),
                   "+v"(fm[10]), "+v"(fm[11]), "+v"(fm[12]), "+v"(fm[13]), "+v"(fm[14]), "+v"(fm[15]));
    }
    CSTAMP(9);
    gemm4<false, false>(acc, xk[5], wb_, xk[0], wa_, xa, 0, qwa + 6 * 8192);
    if (HK_AT == 6) { pace<D_KV, 0>(); ld4s(hk, hko, sws, hks); }
    {
      wovo = (ABL & 4) ? OOB : l8 * 1024 + ((c8 ^ (((l8 >> 1) & 3) << 1)) * 16) + cg * 128;
      cst = tid < 32 ? l16 : OOB;
      cgv = (don && !(ABL & 2) && w < 3 && (w == 0 || ft < 14)) ? l16 : OOB;
      cxv = (don && w != kc && !(ABL & 1)) ? l16 : OOB;
      fbo = OOB;
      if (w == 0 && lane < 8) fbo = WS_FLAGB + wg_of(tt, lane) * 128;
      if (w == 1 && don && lane < 16) fbo = WS_FLAGB + wg_of(8 * kc + (lane & 7), 2 * d2 + (lane >> 3)) * 128;
      if (w == 1 && don && lane >= 16 && lane < 20) fbo = WS_FLAGB + wg_ofd(d2, ft, lane & 3) * 128;
      if (w == 2 && lane < 30) fbo = WS_FLAGB + wg_ofd(cg >> 1, lane >> 1, 2 * (cg & 1) + (lane & 1)) * 128;
      asm volatile("" : "+v"(wovo), "+v"(cst), "+v"(cgv), "+v"(cxv), "+v"(fbo));
      so_o = WS_O + tt * 65536 + cg * 4096;
      so_hn = WS_HN + cg * 262144 + tt * 4096;
      so_st = WS_ST + tt * 4096;
      so_g = don ? WS_G + ((2 * d2 + (kc >> 1)) * 43 + 3 * ft) * 4096 + (kc & 1) * 2048 : 0;
      so_x = don ? WS_XP + (d2 * 15 + ft) * 73728 : 0;
      so_xc = so_x + w * 18432 + ((kc - w - 1) & 3) * 1024;
      asm volatile("" : "+s"(so_o), "+s"(so_hn), "+s"(so_st), "+s"(so_g), "+s"(so_x), "+s"(so_xc));
    }
    gemm4<false, false>(acc, xk[6], wa_, xk[0], wb_, xa, 0, qwa + 7 * 8192);
    {
#pragma unroll
      for (int n = 0; n < 4; ++n) tra[n] = (4 * g4 + q4) * 128 + 8 * (p4 & 1) + (((2 * n + (p4 >> 1)) ^ ((((4 * g4 + q4) >> 1) & 3) << 1)) * 16);
      pcr = L_PC + (8 * w + l8) * 256 + (((2 * c8) ^ ((8 * w + l8) & 15)) * 16);
#pragma unroll
      for (int n = 0; n < 4; ++n) pcw[n] = L_PC + (w & 3) * 16384 + (32 * (w >> 2) + (lane & 15)) * 256 + (((4 * n + g4) ^ (lane & 15)) * 16);
      const int q = 64 * w + lane, row = q / 12, c = (q - 12 * row) ^ (((row >> 2) & 1) << 1);
      w1vo = (don && !(ABL & 8) && (ft < 14 || c < 4)) ? row * 2752 + c * 16 : OOB;
      xro = (64 * tt + 8 * w + l8) * 1024 + cg * 128 + c8 * 16;
      b1o = don ? (96 * ft + 32 * (s6 >> 1) + 4 * g4) * 2 : OOB;
      feo = (wg == 0 && w >= 4) ? WS_FLAGA + ((w - 4) * 64 + lane) * 128 : OOB;
      asm volatile("" : "+v"(pcr), "+v"(pcw[0]), "+v"(pcw[1]), "+v"(pcw[2]), "+v"(pcw[3]), "+v"(tra[0]), "+v"(tra[1]), "+v"(tra[2]), "+v"(tra[3]), "+v"(w1vo),
                   "+v"(xro), "+v"(b1o), "+v"(feo));
      w1so = don ? kc * (512 * 2752) + ft * 192 : 0;
      oso = WS_O + tt * 65536 + (w >> 2) * 32768 + (w & 3) * 8192;
      asm volatile("" : "+s"(oso), "+s"(w1so));
    }
#pragma unroll
    for (int s = 0; s < 4; ++s) acc = mfma32(wb_.f[s], xk[7].f[s], acc);
    ld4(hv, hvo, sws, hso);
    CSTAMP(10);
    const float qs = 0.25f * 1.4426950408889634f;
    qf = asfrag(v4u{cvtpk(acc[0] * qs, acc[1] * qs), cvtpk(acc[2] * qs, acc[3] * qs), cvtpk(acc[4] * qs, acc[5] * qs), cvtpk(acc[6] * qs, acc[7] * qs)});
  }
  STAMP(3);

  u32 flb, fle, b2v;
  v4u xres;
  v2u b1a, b1b;
  bf16x8 wf[4][4];
  {
    const bool ldr = w < 4;
    const v4i swo = mksrd(gwo, 512 * 512 * 2);
    auto bg = [&](int op) {
      if (op < 2) st_sc1(sent4, sws, (ABL & 2) ? cst : l16, so_o + w * 1024 + op * 32768);
      else if (op == 2) st_sc1(sent4, sws, cst, so_st + cg * 512);
      else if (op < 5) st_sc1(sent4, sws, (ABL & 2) ? cst : l16, so_hn + w * 1024 + (op - 3) * 131072);
      else if (op < 7) st_sc1(sent4, sws, cgv, so_g + w * 4096 + (op - 5) * 1024);
      else if (op < 13) st_sc1(sent4, sws, (op >= 9 && ft == 14) ? OOB : cxv, so_xc + (op - 7) * 3072);
      else {
        const int kk = 4 * w + ((op - 13 + loc) & 3);
        dma4<1024, 1024, 1024, 16384, -8192, 16384>(L_WO + kk * 4096, wovo, swo, kk * 32768);
      }
    };
    auto hook = [&](int pt) {
      if (ldr) {
#pragma unroll
        for (int op = 0; op < NBG; ++op) if (BG_AT[op] == pt) bg(op);
        if (pt == BG_SIG) { vmwait<bg_younger()>(); signal<4>(L_CNT, lane, rws, WS_FLAGB + wg * 128, tag); }
      }
      if (pt == BG_BAR) {
        vmwait<0>(); bar();
        xres = aload(xro, mksrd(gx, 2048 * 512 * 2), 0);
        asm volatile("buffer_load_ushort %0, %1, %2, 0 offen" : "=v"(b2v) : "v"((xro >> 10) * 2), "s"(mksrd(gb2, 2048 * 2)) : "memory");
        asm volatile("buffer_load_dwordx2 %0, %2, %3, 0 offen\n\tbuffer_load_dwordx2 %1, %2, %3, 0 offen offset:32" : "=&v"(b1a), "=&v"(b1b) : "v"(b1o), "s"(mksrd(gb1, 1376 * 2)) : "memory");
        fle = ld_flag(feo, sws);
      }
      if (pt == BG_BAR || pt == BG_BAR + 1) {
#pragma unroll
        for (int k = 2 * (pt - BG_BAR); k < 2 * (pt - BG_BAR) + 2; ++k)
#pragma unroll
          for (int n = 0; n < 4; ++n) { const u32 ad = L_WO + (w & 3) * 16384 + tra[n] + k * 4096; wf[k][n] = lds_tr(ad, ad + 2048); }
      }
      if (pt == BG_FLAG) flb = ld_flag(fbo, sws);
      pin();
    };
    f32x16 z16;
#pragma unroll
    for (int i = 0; i < 16; ++i) z16[i] = 0.f;
    bf16x8 kf[9];
    kf[8] = asfrag(lds_r128(kb + 8 * KVT));
    f32x16 sb = mfma32(kf[8], qf, z16);
    take4s<4>(hk, hko, sws, hks, spins);
    CSTAMP(11);
#pragma unroll
    for (int j = 0; j < 4; ++j) { lds_w128(kb + 2 * j * KVT, hk[j]); kf[2 * j] = asfrag(hk[j]); }
    float m4[4] = {-3.0e38f, -3.0e38f, -3.0e38f, -3.0e38f};
    f32x16 sc = mfma32(kf[0], qf, z16), sa = mfma32(kf[2], qf, z16), sd = mfma32(kf[4], qf, z16), se = mfma32(kf[6], qf, z16);
    bar();
    STAMP(4);
#pragma unroll
    for (int i = 1; i < 8; i += 2) kf[i] = asfrag(lds_r128(kb + i * KVT));
    pin();
    hook(0);
    f32x16 sm;
#pragma unroll
    for (int i = 0; i < 16; ++i) { const float a = sb[i], b = sc[i]; sm[i] = __uint_as_float((fm[i] & __float_as_uint(a)) | (~fm[i] & __float_as_uint(b))); }
    max16(m4, sm);
    pin(); max16(m4, sa); pin();
    sa = mfma32(kf[1], qf, z16); sb = mfma32(kf[3], qf, z16);
    pin(); max16(m4, sd); pin();
    sc = mfma32(kf[5], qf, z16);
    pin(); max16(m4, se); pin();
    sd = mfma32(kf[7], qf, z16);
    pin(); max16(m4, sa); max16(m4, sb); max16(m4, sc); max16(m4, sd);
    float mx = fmaxf(fmaxf(m4[0], m4[1]), fmaxf(m4[2], m4[3]));
    {
      const u32 a = __float_as_uint(mx);
      const auto sw = __builtin_amdgcn_permlane32_swap(a, a, false, false);
      const u32 s0 = sw[0], s1 = sw[1];
      mx = fmaxf(__uint_as_float(s0), __uint_as_float(s1));
    }
    CSTAMP(12);
    f32x16 nm;
#pragma unroll
    for (int i = 0; i < 16; ++i) nm[i] = -mx;
    f32x16 sn = mfma32(kf[1], qf, nm);
    pin();
    u32 pk[8];
#pragma unroll
    for (int k = 0; k < 8; ++k) pk[k] = cvtpk(__builtin_amdgcn_exp2f(sm[2 * k] - mx), __builtin_amdgcn_exp2f(sm[2 * k + 1] - mx));
    v4u pl0 = {pk[0] & pm[0], pk[1] & pm[1], pk[2] & pm[2], pk[3] & pm[3]}, pl1 = {pk[4] & pm[4], pk[5] & pm[5], pk[6] & pm[6], pk[7] & pm[7]};
    asm volatile("" : "+v"(pl0), "+v"(pl1));
    v4u pf0 = {pk[0] ^ pl0.x, pk[1] ^ pl0.y, pk[2] ^ pl0.z, pk[3] ^ pl0.w}, pf1 = {pk[4] ^ pl1.x, pk[5] ^ pl1.y, pk[6] ^ pl1.z, pk[7] ^ pl1.w};
    asm volatile("" : "+v"(pf0), "+v"(pf1));
    pin();
    if (!ldr) vmwait<0>();
    take4<bg_at0()>(hv, hvo, sws, hso, spins);
#pragma unroll
    for (int u = 0; u < 4; ++u) lds_w128(w * KVT + u * 2048 + 1024 + l16, hv[u]);
    CSTAMP(13);
    bar();
    v4u v0 = lds_r128(vb0 + 8 * KVT), v1 = lds_r128(vb1 + 8 * KVT), v2 = lds_r128(vb0), v3 = lds_r128(vb1);
    hook(1);
    f32x16 o = z16;
    CSTAMP(14);
#pragma unroll
    for (int i = 1; i <= 7; ++i) {
      const f32x16 s = sn;
      if (i < 7) sn = mfma32(kf[i + 1], qf, nm);
      v4u va = lds_r128(vb0 + i * KVT), vb = lds_r128(vb1 + i * KVT);
      pin();
      float p[8];
#pragma unroll
      for (int j = 0; j < 8; ++j) p[j] = __builtin_amdgcn_exp2f(s[j]);
      const v4u pa = pack8(p);
      if (i == 1) {
        o = mfma32(asfrag(v0), asfrag(pl0), o);
        o = mfma32(asfrag(v1), asfrag(pl1), o);
        o = mfma32(asfrag(v2), asfrag(pf0), o);
        o = mfma32(asfrag(v3), asfrag(pf1), o);
      }
      o = mfma32(asfrag(va), asfrag(pa), o);
      pin();
      hook(2 * i);
#pragma unroll
      for (int j = 0; j < 8; ++j) p[j] = __builtin_amdgcn_exp2f(s[8 + j]);
      const v4u pb = pack8(p);
      o = mfma32(asfrag(vb), asfrag(pb), o);
      pin();
      hook(2 * i + 1);
      CSTAMP(14 + i);
    }
    const float inv = __builtin_amdgcn_rcpf(o[8]);
    const v4u of = guard4(v4u{cvtpk(o[0] * inv, o[1] * inv), cvtpk(o[2] * inv, o[3] * inv), cvtpk(o[4] * inv, o[5] * inv), cvtpk(o[6] * inv, o[7] * inv)});
    st_sc1(of, sws, ovo, so_o);
  }
  STAMP(5);

  auto dma_w1 = [&](int i) {
    const int s = (i + 4 * d2) & 15;
    if (w < 6) dma16(L_W1 + s * 6144 + w * 1024, w1vo, sw1, w1so + s * (32 * 2752));
  };
  v4u hres;
  {
    asm volatile("s_waitcnt vmcnt(1)" : "+v"(flb), "+v"(fle), "+v"(xres), "+v"(b2v), "+v"(b1a), "+v"(b1b)::"memory");
    wait_flag(flb, fbo, sws, tag, spins);
    wait_flag(fle, feo, sws, tag, spins);
    CSTAMP(22);
    bar();
    pace<D_O, 0>();
    v4u ofr[2][4];
    ld4(ofr[0], l16, sws, oso);
    ld4(ofr[1], l16, sws, oso + 4096);
    STAMP(6);
    f32x4 acc[2][4];
#pragma unroll
    for (int m = 0; m < 2; ++m)
#pragma unroll
      for (int n = 0; n < 4; ++n) acc[m][n] = f32x4{0.f, 0.f, 0.f, 0.f};
#pragma unroll
    for (int k = 0; k < 4; ++k) {
      if (k == 0) take4<4>(ofr[0], l16, sws, oso, spins);
      if (k == 2) take4<0>(ofr[1], l16, sws, oso + 4096, spins);
#pragma unroll
      for (int m = 0; m < 2; ++m)
#pragma unroll
        for (int n = 0; n < 4; ++n) acc[m][n] = mfma16(wf[k][n], asfrag(ofr[k >> 1][2 * (k & 1) + m]), acc[m][n]);
      pin();
    }
#pragma unroll
    for (int i = 0; i < W1_A; ++i) dma_w1(i);
    STAMP(7);
#pragma unroll
    for (int m = 0; m < 2; ++m)
#pragma unroll
      for (int n = 0; n < 4; ++n) lds_wf4(pcw[n] + m * 4096, acc[m][n]);
#pragma unroll
    for (int i = W1_A; i < W1_B; ++i) dma_w1(i);
    bar();
    CSTAMP(23);
    f32x4 t[8];
#pragma unroll
    for (int k = 0; k < 4; ++k) { t[2 * k] = lds_rf4(pcr + k * 16384); t[2 * k + 1] = lds_rf4((pcr ^ 16) + k * 16384); }
    pin();
    hres = pack44(unlo(xres) + ((t[0] + t[2]) + (t[4] + t[6])), unhi(xres) + ((t[1] + t[3]) + (t[5] + t[7])));
  }
  {
    float hv[8] = {bflo(hres.x), bfhi(hres.x), bflo(hres.y), bfhi(hres.y), bflo(hres.z), bfhi(hres.z), bflo(hres.w), bfhi(hres.w)};
    const float pm_ = sum8(((hv[0] + hv[1]) + (hv[2] + hv[3])) + ((hv[4] + hv[5]) + (hv[6] + hv[7]))) * (1.f / 64.f);
    float q0 = 0.f, q1 = 0.f;
#pragma unroll
    for (int i = 0; i < 8; i += 2) { q0 = fmaf(hv[i] - pm_, hv[i] - pm_, q0); q1 = fmaf(hv[i + 1] - pm_, hv[i + 1] - pm_, q1); }
    const float q = sum8(q0 + q1);
    const v2u stv = {guard(__float_as_uint(pm_)), guard(__float_as_uint(q))};
    asm volatile("buffer_store_dwordx2 %0, %1, %2, %3 offen sc1" ::"v"(stv), "v"(c8 == 0 ? (8 * w + l8) * 8 : OOB), "s"(sws), "s"(so_st + cg * 512) : "memory");
    STAMP(8);
    if (wg == 0 && tid == 0) __builtin_amdgcn_raw_buffer_store_b32(tag, rws, WS_EPOCH, 0, 16);
#pragma unroll
    for (int i = W1_B; i < W1_C; ++i) dma_w1(i);
    u32 sto = c8 * 512 + (8 * w + l8) * 8;
    u32 hw = L_PC + w * 2048 + l8 * 128 + ((c8 ^ (((l8 >> 1) & 3) << 1)) * 16);
    const int row = 4 * g4 + q4;
    u32 hr = L_PC + (4 * (w >> 2) + (row >> 3)) * 2048 + (row & 7) * 128 + (((2 * (w & 3) + (p4 >> 1)) ^ (((row >> 1) & 3) << 1)) * 16) + 8 * (p4 & 1);
    asm volatile("" : "+v"(sto), "+v"(hw), "+v"(hr));
    if (D_ST < 0 && w >= 6) vmwait<0>();
    pace<D_ST, W1_C - W1_B>();
    v2u st;
    asm volatile("buffer_load_dwordx2 %0, %1, %2, %3 offen sc1" : "=v"(st) : "v"(sto), "s"(sws), "s"(so_st) : "memory");
#pragma unroll
    for (int i = W1_C; i < 16; ++i) dma_w1(i);
    if (w >= 6) vmwait<0>();
    asm volatile("s_waitcnt vmcnt(%1)" : "+v"(st) : "n"(16 - W1_C) : "memory");
    if (__builtin_expect(any_sent(st.x > st.y ? st.x : st.y), 0)) {
#pragma clang loop unroll(disable)
      do {
        asm volatile("buffer_load_dwordx2 %0, %1, %2, %3 offen sc1\n\ts_waitcnt vmcnt(0)" : "+v"(st) : "v"(sto), "s"(sws), "s"(so_st) : "memory");
      } while (any_sent(st.x > st.y ? st.x : st.y) && ++spins < SPIN_CAP);
    }
    STAMP(9);
    const u32 sx_ = st.x, sy_ = st.y;
    const float mc = __uint_as_float(sx_), mean = sum8(mc) * 0.125f;
    const float var = sum8(fmaf(64.f * (mc - mean), mc - mean, __uint_as_float(sy_))) * (1.f / 512.f);
    const float r1 = __builtin_amdgcn_rsqf(var + 1e-5f);
    const float r2 = __builtin_amdgcn_rsqf(var * r1 * r1 + 1e-5f);
    const float sc = r1 * r2;
    const v4u hn = {cvtpk((hv[0] - mean) * sc, (hv[1] - mean) * sc), cvtpk((hv[2] - mean) * sc, (hv[3] - mean) * sc),
                    cvtpk((hv[4] - mean) * sc, (hv[5] - mean) * sc), cvtpk((hv[6] - mean) * sc, (hv[7] - mean) * sc)};
    lds_w128(hw, hn);
    bar();
    const v4u fr = guard4(__builtin_bit_cast(v4u, lds_tr(hr, hr + 2 * 2048)));
    st_sc1(fr, sws, l16, so_hn + ((w >> 1) & 1) * 131072 + (w >> 2) * 2048 + (w & 1) * 1024);
    STAMP(10);
  }
  const v4i sw2 = mksrd(gw2, 1376 * 2048 * 2);
  int lnd = lane;
  asm volatile("" : "+v"(lnd));
  const u32 w2vo = (ABL & 16) ? OOB : (lnd >> 3) * 4096 + (((lnd & 7) ^ (((lnd >> 4) & 3) << 1)) * 16) + tt * 128;
  auto issue_w2 = [&](int i) {
    dma4<1024, 1024, 1024, 32768, 32768, 32768>((i == 5 ? 2 : (i + 2) % 5) * 32768 + w * 4096, w2vo, sw2, (w + 8 * i) * 131072);
  };
  if (don) {
    const int dq = w >> 1, kh = w & 1;
    f32x4 acc[6][2];
#pragma unroll
    for (int b = 0; b < 6; ++b)
#pragma unroll
      for (int m = 0; m < 2; ++m) acc[b][m] = f32x4{0.f, 0.f, 0.f, 0.f};
    const u32 so = WS_HN + (2 * d2 + (dq >> 1)) * 262144 + (dq & 1) * 131072 + (16 * kc + 8 * kh) * 2048;
    const int r4 = (loc >> 2) & 3;
    u32 w1r[2], w1o[4];
#pragma unroll
    for (int e = 0; e < 2; ++e) w1r[e] = L_W1 + kh * 49152 + (lnd >> 2) * 192 + (((2 * e + ((lnd >> 1) & 1)) ^ (((lnd >> 4) & 1) << 1)) * 16) + 8 * (lnd & 1);
#pragma unroll
    for (int i = 0; i < 4; ++i) w1o[i] = ((i + r4) & 3);
    v4u hf[4][4];
    vmwait<1>();
    bar();
    pace<D_HN, 0>();
    ld4(hf[0], l16, sws, so + w1o[0] * 4096);
    ld4(hf[1], l16, sws, so + w1o[1] * 4096);
    CSTAMP(25);
    bf16x8 wc[6], wn[6];
    auto rd_w1 = [&](bf16x8 (&f)[6], int s) {
      const u32 o = w1o[s >> 1] * 12288 + (s & 1) * 6144;
#pragma unroll
      for (int b = 0; b < 6; ++b) { const u32 ad = w1r[b & 1] + o + (b >> 1) * 64; f[b] = lds_tr(ad, ad + 16 * 192); }
    };
    v4u w2r[4][4];
    auto raw_w2 = [&](int i) {
#pragma unroll
      for (int u = 0; u < 4; ++u) w2r[i][u] = aload(w2vo, sw2, (w + 8 * i) * 131072 + u * 32768);
    };
    const int sj = (s6 >> 1) & 3, sm_ = s6 & 1;
    u32 xvo = (dq != kc && (ft < 14 || s6 < 2)) ? l16 : OOB;
    u32 ua0 = L_PD + 49152 + sm_ * 6144 + (sj < 3 ? sj : 0) * 2048 + l16, ua1 = L_PD + (2 * kc + 1 - sm_) * 6144 + (sj < 3 ? sj : 0) * 2048 + l16;
    u32 w2w = 65536 + w * 4096 + l16;
    asm volatile("" : "+v"(xvo), "+v"(ua0), "+v"(ua1), "+v"(w2w));
    u32 xso = so_x + kc * 18432 + (dq != kc ? s6 : 0) * 3072, xst = so_x + dq * 18432 + kh * 3072 + ((kc - dq - 1) & 3) * 1024;
    u32 gso = so_g + (dq != kc ? sj * 4096 + sm_ * 1024 : 0);
    asm volatile("" : "+s"(xso), "+s"(xst), "+s"(gso));
    rd_w1(wc, 0);
#pragma unroll
    for (int s = 0; s < 8; ++s) {
      if (s == 0) { take4<4>(hf[0], l16, sws, so + w1o[0] * 4096, spins); ld4(hf[2], l16, sws, so + w1o[2] * 4096); }
      if (s == 2) { take4<4>(hf[1], l16, sws, so + w1o[1] * 4096, spins); ld4(hf[3], l16, sws, so + w1o[3] * 4096); }
      if (s == 4) { take4<4>(hf[2], l16, sws, so + w1o[2] * 4096, spins); raw_w2(0); }
      if (s == 6) { take4<4>(hf[3], l16, sws, so + w1o[3] * 4096, spins); raw_w2(1); }
      if (s == 7) raw_w2(2);
      const u32 o = w1o[(s + 1) >> 1 & 3] * 12288 + ((s + 1) & 1) * 6144;
      const u32 a0 = w1r[0] + o, a1 = w1r[1] + o;
#pragma unroll
      for (int b = 0; b < 6; ++b) {
#pragma unroll
        for (int m = 0; m < 2; ++m) acc[b][m] = mfma16(wc[b], asfrag(hf[s >> 1][2 * (s & 1) + m]), acc[b][m]);
        if (s < 7) { const u32 ad = ((b & 1) ? a1 : a0) + (b >> 1) * 64; wn[b] = lds_tr(ad, ad + 16 * 192); }
        pin();
      }
#pragma unroll
      for (int b = 0; b < 6; ++b) wc[b] = wn[b];
      CSTAMP(26 + s);
    }
    STAMP(11);
    const u32 pd = L_PD + w * 6144 + l16;
    if (kh) {
#pragma unroll
      for (int b = 0; b < 6; ++b) { lds_wf4(pd + b * 1024, acc[b][0]); acc[b][0] = acc[b][1]; }
    } else {
#pragma unroll
      for (int b = 0; b < 6; ++b) lds_wf4(pd + b * 1024, acc[b][1]);
    }
    bar();
    CSTAMP(34);
    if (dq == kc) {
#pragma unroll
      for (int b = 0; b < 6; ++b) lds_wf4(L_PD + 49152 + kh * 6144 + l16 + b * 1024, acc[b][0]);
    } else {
      f32x4 t[6];
#pragma unroll
      for (int b = 0; b < 6; ++b) t[b] = lds_rf4(L_PD + (w ^ 1) * 6144 + l16 + b * 1024);
      pin();
#pragma unroll
      for (int j = 0; j < 3; ++j)
        st_sc1(guard4(pack44(acc[2 * j][0] + t[2 * j], acc[2 * j + 1][0] + t[2 * j + 1])), sws, (j > 0 && ft == 14) ? OOB : l16, xst + j * 6144);
    }
    CSTAMP(35);
    bar();
    f32x4 u0 = lds_rf4(ua0), u1 = lds_rf4(ua0 + 1024), u2 = lds_rf4(ua1), u3 = lds_rf4(ua1 + 1024);
    pace<D_XP, 0>();
    v4u xp[3];
    ld3(xp, xvo, sws, xso);
    if (dq == kc) vmwait<3>();
    asm volatile("s_waitcnt vmcnt(6)" : "+v"(w2r[0][0]), "+v"(w2r[0][1]), "+v"(w2r[0][2]), "+v"(w2r[0][3]), "+v"(w2r[1][0]), "+v"(w2r[1][1]), "+v"(w2r[1][2]),
                 "+v"(w2r[1][3]), "+v"(w2r[2][0]), "+v"(w2r[2][1]), "+v"(w2r[2][2]), "+v"(w2r[2][3])::"memory");
#pragma unroll
    for (int i = 0; i < 3; ++i)
#pragma unroll
      for (int u = 0; u < 4; ++u) lds_w128(w2w + (i >> 1) * 65536 + (i & 1) * 32768 + u * 1024, w2r[i][u]);
    raw_w2(3);
    u0 += u2 + f32x4{bflo(b1a.x), bfhi(b1a.x), bflo(b1a.y), bfhi(b1a.y)};
    u1 += u3 + f32x4{bflo(b1b.x), bfhi(b1b.x), bflo(b1b.y), bfhi(b1b.y)};
    asm volatile("" : "+v"(u0), "+v"(u1));
    take3<4>(xp, xvo, sws, xso, spins);
    CSTAMP(38);
    u0 += (unlo(xp[0]) + unlo(xp[1])) + unlo(xp[2]);
    u1 += (unhi(xp[0]) + unhi(xp[1])) + unhi(xp[2]);
    const v4u gv = guard4(v4u{cvtpk(gelu_tanh(u0.x), gelu_tanh(u0.y)), cvtpk(gelu_tanh(u0.z), gelu_tanh(u0.w)),
                              cvtpk(gelu_tanh(u1.x), gelu_tanh(u1.y)), cvtpk(gelu_tanh(u1.z), gelu_tanh(u1.w))});
    st_sc1(gv, sws, xvo, gso);
    STAMP(12);
    bar();
    issue_w2(4);
    asm volatile("s_waitcnt vmcnt(5)" : "+v"(w2r[3][0]), "+v"(w2r[3][1]), "+v"(w2r[3][2]), "+v"(w2r[3][3])::"memory");
#pragma unroll
    for (int u = 0; u < 4; ++u) lds_w128(w2w - 65536 + u * 1024, w2r[3][u]);
  } else {
    vmwait<0>();
    bar();
    const u32 p = (xcd - 4) * 4 + (loc & 3);
#pragma clang loop unroll(disable)
    for (int i = 0; i < W2PF; ++i) {
      dma16(w * 4096, l16, sw2, (i * 16 + p) * 8192 + w * 1024);
      vmwait<6>();
    }
    vmwait<0>();
    issue_w2(0); issue_w2(1); issue_w2(2); issue_w2(3); issue_w2(4);
    STAMP(12);
  }
  {
    f32x4 acc[4][4];
#pragma unroll
    for (int d = 0; d < 4; ++d)
#pragma unroll
      for (int n = 0; n < 4; ++n) acc[d][n] = f32x4{0.f, 0.f, 0.f, 0.f};
    const u32 so = WS_G + (cg * 43 + w) * 4096;
    u32 wr[4], wr2[4], pew[4];
    int ln = lane;
    asm volatile("" : "+v"(ln));
#pragma unroll
    for (int n = 0; n < 4; ++n) { wr[n] = tra[n] + w * 4096; wr2[n] = wr[n] + 65536; }
#pragma unroll
    for (int d = 0; d < 4; ++d) pew[d] = w * 4096 + (ln & 15) * 256 + (((4 * d + (ln >> 4)) ^ (ln & 15)) * 16);
    const int tok = 8 * w + (ln >> 3);
    u32 per = (w >> 1) * 32768 + (tok & 15) * 256 + (((2 * (ln & 7)) ^ (tok & 15)) * 16);
    u32 outo = xro;
    asm volatile("" : "+v"(wr[0]), "+v"(wr[1]), "+v"(wr[2]), "+v"(wr[3]), "+v"(wr2[0]), "+v"(wr2[1]), "+v"(wr2[2]), "+v"(wr2[3]),
                 "+v"(pew[0]), "+v"(pew[1]), "+v"(pew[2]), "+v"(pew[3]), "+v"(per), "+v"(outo));
    auto take_w2 = [&](int i, bf16x8 (&f)[4]) {
      const int sl = i == 5 ? 2 : (i + 2) % 5;
#pragma unroll
      for (int n = 0; n < 4; ++n) {
        const u32 ad = sl < 2 ? wr[n] + sl * 32768 : sl < 4 ? wr2[n] + (sl - 2) * 32768 : wr2[n] + 65536;
        f[n] = lds_tr(ad, ad + 2048);
      }
    };
    v4u gf[3][4];
    bf16x8 wc[4];
    pace<D_G, 4>();
    CSTAMP(36);
    ld4(gf[0], l16, sws, so);
    ld4(gf[1], l16, sws, so + 32768);
    CSTAMP(37);
#pragma unroll
    for (int j = 0; j < 6; ++j) {
      if (j < 5 || w < 3) {
        if (w >= 3) { if (j == 2 || j == 3) vmwait<4>(); else if (j == 4) vmwait<0>(); }
        if (j == 2) take4<8>(gf[j % 3], l16, sws, so + j * 32768, spins);
        else if (j == 3) take4<8>(gf[j % 3], l16, sws, so + j * 32768, spins);
        else if (j < 5) take4<4>(gf[j % 3], l16, sws, so + j * 32768, spins);
        else take4<0>(gf[j % 3], l16, sws, so + j * 32768, spins);
        if (j < 3 || (j == 3 && w < 3)) ld4(gf[(j + 2) % 3], l16, sws, so + (j + 2) * 32768);
        if (j == 0) take_w2(0, wc);
        bf16x8 wn[4];
        const int sl = j == 4 ? 2 : (j + 3) % 5;
#pragma unroll
        for (int n = 0; n < 4; ++n) {
#pragma unroll
          for (int d = 0; d < 4; ++d) acc[d][n] = mfma16(asfrag(gf[j % 3][d]), wc[n], acc[d][n]);
          if (j < 5) {
            const u32 ad = sl < 2 ? wr[n] + sl * 32768 : sl < 4 ? wr2[n] + (sl - 2) * 32768 : wr2[n] + 65536;
            wn[n] = lds_tr(ad, ad + 2048);
          }
          pin();
        }
        if (j == 1 && w < 3) issue_w2(5);
#pragma unroll
        for (int n = 0; n < 4; ++n) wc[n] = wn[n];
      }
      pin();
      CSTAMP(52 + j);
    }
    STAMP(13);
    asm volatile("s_waitcnt lgkmcnt(0)" ::: "memory");
#pragma unroll
    for (int n = 0; n < 2; ++n)
#pragma unroll
      for (int d = 0; d < 4; ++d) lds_wf4(pew[d] + n * 32768, acc[d][n]);
#pragma unroll
    for (int d = 0; d < 4; ++d) { pew[d] += 65536; asm volatile("" : "+v"(pew[d])); }
#pragma unroll
    for (int n = 0; n < 2; ++n)
#pragma unroll
      for (int d = 0; d < 4; ++d) lds_wf4(pew[d] + n * 32768, acc[d][2 + n]);
    const float bb = bflo(b2v);
    f32x4 s0 = unlo(hres) + bb, s1 = unhi(hres) + bb;
    u32 per1 = per ^ 16;
    asm volatile("" : "+v"(s0), "+v"(s1), "+v"(per1));
    bar();
    CSTAMP(58);
    f32x4 t[16];
#pragma unroll
    for (int k = 0; k < 8; ++k) { t[2 * k] = lds_rf4(per + k * 4096); t[2 * k + 1] = lds_rf4(per1 + k * 4096); }
    pin();
    s0 += ((t[0] + t[2]) + (t[4] + t[6])) + ((t[8] + t[10]) + (t[12] + t[14]));
    s1 += ((t[1] + t[3]) + (t[5] + t[7])) + ((t[9] + t[11]) + (t[13] + t[15]));
    const v4u ov = pack44(s0, s1);
    asm volatile(".Lmega_pf_end:\n\tbuffer_store_dwordx4 %0, %1, %2, 0 offen" ::"v"(ov), "v"(outo), "s"(mksrd(gout, 2048 * 512 * 2)) : "memory");
  }
  STAMP(14);
}

extern "C" void kernel_launch(void* const* d_in, const int* in_sizes, int n_in, void* d_out, int out_size, void* d_ws, size_t ws_size,
                              hipStream_t stream) {
  (void)hipFuncSetAttribute((const void*)mega, hipFuncAttributeMaxDynamicSharedMemorySize, LDS_BYTES);
  mega<<<256, 512, LDS_BYTES, stream>>>((const u16*)d_in[0], (const u16*)d_in[1], (const u16*)d_in[2], (const u16*)d_in[4], (const u16*)d_in[5],
                                        (const u16*)d_in[6], (const u16*)d_in[7], (u16*)d_out, (char*)d_ws);
}
```

```cpp
#include <hip/hip_runtime.h>
#include <stdint.h>

typedef unsigned u32;
typedef unsigned short u16;
typedef u32 v4u __attribute__((ext_vector_type(4)));
typedef u32 v2u __attribute__((ext_vector_type(2)));
typedef int v4i __attribute__((ext_vector_type(4)));
typedef float f32x2 __attribute__((ext_vector_type(2)));
typedef float f32x4 __attribute__((ext_vector_type(4)));
typedef float f32x16 __attribute__((ext_vector_type(16)));
typedef short bf16x8 __attribute__((ext_vector_type(8)));
typedef short s16x4 __attribute__((ext_vector_type(4)));
typedef __bf16 bf2 __attribute__((ext_vector_type(2)));
#define LAS __attribute__((address_space(3)))
#define DI __device__ __forceinline__

#define ABL 0
#define CODE_PF 1
#define STAMPS 0
#define D_KV 0
#define CLR_POS 2
#define HK_AT 3
#define HV_AT 6
#define W1_0 3
#define W1_A 3
#define W1_B 6
#define W1_C 8
#define W1_D 10
#define R3 1
#define XP_AT 3
#define W2PF 19
#define EROT 1
#define D_O 4
#define D_ST -1
#define D_HN -1
#define D_XP -1
#define D_G -1
#define BG_TABLE 0, 0, 0,   1, 1, 2, 3,   4, 5, 6, 7, 8, 9, 10, 11, 11
#define BG_SIG 4
#define BG_BAR 10
#define BG_FLAG 13
constexpr int NBG = 16;
constexpr int BG_AT[NBG] = {BG_TABLE};
constexpr int bg_after(int op0, int pt) { int n = 0; for (int op = op0 + 1; op < NBG; ++op) n += BG_AT[op] <= pt ? (op >= 3 && op < 7 ? 4 : 1) : 0; return n; }
constexpr bool bg_ok() { for (int op = 1; op < NBG; ++op) if (BG_AT[op] < BG_AT[op - 1]) return false; return BG_AT[2] <= BG_SIG && BG_AT[6] <= BG_BAR && BG_AT[3] > 0; }
static_assert(bg_ok(), "the table lists the transfers in issue order");
#define SETUP0 4
template <bool B> struct BoolC { static constexpr bool value = B; };

constexpr u32 WS_EPOCH = 0, WS_FLAGA = 1u << 19, WS_FLAGB = WS_FLAGA + 32768, WS_FLAGC = WS_FLAGB + 32768, WS_DBG = 65536;
constexpr u32 WS_KV = 1u << 20;
constexpr u32 WS_O = WS_KV + (4u << 20);
constexpr u32 WS_HN = WS_O + (2u << 20);
constexpr u32 WS_G = WS_HN + (2u << 20);
constexpr u32 WS_ST = WS_G + (2u << 20);
constexpr u32 WS_XP = WS_ST + (1u << 17);
constexpr u32 WS_END = WS_XP + 240 * 18432;
constexpr u32 SENT = 0xFFFFFFFFu;
constexpr u32 OOB = 0x7FFF0000u;
constexpr int SPIN_CAP = 1 << 16;

constexpr u32 L_XN = 0;
constexpr u32 KVT = 8448;
constexpr u32 L_CNT = 85504;
constexpr u32 L_RING = 86016;
constexpr u32 wslot(int st) { return st < 4 ? L_RING + st * 16384 : st == 4 ? 65536 : L_RING + ((st - 5) & 3) * 16384; }
constexpr u32 L_JUNK = L_RING + 65536;
constexpr u32 L_WO = 98304;
constexpr u32 L_PC = 0;
constexpr u32 L_W1 = 65536;
constexpr u32 L_PD = 0;
constexpr u32 L_W2A = 65536;
constexpr u32 LDS_BYTES = 163840;

DI u32 cvtpk(float a, float b) { f32x2 v = {a, b}; bf2 r = __builtin_convertvector(v, bf2); return __builtin_bit_cast(u32, r); }
DI float bflo(u32 x) { return __uint_as_float(x << 16); }
DI float bfhi(u32 x) { return __uint_as_float(x & 0xffff0000u); }
DI u32 guard(u32 x) { return x < 0xFFFFFFFEu ? x : 0xFFFFFFFEu; }
DI v4u guard4(v4u v) { v4u r = {guard(v.x), guard(v.y), guard(v.z), guard(v.w)}; return r; }
DI u32 umax3(u32 a, u32 b, u32 c) { u32 m = a > b ? a : b; return m > c ? m : c; }
DI u32 smax4(u32 m, v4u v) { return umax3(umax3(m, v.x, v.y), v.z, v.w); }
DI u32 vmax(v4u v) { return umax3(v.x > v.y ? v.x : v.y, v.z, v.w); }
DI u32 chk4(const v4u (&f)[4]) { return umax3(smax4(vmax(f[0]), f[1]), vmax(f[2]), vmax(f[3])); }
DI bool any_sent(u32 m) { return __builtin_amdgcn_ballot_w64(m == SENT) != 0; }
DI bf16x8 asfrag(v4u v) { return __builtin_bit_cast(bf16x8, v); }
DI f32x16 mfma32(bf16x8 a, bf16x8 b, f32x16 c) { return __builtin_amdgcn_mfma_f32_32x32x16_bf16(a, b, c, 0, 0, 0); }
DI f32x4 mfma16(bf16x8 a, bf16x8 b, f32x4 c) { return __builtin_amdgcn_mfma_f32_16x16x32_bf16(a, b, c, 0, 0, 0); }
DI v4u lds_r128(u32 a) { return *(const LAS v4u*)(uintptr_t)a; }
DI f32x4 lds_rf4(u32 a) { return *(const LAS f32x4*)(uintptr_t)a; }
DI void lds_w128(u32 a, v4u v) { *(LAS v4u*)(uintptr_t)a = v; }
DI void lds_wf4(u32 a, f32x4 v) { *(LAS f32x4*)(uintptr_t)a = v; }
DI bf16x8 lds_tr(u32 a0, u32 a1) {
  s16x4 lo = __builtin_amdgcn_ds_read_tr16_b64_v4i16((LAS s16x4*)(uintptr_t)a0);
  s16x4 hi = __builtin_amdgcn_ds_read_tr16_b64_v4i16((LAS s16x4*)(uintptr_t)a1);
  return __builtin_shufflevector(lo, hi, 0, 1, 2, 3, 4, 5, 6, 7);
}
template <int CTRL> DI float dpp(float v) { return __int_as_float(__builtin_amdgcn_update_dpp(0, __float_as_int(v), CTRL, 0xf, 0xf, true)); }
DI float sum8(float v) { v += dpp<0xB1>(v); v += dpp<0x4E>(v); v += dpp<0x141>(v); return v; }
DI v4i mksrd(const void* p, u32 bytes) { v4i s = {(int)(u32)(uintptr_t)p, (int)(u32)((uintptr_t)p >> 32), (int)bytes, 0x00020000}; return s; }
DI __amdgpu_buffer_rsrc_t mkrs(const void* p, u32 bytes) { return __builtin_amdgcn_make_buffer_rsrc((void*)p, 0, bytes, 0x00020000); }
DI void dma16(u32 lds, u32 voff, v4i srd, u32 soff) {
  asm volatile("s_mov_b32 m0, %0\n\ts_nop 0\n\tbuffer_load_dwordx4 %1, %2, %3 offen lds" ::"s"(lds), "v"(voff), "s"(srd), "s"(soff) : "memory");
}
DI v4u aload_sc1(u32 voff, v4i srd, u32 soff) {
  v4u r;
  asm volatile("buffer_load_dwordx4 %0, %1, %2, %3 offen sc1" : "=v"(r) : "v"(voff), "s"(srd), "s"(soff) : "memory");
  return r;
}
DI v4u aload(u32 voff, v4i srd, u32 soff) {
  v4u r;
  asm volatile("buffer_load_dwordx4 %0, %1, %2, %3 offen" : "=v"(r) : "v"(voff), "s"(srd), "s"(soff) : "memory");
  return r;
}
DI void ld4(v4u (&f)[4], u32 voff, v4i srd, u32 soff) {
  asm volatile("buffer_load_dwordx4 %0, %4, %5, %6 offen sc1\n\tbuffer_load_dwordx4 %1, %4, %5, %6 offen offset:1024 sc1\n\t"
               "buffer_load_dwordx4 %2, %4, %5, %6 offen offset:2048 sc1\n\tbuffer_load_dwordx4 %3, %4, %5, %6 offen offset:3072 sc1"
               : "=&v"(f[0]), "=&v"(f[1]), "=&v"(f[2]), "=&v"(f[3]) : "v"(voff), "s"(srd), "s"(soff) : "memory");
}
DI void ld4s(v4u (&f)[4], const u32 (&voff)[4], v4i srd, const u32 (&so)[4]) {
  asm volatile("buffer_load_dwordx4 %0, %4, %8, %9 offen sc1\n\tbuffer_load_dwordx4 %1, %5, %8, %10 offen sc1\n\t"
               "buffer_load_dwordx4 %2, %6, %8, %11 offen sc1\n\tbuffer_load_dwordx4 %3, %7, %8, %12 offen sc1"
               : "=&v"(f[0]), "=&v"(f[1]), "=&v"(f[2]), "=&v"(f[3])
               : "v"(voff[0]), "v"(voff[1]), "v"(voff[2]), "v"(voff[3]), "s"(srd), "s"(so[0]), "s"(so[1]), "s"(so[2]), "s"(so[3]) : "memory");
}
template <int N> DI void take4s(v4u (&f)[4], const u32 (&voff)[4], v4i srd, const u32 (&so)[4], int& spins) {
  asm volatile("s_waitcnt vmcnt(%4)" : "+v"(f[0]), "+v"(f[1]), "+v"(f[2]), "+v"(f[3]) : "n"(N) : "memory");
  if (__builtin_expect(any_sent(chk4(f)), 0)) {
#pragma clang loop unroll(disable)
    do {
      asm volatile("s_sleep 1\n\tbuffer_load_dwordx4 %0, %4, %8, %9 offen sc1\n\tbuffer_load_dwordx4 %1, %5, %8, %10 offen sc1\n\t"
                   "buffer_load_dwordx4 %2, %6, %8, %11 offen sc1\n\tbuffer_load_dwordx4 %3, %7, %8, %12 offen sc1\n\ts_waitcnt vmcnt(0)"
                   : "+v"(f[0]), "+v"(f[1]), "+v"(f[2]), "+v"(f[3])
                   : "v"(voff[0]), "v"(voff[1]), "v"(voff[2]), "v"(voff[3]), "s"(srd), "s"(so[0]), "s"(so[1]), "s"(so[2]), "s"(so[3]) : "memory");
    } while (any_sent(chk4(f)) && ++spins < SPIN_CAP);
  }
}
template <int N> DI void take4(v4u (&f)[4], u32 voff, v4i srd, u32 soff, int& spins) {
  asm volatile("s_waitcnt vmcnt(%4)" : "+v"(f[0]), "+v"(f[1]), "+v"(f[2]), "+v"(f[3]) : "n"(N) : "memory");
  if (__builtin_expect(any_sent(chk4(f)), 0)) {
#pragma clang loop unroll(disable)
    do {
      asm volatile("s_sleep 1\n\tbuffer_load_dwordx4 %0, %4, %5, %6 offen sc1\n\tbuffer_load_dwordx4 %1, %4, %5, %6 offen offset:1024 sc1\n\t"
                   "buffer_load_dwordx4 %2, %4, %5, %6 offen offset:2048 sc1\n\tbuffer_load_dwordx4 %3, %4, %5, %6 offen offset:3072 sc1\n\t"
                   "s_waitcnt vmcnt(0)"
                   : "+v"(f[0]), "+v"(f[1]), "+v"(f[2]), "+v"(f[3]) : "v"(voff), "s"(srd), "s"(soff) : "memory");
    } while (any_sent(chk4(f)) && ++spins < SPIN_CAP);
  }
}
template <int M1, int M2, int M3, int S1, int S2, int S3>
DI void dma4(u32 lds, u32 voff, v4i srd, u32 soff) {
  u32 t;
  soff = __builtin_amdgcn_readfirstlane(soff);
  lds = __builtin_amdgcn_readfirstlane(lds);
  asm volatile("s_mov_b32 m0, %1\n\ts_mov_b32 %0, %4\n\tbuffer_load_dwordx4 %2, %3, %0 offen lds\n\t"
               "s_add_i32 m0, m0, %5\n\ts_add_i32 %0, %0, %8\n\tbuffer_load_dwordx4 %2, %3, %0 offen lds\n\t"
               "s_add_i32 m0, m0, %6\n\ts_add_i32 %0, %0, %9\n\tbuffer_load_dwordx4 %2, %3, %0 offen lds\n\t"
               "s_add_i32 m0, m0, %7\n\ts_add_i32 %0, %0, %10\n\tbuffer_load_dwordx4 %2, %3, %0 offen lds"
               : "=&s"(t) : "s"(lds), "v"(voff), "s"(srd), "s"(soff), "n"(M1), "n"(M2), "n"(M3), "n"(S1), "n"(S2), "n"(S3) : "memory", "scc");
}
template <int N> DI void vmwait() { asm volatile("s_waitcnt vmcnt(%0)" ::"n"(N) : "memory"); }
DI void bar() { asm volatile("s_waitcnt lgkmcnt(0)\n\ts_barrier" ::: "memory"); }
DI v4u ld_sc1(__amdgpu_buffer_rsrc_t rs, u32 voff, u32 soff) { return __builtin_amdgcn_raw_buffer_load_b128(rs, voff, soff, 16); }
DI void st_sc1(v4u v, v4i srd, u32 voff, u32 soff) {
  asm volatile("buffer_store_dwordx4 %0, %1, %2, %3 offen sc1\n\ts_nop 1" ::"v"(v), "v"(voff), "s"(srd), "s"(soff) : "memory");
}
template <int D, int YOUNGER> DI void pace() { if (D < 0) vmwait<YOUNGER>(); else if (D > 0) __builtin_amdgcn_s_sleep(D < 0 ? 0 : D); }
DI void pin() { __builtin_amdgcn_sched_barrier(0); }

#define STAMP(i) do {} while (0)
#define CSTAMP(i) do {} while (0)

DI void max16(float (&m)[4], const f32x16& s) {
#pragma unroll
  for (int j = 0; j < 8; ++j) m[j & 3] = fmaxf(fmaxf(m[j & 3], s[2 * j]), s[2 * j + 1]);
}
DI v4u pack8(const float* p) { return v4u{cvtpk(p[0], p[1]), cvtpk(p[2], p[3]), cvtpk(p[4], p[5]), cvtpk(p[6], p[7])}; }
DI v4u pack44(f32x4 a, f32x4 b) { return v4u{cvtpk(a.x, a.y), cvtpk(a.z, a.w), cvtpk(b.x, b.y), cvtpk(b.z, b.w)}; }
DI f32x4 unlo(v4u v) { return f32x4{bflo(v.x), bfhi(v.x), bflo(v.y), bfhi(v.y)}; }
DI f32x4 unhi(v4u v) { return f32x4{bflo(v.z), bfhi(v.z), bflo(v.w), bfhi(v.w)}; }
DI float gelu_tanh(float x) {
  const float c1 = -2.0f * 0.7978845608028654f * 1.4426950408889634f, c2 = c1 * 0.044715f;
  const float e = __builtin_amdgcn_exp2f(x * (c1 + c2 * x * x));
  return x * __builtin_amdgcn_rcpf(1.0f + e);
}
struct Frag4 { bf16x8 f[4]; };
template <bool XFIRST, bool RDX>
DI void gemm4(f32x16& acc, const Frag4& xc, const Frag4& wc, Frag4& xn, Frag4& wn, const u32 (&xa)[4], u32 xo, u32 wo) {
#pragma unroll
  for (int s = 0; s < 4; ++s) {
    acc = XFIRST ? mfma32(xc.f[s], wc.f[s], acc) : mfma32(wc.f[s], xc.f[s], acc);
    if (RDX) xn.f[s] = asfrag(lds_r128(xa[s] + xo));
    wn.f[s] = lds_tr(wo + s * 2048, wo + s * 2048 + 512);
    pin();
  }
}
DI void ld3(v4u (&f)[3], u32 voff, v4i srd, u32 soff) {
  asm volatile("buffer_load_dwordx4 %0, %3, %4, %5 offen sc1\n\tbuffer_load_dwordx4 %1, %3, %4, %5 offen offset:1024 sc1\n\t"
               "buffer_load_dwordx4 %2, %3, %4, %5 offen offset:2048 sc1"
               : "=&v"(f[0]), "=&v"(f[1]), "=&v"(f[2]) : "v"(voff), "s"(srd), "s"(soff) : "memory");
}
template <int N> DI void take3(v4u (&f)[3], u32 voff, v4i srd, u32 soff, int& spins) {
  asm volatile("s_waitcnt vmcnt(%3)" : "+v"(f[0]), "+v"(f[1]), "+v"(f[2]) : "n"(N) : "memory");
  if (__builtin_expect(any_sent(umax3(vmax(f[0]), vmax(f[1]), vmax(f[2]))), 0)) {
#pragma clang loop unroll(disable)
    do {
      asm volatile("s_sleep 1\n\tbuffer_load_dwordx4 %0, %3, %4, %5 offen sc1\n\tbuffer_load_dwordx4 %1, %3, %4, %5 offen offset:1024 sc1\n\t"
                   "buffer_load_dwordx4 %2, %3, %4, %5 offen offset:2048 sc1\n\ts_waitcnt vmcnt(0)"
                   : "+v"(f[0]), "+v"(f[1]), "+v"(f[2]) : "v"(voff), "s"(srd), "s"(soff) : "memory");
    } while (any_sent(umax3(vmax(f[0]), vmax(f[1]), vmax(f[2]))) && ++spins < SPIN_CAP);
  }
}
template <int N> DI void signal(u32 cnt, int lane, __amdgpu_buffer_rsrc_t rws, u32 flag, u32 tag) {
  u32 old = 0;
  if (lane == 0) old = __hip_atomic_fetch_add((LAS u32*)(uintptr_t)cnt, 1u, __ATOMIC_RELAXED, __HIP_MEMORY_SCOPE_WORKGROUP);
  if (__builtin_amdgcn_readfirstlane(old) == N - 1 && lane == 0) __builtin_amdgcn_raw_buffer_store_b32(tag, rws, flag, 0, 16);
}
DI int wg_of(int tt, int cg) { return ((tt & 7) + 8 * (cg & 3)) * 8 + 2 * (tt >> 3) + (cg >> 2); }
DI int wg_ofd(int d2, int ft, int kc) { return (d2 + 4 * (ft & 7)) * 8 + 4 * (ft >> 3) + kc; }
DI u32 ld_flag(u32 off, v4i sws) {
  u32 f;
  asm volatile("buffer_load_dword %0, %1, %2, 0 offen sc1" : "=v"(f) : "v"(off), "s"(sws) : "memory");
  return f;
}
DI void wait_flag(u32& f, u32 off, v4i sws, u32 tag, int& spins) {
  if (__builtin_expect(__builtin_amdgcn_ballot_w64(off != OOB && f != tag) != 0, 0)) {
#pragma clang loop unroll(disable)
    do {
      asm volatile("s_sleep 2\n\tbuffer_load_dword %0, %1, %2, 0 offen sc1\n\ts_waitcnt vmcnt(0)" : "+v"(f) : "v"(off), "s"(sws) : "memory");
    } while (__builtin_amdgcn_ballot_w64(off != OOB && f != tag) != 0 && ++spins < SPIN_CAP);
  }
}

__global__ void __launch_bounds__(512, 2)
mega(const u16* gx, const u16* gwqkv, const u16* gwo, const u16* gw1, const u16* gb1, const u16* gw2, const u16* gb2, u16* gout, char* ws) {
  asm volatile("" : "+s"(gx), "+s"(gwqkv), "+s"(gwo), "+s"(gw1), "+s"(gb1), "+s"(gw2), "+s"(gb2), "+s"(gout), "+s"(ws));
  const int tid = threadIdx.x, lane = tid & 63;
  const int w = __builtin_amdgcn_readfirstlane(tid >> 6);
  const int wg = blockIdx.x, xcd = wg & 7, loc = wg >> 3;
  const int tt = 8 * (xcd >> 1) + (loc & 7), cg = 4 * (xcd & 1) + (loc >> 3);
  const int kc = xcd & 3, d2 = loc & 3, ft = 8 * (xcd >> 2) + (loc >> 2);
  const bool don = ft < 15;
  const int rot = ((loc & 7) + 2 * (loc >> 3)) & 7;
  STAMP(0);
  const u32 l16 = lane * 16;
  const int l8 = lane >> 3, c8 = lane & 7;
  int spins = 0;

  v4u xr[8];
  const v4i sx = mksrd(gx, 2048 * 512 * 2);
  u32 xro = (64 * tt + 8 * w + l8) * 1024 + c8 * 16;
  auto load_x = [&]() {
#pragma unroll
    for (int i = 0; i < 8; ++i) xr[i] = aload(xro, sx, ((i + rot) & 7) * 128);
  };
  CSTAMP(42);
  if (w < 4) load_x();
  CSTAMP(43);
  const v4i swqkv = mksrd(gwqkv, 512 * 1536 * 2), sw1 = mksrd(gw1, 2048 * 1376 * 2);
  const __amdgpu_buffer_rsrc_t rws = mkrs(ws, WS_END);
  const v4i sws = mksrd(ws, WS_END);
  const int r32 = lane & 31, h = lane >> 5, blk = (lane >> 4) & 1, q4 = (lane & 15) >> 2, p4 = lane & 3, g4 = lane >> 4;
  const int hh = w & 3, tb = w >> 2;
  const int isV = w & 1, hp = (w >> 1) & 1;
  const int s6 = w < 2 * kc ? w : w - 2;
  v4u sent4 = {SENT, SENT, SENT, SENT};
  u32 kvs = WS_KV + (tt * 8 + cg) * 16384;
  u32 wvq = (8 * w + l8) * 3072 + ((c8 ^ (((l8 >> 1) & 1) << 2)) * 16) + cg * 128;
  u32 ko[8];
#pragma unroll
  for (int i = 0; i < 8; ++i) ko[i] = ((i + rot) & 7) * (64 * 3072);
  u32 ring = w * 1024;
  u32 xa0 = L_XN + (32 * tb + r32) * 128 + ((h ^ ((r32 >> 1) & 7)) * 16);
  u32 wa = 65536 + isV * 8192 + (8 * h + q4) * 128 + (((4 * hp + 2 * blk + (p4 >> 1)) ^ (((q4 >> 1) & 1) << 2)) * 16) + 8 * (p4 & 1);
  u32 xw = L_XN + (8 * w + l8) * 128 + ((c8 ^ (((8 * w + l8) >> 1) & 7)) * 16);
  asm volatile("" : "+v"(sent4), "+v"(xa0), "+v"(wa), "+v"(xw), "+v"(wvq));
  asm volatile("" : "+s"(ko[0]), "+s"(ko[1]), "+s"(ko[2]), "+s"(ko[3]), "+s"(ko[4]), "+s"(ko[5]), "+s"(ko[6]), "+s"(ko[7]), "+s"(kvs), "+s"(ring));
  u32 qwa, fao, kvo, kvl, hso, hks[4], vb0, mbits, ovo, swz, fbo, fco, so_o, so_hn, so_st, so_g, so_x, tra0, pcr, pcw0, w1vo, w1so;
  const int hpt = tt - 4 + (w >> 1);
  auto setup = [&](int i) {
    if (i == 0) {
      qwa = L_RING + (8 * h + q4) * 128 + (((2 * hh + (p4 >> 1)) ^ (((q4 >> 1) & 1) << 2)) * 16) + 8 * (p4 & 1);
      kvo = isV ? (2 * hp + (r32 >> 4)) * 1024 + h * 256 + (r32 & 15) * 16 : 2 * hp * 1024 + l16;
      kvl = (8 + tb) * KVT + (isV ? (2 * hp + (r32 >> 4)) * 2048 + 1024 + h * 256 + (r32 & 15) * 16 : 2 * hp * 2048 + l16);
      asm volatile("" : "+v"(qwa), "+v"(kvo), "+v"(kvl));
    } else if (i == 1) {
      fao = (tt - 4 + lane >= 0 && lane < 4) ? WS_FLAGA + wg_of(tt - 4 + lane, cg) * 128 : OOB;
      hso = WS_KV + ((hpt >= 0 ? hpt : 0) * 8 + cg) * 16384 + (w & 1) * 4096 + 8192;
#pragma unroll
      for (int j = 0; j < 4; ++j) hks[j] = WS_KV + cg * 16384 + hh * 1024 + (tt - 4 + j >= 0 ? (tt - 4 + j) * 131072 + tb * 4096 : 0);
      asm volatile("" : "+v"(fao));
      asm volatile("" : "+s"(hso), "+s"(hks[0]), "+s"(hks[1]), "+s"(hks[2]), "+s"(hks[3]));
    } else if (i == 2) {
      vb0 = r32 < 16 ? tb * KVT + hh * 2048 + 1024 + h * 256 + r32 * 16 : tb * KVT + 8192 + ((r32 >= 20 && r32 < 28) ? 64 : 0);
      const int t = r32 - 4 * h, n = t < 0 ? 0 : 4 * (t >> 3) + ((t & 7) < 3 ? (t & 7) + 1 : 4);
      mbits = (1u << n) - 1u;
      fbo = lane < 8 ? WS_FLAGB + wg_of(tt, lane) * 128 : OOB;
      asm volatile("" : "+v"(vb0), "+v"(mbits), "+v"(fbo));
    } else if (i == 3) {
      ovo = (r32 >> 4) * 1024 + ((2 * (hh & 1) + h) * 16 + (r32 & 15)) * 16 + tb * 32768 + (hh >> 1) * 2048;
      swz = (c8 ^ (((l8 >> 1) & 3) << 1)) * 16;
      tra0 = (lane >> 2) * 128 + 8 * (p4 & 1) + (((p4 >> 1) ^ (((lane >> 3) & 3) << 1)) * 16);
      asm volatile("" : "+v"(ovo), "+v"(swz), "+v"(tra0));
    } else if (i == 4) {
      const int pd = w == 6 ? wg_ofd(cg >> 1, lane >> 1, 2 * (cg & 1) + (lane & 1)) : wg_ofd(d2, ft, lane & 3);
      const int pw = w < 4 ? w * 64 + lane : (w == 5 && lane < 16) ? wg_of(8 * kc + (lane & 7), 2 * d2 + (lane >> 3)) : pd;
      const int cnt = w < 4 ? (wg == 0 ? 64 : 0) : w == 5 ? (don ? 20 : 0) : w == 6 ? 30 : 0;
      fco = lane < cnt ? (w < 4 ? WS_FLAGA : WS_FLAGC) + pw * 128 : OOB;
      asm volatile("" : "+v"(fco));
    } else if (i == 5) {
      const int q = 64 * w + lane, row = q / 12, c = (q - 12 * row) ^ (((row >> 2) & 1) << 1);
      w1vo = (don && !(ABL & 8) && (ft < 14 || c < 4)) ? row * 2752 + c * 16 : OOB;
      pcr = L_PC + (8 * w + l8) * 256 + (((2 * c8) ^ ((8 * w + l8) & 15)) * 16);
      pcw0 = (lane & 15) * 256 + ((g4 ^ (lane & 15)) * 16);
      asm volatile("" : "+v"(w1vo), "+v"(pcr), "+v"(pcw0));
    } else {
      so_o = WS_O + tt * 65536 + cg * 4096; so_hn = WS_HN + cg * 262144 + tt * 4096; so_st = WS_ST + tt * 4096;
      so_g = don ? WS_G + ((4 * d2 + kc) * 43 + 3 * ft) * 2048 : 0;
      so_x = don ? WS_XP + (d2 * 15 + ft) * 73728 : 0;
      w1so = don ? kc * (512 * 2752) + ft * 192 : 0;
      asm volatile("" : "+s"(so_o), "+s"(so_hn), "+s"(so_st), "+s"(so_g), "+s"(so_x), "+s"(w1so));
    }
    pin();
  };
#pragma unroll
  for (int i = 0; i < SETUP0; ++i) setup(i);
  CSTAMP(44);
  if (w >= 4) load_x();
  CSTAMP(45);
  {
    unsigned long long pc;
    int len;
    asm volatile("s_getpc_b64 %0\n.Lmega_pf_start:\n\ts_mov_b32 %1, .Lmega_pf_end-.Lmega_pf_start" : "=s"(pc), "=s"(len));
    if (w == 0) dma16(L_JUNK, l16 + loc * 1024, mksrd((const void*)(pc & ~63ull), len > 0 ? len : 0), 0);
  }
  auto clear_kv = [&](int g) {
    if ((loc & 1) == g && w < 4 && !(ABL & 32)) {
#pragma unroll
      for (int i = 0; i < 4; ++i) st_sc1(sent4, sws, l16, kvs + w * 1024 + i * 4096);
    }
  };
  const u32 wvk = wvq + 1024, wvv = wvq + 2048;
  auto issue_w = [&](int st) {
    dma16(ring + wslot(st), st < 8 ? wvk : wvq, swqkv, st < 8 ? ko[st & 7] : ko[(2 * st) & 7]);
    dma16(ring + wslot(st) + 8192, st < 8 ? wvv : wvq, swqkv, st < 8 ? ko[st & 7] : ko[(2 * st + 1) & 7]);
  };
  auto issue_w03 = [&](int st) {
    if (w < 4) {
      dma4<4096, 4096, 4096, 32 * 3072, 1024 - 32 * 3072, 32 * 3072>(ring + wslot(st), wvk, swqkv, ko[st]);
    }
  };
  u32 epoch;
  {
    f32x2 xv[32];
    f32x2 s1a = {0.f, 0.f}, s1b = s1a, s2a = s1a, s2b = s1a, shift;
    auto acc4 = [&](int i) {
      const u32 d[4] = {xr[i].x, xr[i].y, xr[i].z, xr[i].w};
#pragma unroll
      for (int j = 0; j < 4; ++j) {
        const f32x2 a = f32x2{bflo(d[j]), bfhi(d[j])} - shift;
        xv[4 * i + j] = a;
        if (j & 1) { s1b += a; s2b = __builtin_elementwise_fma(a, a, s2b); } else { s1a += a; s2a = __builtin_elementwise_fma(a, a, s2a); }
      }
    };
    CSTAMP(40);
    asm volatile("s_load_dword %0, %1, 0x0" : "=s"(epoch) : "s"(ws) : "memory");
    asm volatile("s_waitcnt vmcnt(6)" : "+v"(xr[0]), "+v"(xr[1])::"memory");
    CSTAMP(41);
    {
      const float sh0 = dpp<0x00>(bflo(xr[0].x)), sh1 = dpp<0x141>(sh0), sh = (lane & 4) ? sh1 : sh0;
      shift = f32x2{sh, sh};
    }
    acc4(0); acc4(1); pin();
    asm volatile("s_waitcnt vmcnt(4)" : "+v"(xr[2]), "+v"(xr[3])::"memory");
    acc4(2); acc4(3); pin();
    asm volatile("s_waitcnt vmcnt(2)" : "+v"(xr[4]), "+v"(xr[5])::"memory");
    acc4(4); acc4(5); pin();
    asm volatile("s_waitcnt vmcnt(0)" : "+v"(xr[6]), "+v"(xr[7])::"memory");
    CSTAMP(47);
    acc4(6); acc4(7);
    s1a += s1b; s2a += s2b;
    const float s1 = sum8(s1a.x + s1a.y) * (1.f / 512.f), s2 = sum8(s2a.x + s2a.y) * (1.f / 512.f);
    const float rs = __builtin_amdgcn_rsqf(fmaxf(s2 - s1 * s1, 0.f) + 1e-5f), nb = -s1 * rs;
    const f32x2 rs2 = {rs, rs}, nb2 = {nb, nb};
    pin(); issue_w03(0); pin();
#pragma unroll
    for (int i = 0; i < 8; ++i) {
      u32 o[4];
#pragma unroll
      for (int j = 0; j < 4; ++j) { const f32x2 y = __builtin_elementwise_fma(xv[4 * i + j], rs2, nb2); o[j] = cvtpk(y.x, y.y); }
      lds_w128(xw + i * 8192, v4u{o[0], o[1], o[2], o[3]});
      if (i == 3) { pin(); issue_w03(1); pin(); }
    }
    if (tid == 0) *(LAS u32*)(uintptr_t)L_CNT = 0;
    pin();
    issue_w03(2);
    if (CLR_POS == 0) clear_kv(0);
    issue_w03(3);
    if (CLR_POS == 0) clear_kv(1);
    if (CLR_POS == 1) clear_kv(0);
    issue_w03(4);
    if (CLR_POS == 1) clear_kv(1);
    if (CLR_POS == 2) { clear_kv(0); clear_kv(1); }
    CSTAMP(48);
  }
  asm volatile("s_waitcnt lgkmcnt(0)" : "+s"(epoch)::"memory");
  const u32 tag = epoch + 1;
  STAMP(1);

  Frag4 xk[8], wa_, wb_;
  {
    f32x16 acc;
#pragma unroll
    for (int i = 0; i < 16; ++i) acc[i] = 0.f;
    const u32 xa[4] = {xa0, xa0 ^ 32, xa0 ^ 64, xa0 ^ 96}, wa2 = wa + 65536;
    vmwait<12>(); bar();
    CSTAMP(51);
    auto loop = [&](auto xfirst) {
      constexpr bool XF = decltype(xfirst)::value;
      auto wo = [&](int st) { return (st < 8 && wslot(st) < 131072) ? wa + (wslot(st) - 65536) : wa2 + (wslot(st) - 131072); };
#pragma unroll
      for (int s = 0; s < 4; ++s) { xk[0].f[s] = asfrag(lds_r128(xa[s])); wa_.f[s] = lds_tr(wo(0) + s * 2048, wo(0) + s * 2048 + 512); }
      pin();
      gemm4<XF, true>(acc, xk[0], wa_, xk[1], wb_, xa, 1 * 8192, wo(1));
      vmwait<8>(); bar(); issue_w(5); issue_w(6);
      gemm4<XF, true>(acc, xk[1], wb_, xk[2], wa_, xa, 2 * 8192, wo(2));
      CSTAMP(0);
      vmwait<8>(); bar(); issue_w(7);
      gemm4<XF, true>(acc, xk[2], wa_, xk[3], wb_, xa, 3 * 8192, wo(3));
      vmwait<6>(); bar(); issue_w(8);
      gemm4<XF, true>(acc, xk[3], wb_, xk[4], wa_, xa, 4 * 8192, wo(4));
      CSTAMP(2);
      vmwait<6>();
      bar();
      gemm4<XF, true>(acc, xk[4], wa_, xk[5], wb_, xa, 5 * 8192, wo(5));
      vmwait<4>();
      bar(); issue_w(9);
      if (tid == 0) __builtin_amdgcn_raw_buffer_store_b32(tag, rws, WS_FLAGA + wg * 128, 0, 16);
      gemm4<XF, true>(acc, xk[5], wb_, xk[6], wa_, xa, 6 * 8192, wo(6));
      CSTAMP(4);
      vmwait<4>();
      bar(); issue_w(10);
      gemm4<XF, true>(acc, xk[6], wa_, xk[7], wb_, xa, 7 * 8192, wo(7));
      vmwait<2>(); bar(); issue_w(11);
      gemm4<XF, false>(acc, xk[7], wb_, xk[7], wa_, xa, 0, qwa + (wslot(8) - L_RING));
      CSTAMP(6);
      const v4u a = guard4(v4u{cvtpk(acc[0], acc[1]), cvtpk(acc[2], acc[3]), cvtpk(acc[4], acc[5]), cvtpk(acc[6], acc[7])});
      st_sc1(a, sws, kvo, kvs + (XF ? 8192 : 0) + tb * 4096);
      const v4u b = guard4(v4u{cvtpk(acc[8], acc[9]), cvtpk(acc[10], acc[11]), cvtpk(acc[12], acc[13]), cvtpk(acc[14], acc[15])});
      st_sc1(b, sws, kvo, kvs + (XF ? 8192 + 512 : 1024) + tb * 4096);
      lds_w128(kvl, a);
      lds_w128(kvl + (XF ? 512 : 2048), b);
    };
    if (isV) loop(BoolC<true>{}); else loop(BoolC<false>{});
    STAMP(2);
  }
  bf16x8 qf;
  v4u hk[4], hv[4];
  u32 hko[4];
#pragma unroll
  for (int j = 0; j < 4; ++j) hko[j] = tt - 4 + j >= 0 ? l16 : h * 512;
  const u32 hvo = hpt >= 0 ? l16 : OOB;
  {
    const u32 xa[4] = {0, 0, 0, 0};
    f32x16 acc;
#pragma unroll
    for (int i = 0; i < 16; ++i) acc[i] = 0.f;
    auto qo = [&](int hs) { return qwa + (wslot(8 + (hs >> 1)) - L_RING) + (hs & 1) * 8192; };
    gemm4<false, false>(acc, xk[0], wa_, xk[0], wb_, xa, 0, qo(1));
    gemm4<false, false>(acc, xk[1], wb_, xk[0], wa_, xa, 0, qo(2));
    u32 fla = ld_flag(fao, sws);
    if (tid < 160) { const u32 one = (tid >> 5) + tt >= 4 ? 0x3F803F80u : 0u; lds_w128((tid >> 4) * KVT + 8192 + (tid & 15) * 16, v4u{one, one, one, one}); }
    gemm4<false, false>(acc, xk[2], wa_, xk[0], wb_, xa, 0, qo(3));
    CSTAMP(7);
    vmwait<3>(); bar();
    CSTAMP(8);
    auto get_k = [&]() {
      asm volatile("s_waitcnt vmcnt(0)" : "+v"(fla)::"memory");
      wait_flag(fla, fao, sws, tag, spins);
      ld4s(hk, hko, sws, hks);
    };
    if (HK_AT == 3) get_k();
    gemm4<false, false>(acc, xk[3], wb_, xk[0], wa_, xa, 0, qo(4));
    if (HK_AT == 4) get_k();
    gemm4<false, false>(acc, xk[4], wa_, xk[0], wb_, xa, 0, qo(5));
    if (HK_AT == 5) get_k();
    if (HV_AT == 5) ld4(hv, hvo, sws, hso);
    CSTAMP(9);
    gemm4<false, false>(acc, xk[5], wb_, xk[0], wa_, xa, 0, qo(6));
    if (HV_AT == 6) ld4(hv, hvo, sws, hso);
    gemm4<false, false>(acc, xk[6], wa_, xk[0], wb_, xa, 0, qo(7));
    if (HV_AT == 7) ld4(hv, hvo, sws, hso);
#pragma unroll
    for (int s = 0; s < 4; ++s) acc = mfma32(wb_.f[s], xk[7].f[s], acc);
    CSTAMP(10);
    const float qs = 0.25f * 1.4426950408889634f;
    qf = asfrag(v4u{cvtpk(acc[0] * qs, acc[1] * qs), cvtpk(acc[2] * qs, acc[3] * qs), cvtpk(acc[4] * qs, acc[5] * qs), cvtpk(acc[6] * qs, acc[7] * qs)});
  }
  STAMP(3);
  if (SETUP0 <= 2) setup(2);
  if (SETUP0 <= 3) setup(3);
  if (SETUP0 <= 6) setup(6);

  u32 flb, b2v;
  v4u xres;
  v2u b1a, b1b;
  bf16x8 wf[4][4];
  {
    const bool ldr = w < 4;
    const v4i swo = mksrd(gwo, 512 * 512 * 2);
    const u32 kb = tb * KVT + hh * 2048 + l16, vb1 = r32 < 16 ? vb0 + 512 : vb0;
    const u32 wovo = (ABL & 4) ? OOB : l8 * 1024 + swz + cg * 128;
    const u32 dov = don ? l16 : OOB, so_xc = so_x + kc * 18432 + w * 1024;
    auto bg = [&](int op) {
      if (op < 2) st_sc1(sent4, sws, (ABL & 2) ? OOB : l16, so_o + w * 1024 + op * 32768);
      else if (op == 2) st_sc1(sent4, sws, tid < 32 ? l16 : OOB, so_st + cg * 512);
      else if (op < 7) {
        const int kk = 4 * w + ((op - 3 + loc) & 3);
        dma4<1024, 1024, 1024, 16384, -8192, 16384>(L_WO + kk * 4096, wovo, swo, kk * 32768);
      }
      else if (op < 9) st_sc1(sent4, sws, (ABL & 2) ? OOB : l16, so_hn + w * 1024 + (op - 7) * 131072);
      else if (op < 11) st_sc1(sent4, sws, (!(ABL & 2) && (op == 9 ? (ft < 14 || w < 2) : (ft < 14 && w < 2))) ? dov : OOB, so_g + w * 1024 + (op - 9) * 4096);
      else st_sc1(sent4, sws, ((op == 15 && w >= 2) || (ABL & 1)) ? OOB : dov, so_xc + (op - 11) * 4096);
    };
    auto hook = [&](int pt) {
      if (ldr) {
#pragma unroll
        for (int op = 0; op < NBG; ++op) if (BG_AT[op] == pt) bg(op);
        if (pt == BG_SIG) { vmwait<bg_after(2, BG_SIG)>(); signal<4>(L_CNT, lane, rws, WS_FLAGB + wg * 128, tag); }
      }
      if (pt == BG_BAR) {
        vmwait<bg_after(6, BG_BAR)>(); bar();
        xres = aload(xro, mksrd(gx, 2048 * 512 * 2), cg * 128);
        asm volatile("buffer_load_ushort %0, %1, %2, 0 offen" : "=v"(b2v) : "v"((xro >> 10) * 2), "s"(mksrd(gb2, 2048 * 2)) : "memory");
        asm volatile("buffer_load_dwordx2 %0, %2, %3, 0 offen\n\tbuffer_load_dwordx2 %1, %2, %3, 0 offen offset:32" : "=&v"(b1a), "=&v"(b1b) : "v"(don ? (96 * ft + 32 * (s6 >> 1) + 4 * g4) * 2 : OOB), "s"(mksrd(gb1, 1376 * 2)) : "memory");
      }
      if (pt == BG_BAR || pt == BG_BAR + 1) {
#pragma unroll
        for (int k = 2 * (pt - BG_BAR); k < 2 * (pt - BG_BAR) + 2; ++k)
#pragma unroll
          for (int n = 0; n < 4; ++n) { const u32 ad = ((L_WO + (w & 3) * 16384 + tra0) ^ (32 * n)) + k * 4096; wf[k][n] = lds_tr(ad, ad + 2048); }
      }
      if (pt == BG_FLAG && w == 4) flb = ld_flag(fbo, sws);
      pin();
    };
    f32x16 z16;
#pragma unroll
    for (int i = 0; i < 16; ++i) z16[i] = 0.f;
    bf16x8 kf[9];
    kf[8] = asfrag(lds_r128(kb + 8 * KVT));
    f32x16 sb = mfma32(kf[8], qf, z16);
    u32 fm[16], pm[8];
#pragma unroll
    for (int i = 0; i < 16; ++i) fm[i] = (u32)__builtin_amdgcn_sbfe((int)mbits, i, 1);
#pragma unroll
    for (int k = 0; k < 8; ++k) pm[k] = (fm[2 * k] & 0xFFFFu) | (fm[2 * k + 1] & 0xFFFF0000u);
    asm volatile("" : "+v"(pm[0]), "+v"(pm[1]), "+v"(pm[2]), "+v"(pm[3]), "+v"(pm[4]), "+v"(pm[5]), "+v"(pm[6]), "+v"(pm[7]));
    asm volatile("" : "+v"(fm[0]), "+v"(fm[1]), "+v"(fm[2]), "+v"(fm[3]), "+v"(fm[4]), "+v"(fm[5]), "+v"(fm[6]), "+v"(fm[7]), "+v"(fm[8]), "+v"(fm[9]),
                 "+v"(fm[10]), "+v"(fm[11]), "+v"(fm[12]), "+v"(fm[13]), "+v"(fm[14]), "+v"(fm[15]));
    take4s<4>(hk, hko, sws, hks, spins);
    CSTAMP(11);
#pragma unroll
    for (int j = 0; j < 4; ++j) { lds_w128(kb + 2 * j * KVT, hk[j]); kf[2 * j] = asfrag(hk[j]); }
    float m4[4] = {-3.0e38f, -3.0e38f, -3.0e38f, -3.0e38f};
    f32x16 sc = mfma32(kf[0], qf, z16), sa = mfma32(kf[2], qf, z16), sd = mfma32(kf[4], qf, z16), se = mfma32(kf[6], qf, z16);
    pin();
    bar();
    STAMP(4);
#pragma unroll
    for (int i = 1; i < 8; i += 2) kf[i] = asfrag(lds_r128(kb + i * KVT));
    pin();
    hook(0);
    f32x16 sm;
#pragma unroll
    for (int i = 0; i < 16; ++i) { const float a = sb[i], b = sc[i]; sm[i] = __uint_as_float((fm[i] & __float_as_uint(a)) | (~fm[i] & __float_as_uint(b))); }
    max16(m4, sm);
    pin(); max16(m4, sa); pin();
    sa = mfma32(kf[1], qf, z16); sb = mfma32(kf[3], qf, z16);
    pin(); max16(m4, sd); pin();
    sc = mfma32(kf[5], qf, z16);
    pin(); max16(m4, se); pin();
    sd = mfma32(kf[7], qf, z16);
    pin(); max16(m4, sa); max16(m4, sb); max16(m4, sc); max16(m4, sd);
    float mx = fmaxf(fmaxf(m4[0], m4[1]), fmaxf(m4[2], m4[3]));
    {
      const u32 a = __float_as_uint(mx);
      const auto sw = __builtin_amdgcn_permlane32_swap(a, a, false, false);
      const u32 s0 = sw[0], s1 = sw[1];
      mx = fmaxf(__uint_as_float(s0), __uint_as_float(s1));
    }
    CSTAMP(12);
    f32x16 nm;
#pragma unroll
    for (int i = 0; i < 16; ++i) nm[i] = -mx;
    f32x16 sn = mfma32(kf[1], qf, nm);
    pin();
    u32 pk[8];
#pragma unroll
    for (int k = 0; k < 8; ++k) pk[k] = cvtpk(__builtin_amdgcn_exp2f(sm[2 * k] - mx), __builtin_amdgcn_exp2f(sm[2 * k + 1] - mx));
    v4u pl0 = {pk[0] & pm[0], pk[1] & pm[1], pk[2] & pm[2], pk[3] & pm[3]}, pl1 = {pk[4] & pm[4], pk[5] & pm[5], pk[6] & pm[6], pk[7] & pm[7]};
    asm volatile("" : "+v"(pl0), "+v"(pl1));
    v4u pf0 = {pk[0] ^ pl0.x, pk[1] ^ pl0.y, pk[2] ^ pl0.z, pk[3] ^ pl0.w}, pf1 = {pk[4] ^ pl1.x, pk[5] ^ pl1.y, pk[6] ^ pl1.z, pk[7] ^ pl1.w};
    asm volatile("" : "+v"(pf0), "+v"(pf1));
    pin();
    if (!ldr) vmwait<0>();
    take4<bg_after(-1, 0)>(hv, hvo, sws, hso, spins);
#pragma unroll
    for (int u = 0; u < 4; ++u) lds_w128(w * KVT + u * 2048 + 1024 + l16, hv[u]);
    CSTAMP(13);
    bar();
    v4u v0 = lds_r128(vb0 + 8 * KVT), v1 = lds_r128(vb1 + 8 * KVT), v2 = lds_r128(vb0), v3 = lds_r128(vb1);
    hook(1);
    f32x16 o = z16;
    CSTAMP(14);
#pragma unroll
    for (int i = 1; i <= 7; ++i) {
      const f32x16 s = sn;
      if (i < 7) sn = mfma32(kf[i + 1], qf, nm);
      v4u va = lds_r128(vb0 + i * KVT), vb = lds_r128(vb1 + i * KVT);
      pin();
      float p[8];
#pragma unroll
      for (int j = 0; j < 8; ++j) p[j] = __builtin_amdgcn_exp2f(s[j]);
      const v4u pa = pack8(p);
      if (i == 1) {
        o = mfma32(asfrag(v0), asfrag(pl0), o);
        o = mfma32(asfrag(v1), asfrag(pl1), o);
        o = mfma32(asfrag(v2), asfrag(pf0), o);
        o = mfma32(asfrag(v3), asfrag(pf1), o);
      }
      o = mfma32(asfrag(va), asfrag(pa), o);
      pin();
      hook(2 * i);
#pragma unroll
      for (int j = 0; j < 8; ++j) p[j] = __builtin_amdgcn_exp2f(s[8 + j]);
      const v4u pb = pack8(p);
      o = mfma32(asfrag(vb), asfrag(pb), o);
      pin();
      hook(2 * i + 1);
      CSTAMP(14 + i);
    }
    const float inv = __builtin_amdgcn_rcpf(o[8]);
    const v4u of = guard4(v4u{cvtpk(o[0] * inv, o[1] * inv), cvtpk(o[2] * inv, o[3] * inv), cvtpk(o[4] * inv, o[5] * inv), cvtpk(o[6] * inv, o[7] * inv)});
    st_sc1(of, sws, ovo, so_o);
  }
  STAMP(5);

  auto dma_w1 = [&](int i) {
    const int s = (i + 4 * d2) & 15;
    if (w < 6) dma16(L_W1 + s * 6144 + w * 1024, w1vo, sw1, w1so + s * (32 * 2752));
  };
  v4u hres;
  {
    const u32 oso = WS_O + tt * 65536 + (w >> 2) * 32768 + (w & 3) * 8192;
    if (w == 4) {
      asm volatile("s_waitcnt vmcnt(1)" : "+v"(flb)::"memory");
      wait_flag(flb, fbo, sws, tag, spins);
    }
    CSTAMP(22);
    bar();
    pace<D_O, 0>();
    v4u ofr[2][4];
    ld4(ofr[0], l16, sws, oso);
    ld4(ofr[1], l16, sws, oso + 4096);
    STAMP(6);
    setup(5); setup(4);
    f32x4 acc[2][4];
#pragma unroll
    for (int m = 0; m < 2; ++m)
#pragma unroll
      for (int n = 0; n < 4; ++n) acc[m][n] = f32x4{0.f, 0.f, 0.f, 0.f};
#pragma unroll
    for (int k = 0; k < 4; ++k) {
      if (k == 0) take4<4>(ofr[0], l16, sws, oso, spins);
      if (k == 2) {
        take4<0>(ofr[1], l16, sws, oso + 4096, spins);
        asm volatile("" : "+v"(xres), "+v"(b2v), "+v"(b1a), "+v"(b1b));
#pragma unroll
        for (int i = 0; i < W1_0; ++i) dma_w1(i);
      }
#pragma unroll
      for (int m = 0; m < 2; ++m)
#pragma unroll
        for (int n = 0; n < 4; ++n) acc[m][n] = mfma16(wf[k][n], asfrag(ofr[k >> 1][2 * (k & 1) + m]), acc[m][n]);
      pin();
    }
#pragma unroll
    for (int i = W1_0; i < W1_A; ++i) dma_w1(i);
    STAMP(7);
#pragma unroll
    for (int m = 0; m < 2; ++m)
#pragma unroll
      for (int n = 0; n < 4; ++n) lds_wf4(((L_PC + (w & 3) * 16384 + (w >> 2) * 8192 + pcw0) ^ (64 * n)) + m * 4096, acc[m][n]);
#pragma unroll
    for (int i = W1_A; i < W1_B; ++i) dma_w1(i);
    bar();
    if (tid == 448) __builtin_amdgcn_raw_buffer_store_b32(tag, rws, WS_FLAGC + wg * 128, 0, 16);
    CSTAMP(23);
    f32x4 t[8];
#pragma unroll
    for (int k = 0; k < 4; ++k) { t[2 * k] = lds_rf4(pcr + k * 16384); t[2 * k + 1] = lds_rf4((pcr ^ 16) + k * 16384); }
    pin();
    hres = pack44(unlo(xres) + ((t[0] + t[2]) + (t[4] + t[6])), unhi(xres) + ((t[1] + t[3]) + (t[5] + t[7])));
  }
  {
    float hv[8] = {bflo(hres.x), bfhi(hres.x), bflo(hres.y), bfhi(hres.y), bflo(hres.z), bfhi(hres.z), bflo(hres.w), bfhi(hres.w)};
    const float pm_ = sum8(((hv[0] + hv[1]) + (hv[2] + hv[3])) + ((hv[4] + hv[5]) + (hv[6] + hv[7]))) * (1.f / 64.f);
    float q0 = 0.f, q1 = 0.f;
#pragma unroll
    for (int i = 0; i < 8; i += 2) { q0 = fmaf(hv[i] - pm_, hv[i] - pm_, q0); q1 = fmaf(hv[i + 1] - pm_, hv[i + 1] - pm_, q1); }
    const float q = sum8(q0 + q1);
    const v2u stv = {guard(__float_as_uint(pm_)), guard(__float_as_uint(q))};
    asm volatile("buffer_store_dwordx2 %0, %1, %2, %3 offen sc1" ::"v"(stv), "v"(c8 == 0 ? (8 * w + l8) * 8 : OOB), "s"(sws), "s"(so_st + cg * 512) : "memory");
    STAMP(8);
#pragma unroll
    for (int i = W1_B; i < W1_C; ++i) dma_w1(i);
    u32 sto = c8 * 512 + (8 * w + l8) * 8;
    u32 hw = L_PC + w * 2048 + l8 * 128 + swz;
    const int row = 4 * g4 + q4;
    u32 hr = L_PC + (4 * (w >> 2) + (row >> 3)) * 2048 + (row & 7) * 128 + (((2 * (w & 3) + (p4 >> 1)) ^ (((row >> 1) & 3) << 1)) * 16) + 8 * (p4 & 1);
    asm volatile("" : "+v"(sto), "+v"(hw), "+v"(hr));
    if (D_ST < 0 && w >= 6) vmwait<0>();
    pace<D_ST, W1_C - W1_B>();
    u32 flc = ld_flag(fco, sws);
    v2u st;
    asm volatile("buffer_load_dwordx2 %0, %1, %2, %3 offen sc1" : "=v"(st) : "v"(sto), "s"(sws), "s"(so_st) : "memory");
#pragma unroll
    for (int i = W1_C; i < W1_D; ++i) dma_w1(i);
    if (w >= 6) vmwait<0>();
    asm volatile("s_waitcnt vmcnt(%2)" : "+v"(st), "+v"(flc) : "n"(W1_D - W1_C) : "memory");
    if (__builtin_expect(any_sent(st.x > st.y ? st.x : st.y), 0)) {
#pragma clang loop unroll(disable)
      do {
        asm volatile("buffer_load_dwordx2 %0, %1, %2, %3 offen sc1\n\ts_waitcnt vmcnt(0)" : "+v"(st) : "v"(sto), "s"(sws), "s"(so_st) : "memory");
      } while (any_sent(st.x > st.y ? st.x : st.y) && ++spins < SPIN_CAP);
    }
    STAMP(9);
    const u32 sx_ = st.x, sy_ = st.y;
    const float mc = __uint_as_float(sx_), mean = sum8(mc) * 0.125f;
    const float var = sum8(fmaf(64.f * (mc - mean), mc - mean, __uint_as_float(sy_))) * (1.f / 512.f);
    const float r1 = __builtin_amdgcn_rsqf(var + 1e-5f);
    const float r2 = __builtin_amdgcn_rsqf(var * r1 * r1 + 1e-5f);
    const float sc = r1 * r2;
    const v4u hn = {cvtpk((hv[0] - mean) * sc, (hv[1] - mean) * sc), cvtpk((hv[2] - mean) * sc, (hv[3] - mean) * sc),
                    cvtpk((hv[4] - mean) * sc, (hv[5] - mean) * sc), cvtpk((hv[6] - mean) * sc, (hv[7] - mean) * sc)};
    lds_w128(hw, hn);
    wait_flag(flc, fco, sws, tag, spins);
    bar();
    if (wg == 0 && tid == 0) __builtin_amdgcn_raw_buffer_store_b32(tag, rws, WS_EPOCH, 0, 16);
    const v4u fr = guard4(__builtin_bit_cast(v4u, lds_tr(hr, hr + 2 * 2048)));
    st_sc1(fr, sws, l16, so_hn + ((w >> 1) & 1) * 131072 + (w >> 2) * 2048 + (w & 1) * 1024);
    STAMP(10);
#pragma unroll
    for (int i = W1_D; i < 16; ++i) dma_w1(i);
  }
  const v4i sw2 = mksrd(gw2, 1376 * 2048 * 2);
  int lnd = lane;
  asm volatile("" : "+v"(lnd));
  const u32 w2vo = (ABL & 16) ? OOB : (lnd >> 3) * 4096 + swz + tt * 128;
  const int eq = w >> 1, eh = w & 1;
  u32 pb[5];
  { const int r3 = EROT ? (tt + cg) % 3 : 0, r2 = EROT ? (tt + cg) & 1 : 0;
#pragma unroll
    for (int b = 0; b < 5; ++b) pb[b] = 11 * eq + 2 * (b < 3 ? (b + r3 >= 3 ? b + r3 - 3 : b + r3) : 3 + ((b - 3) ^ r2)); }
  auto w2slot = [&](int b) { return b < 3 ? L_W2A + eq * 24576 + (2 * b + eh) * 4096 : eq * 16384 + (2 * (b - 3) + eh) * 4096; };
  auto issue_w2 = [&](int b) { dma4<1024, 1024, 1024, 32768, 32768, 32768>(w2slot(b), w2vo, sw2, (pb[b] + eh) * 131072); };
  if (don) {
    const int dq = w >> 1, kh = w & 1;
    f32x4 acc[6][2];
#pragma unroll
    for (int b = 0; b < 6; ++b)
#pragma unroll
      for (int m = 0; m < 2; ++m) acc[b][m] = f32x4{0.f, 0.f, 0.f, 0.f};
    const u32 so = WS_HN + (2 * d2 + (dq >> 1)) * 262144 + (dq & 1) * 131072 + (16 * kc + 8 * kh) * 2048;
    const int r4 = (loc >> 2) & 3;
    u32 w1r[2], w1o[4];
#pragma unroll
    for (int e = 0; e < 2; ++e) w1r[e] = L_W1 + kh * 49152 + (lnd >> 2) * 192 + (((2 * e + ((lnd >> 1) & 1)) ^ (((lnd >> 4) & 1) << 1)) * 16) + 8 * (lnd & 1);
#pragma unroll
    for (int i = 0; i < 4; ++i) w1o[i] = ((i + r4) & 3);
    v4u hf[4][4];
    vmwait<0>();
    bar();
    ld4(hf[0], l16, sws, so + w1o[0] * 4096);
    ld4(hf[1], l16, sws, so + w1o[1] * 4096);
    CSTAMP(25);
    bf16x8 wc[6], wn[6];
    auto rd_w1 = [&](bf16x8 (&f)[6], int s) {
      const u32 o = w1o[s >> 1] * 12288 + (s & 1) * 6144;
#pragma unroll
      for (int b = 0; b < 6; ++b) { const u32 ad = w1r[b & 1] + o + (b >> 1) * 64; f[b] = lds_tr(ad, ad + 16 * 192); }
    };
    v4u w2r[4][4];
    auto raw_w2 = [&](int i) {
#pragma unroll
      for (int u = 0; u < 4; ++u) w2r[i][u] = aload(w2vo, sw2, (pb[i] + eh) * 131072 + u * 32768);
    };
    const int sj = (s6 >> 1) & 3, sm_ = s6 & 1;
    u32 xvo = (dq != kc && (ft < 14 || s6 < 2)) ? l16 : OOB;
    u32 ua0 = L_PD + 49152 + sm_ * 6144 + (sj < 3 ? sj : 0) * 2048 + l16, ua1 = L_PD + (2 * kc + 1 - sm_) * 6144 + (sj < 3 ? sj : 0) * 2048 + l16;
    u32 w2w = w2slot(0) + l16;
    asm volatile("" : "+v"(xvo), "+v"(ua0), "+v"(ua1), "+v"(w2w));
    u32 xso = so_x + kc * 18432 + (dq != kc ? s6 : 0) * 3072, xst = so_x + dq * 18432 + kh * 3072 + ((kc - dq - 1) & 3) * 1024;
    u32 gso = so_g + (dq != kc ? s6 * 1024 : 0);
    asm volatile("" : "+s"(xso), "+s"(xst), "+s"(gso));
    rd_w1(wc, 0);
#pragma unroll
    for (int s = 0; s < 8; ++s) {
      if (s == 0) { take4<4>(hf[0], l16, sws, so + w1o[0] * 4096, spins); ld4(hf[2], l16, sws, so + w1o[2] * 4096); }
      if (s == 2) { take4<4>(hf[1], l16, sws, so + w1o[1] * 4096, spins); ld4(hf[3], l16, sws, so + w1o[3] * 4096); }
      if (s == 4) { take4<4>(hf[2], l16, sws, so + w1o[2] * 4096, spins); raw_w2(0); }
      if (s == 6) { take4<4>(hf[3], l16, sws, so + w1o[3] * 4096, spins); raw_w2(1); }
      if (s == 7) raw_w2(2);
      const u32 o = w1o[(s + 1) >> 1 & 3] * 12288 + ((s + 1) & 1) * 6144;
      u32 a0 = w1r[0] + o, a1 = w1r[1] + o;
      asm volatile("" : "+v"(a0), "+v"(a1));
#pragma unroll
      for (int b = 0; b < 6; ++b) {
#pragma unroll
        for (int m = 0; m < 2; ++m) acc[b][m] = mfma16(wc[b], asfrag(hf[s >> 1][2 * (s & 1) + m]), acc[b][m]);
        if (s < 7) { const u32 ad = ((b & 1) ? a1 : a0) + (b >> 1) * 64; wn[b] = lds_tr(ad, ad + 16 * 192); }
        pin();
      }
#pragma unroll
      for (int b = 0; b < 6; ++b) wc[b] = wn[b];
      CSTAMP(26 + s);
    }
    STAMP(11);
    const u32 pd = L_PD + w * 6144 + l16;
    if (kh) {
#pragma unroll
      for (int b = 0; b < 6; ++b) { lds_wf4(pd + b * 1024, acc[b][0]); acc[b][0] = acc[b][1]; }
    } else {
#pragma unroll
      for (int b = 0; b < 6; ++b) lds_wf4(pd + b * 1024, acc[b][1]);
    }
    bar();
    CSTAMP(34);
    if (dq == kc) {
#pragma unroll
      for (int b = 0; b < 6; ++b) lds_wf4(L_PD + 49152 + kh * 6144 + l16 + b * 1024, acc[b][0]);
    } else {
      f32x4 t[6];
#pragma unroll
      for (int b = 0; b < 6; ++b) t[b] = lds_rf4(L_PD + (w ^ 1) * 6144 + l16 + b * 1024);
      pin();
#pragma unroll
      for (int j = 0; j < 3; ++j)
        st_sc1(guard4(pack44(acc[2 * j][0] + t[2 * j], acc[2 * j + 1][0] + t[2 * j + 1])), sws, (j > 0 && ft == 14) ? OOB : l16, xst + j * 6144);
    }
    CSTAMP(35);
    bar();
    f32x4 u0 = lds_rf4(ua0), u1 = lds_rf4(ua0 + 1024), u2 = lds_rf4(ua1), u3 = lds_rf4(ua1 + 1024);
    if (dq == kc) vmwait<0>();
    asm volatile("s_waitcnt vmcnt(3)" : "+v"(w2r[0][0]), "+v"(w2r[0][1]), "+v"(w2r[0][2]), "+v"(w2r[0][3]), "+v"(w2r[1][0]), "+v"(w2r[1][1]), "+v"(w2r[1][2]),
                 "+v"(w2r[1][3]), "+v"(w2r[2][0]), "+v"(w2r[2][1]), "+v"(w2r[2][2]), "+v"(w2r[2][3])::"memory");
    v4u xp[3];
#pragma unroll
    for (int i = 0; i < 3; ++i) {
      if (i == XP_AT) { pin(); pace<D_XP, 0>(); ld3(xp, xvo, sws, xso); pin(); }
#pragma unroll
      for (int u = 0; u < 4; ++u) lds_w128(w2w + i * 8192 + u * 1024, w2r[i][u]);
    }
    if (XP_AT == 3) { pin(); pace<D_XP, 0>(); ld3(xp, xvo, sws, xso); }
    if (R3) raw_w2(3);
    u0 += u2 + f32x4{bflo(b1a.x), bfhi(b1a.x), bflo(b1a.y), bfhi(b1a.y)};
    u1 += u3 + f32x4{bflo(b1b.x), bfhi(b1b.x), bflo(b1b.y), bfhi(b1b.y)};
    asm volatile("" : "+v"(u0), "+v"(u1));
    take3<R3 ? 4 : 0>(xp, xvo, sws, xso, spins);
    CSTAMP(38);
    u0 += (unlo(xp[0]) + unlo(xp[1])) + unlo(xp[2]);
    u1 += (unhi(xp[0]) + unhi(xp[1])) + unhi(xp[2]);
    const v4u gv = guard4(v4u{cvtpk(gelu_tanh(u0.x), gelu_tanh(u0.y)), cvtpk(gelu_tanh(u0.z), gelu_tanh(u0.w)),
                              cvtpk(gelu_tanh(u1.x), gelu_tanh(u1.y)), cvtpk(gelu_tanh(u1.z), gelu_tanh(u1.w))});
    st_sc1(gv, sws, xvo, gso);
    STAMP(12);
    bar();
    if (!R3) issue_w2(3);
    issue_w2(4);
    if (R3) {
      asm volatile("s_waitcnt vmcnt(5)" : "+v"(w2r[3][0]), "+v"(w2r[3][1]), "+v"(w2r[3][2]), "+v"(w2r[3][3])::"memory");
#pragma unroll
      for (int u = 0; u < 4; ++u) lds_w128(w2slot(3) + l16 + u * 1024, w2r[3][u]);
    }
  } else {
    vmwait<0>();
    bar();
    const u32 p = (xcd - 4) * 4 + (loc & 3);
#pragma clang loop unroll(disable)
    for (int i = 0; i < W2PF; ++i) {
      dma16(w * 4096, l16, sw2, (i < 16 ? 11 * (i & 3) + 6 + (i >> 2) : 11 * (i - 16) + 10) * 131072 + p * 8192 + w * 1024);
      vmwait<6>();
    }
    vmwait<0>();
    bar();
    issue_w2(0); issue_w2(1); issue_w2(2);
    vmwait<0>();
    bar();
    issue_w2(3); issue_w2(4);
    STAMP(12);
  }
  {
    f32x4 acc[2][4];
#pragma unroll
    for (int m = 0; m < 2; ++m)
#pragma unroll
      for (int n = 0; n < 4; ++n) acc[m][n] = f32x4{0.f, 0.f, 0.f, 0.f};
    const u32 so = WS_G + (cg * 2 + eh) * (43 * 2048);
    auto gso = [&](int b) { return so + (b < 5 ? pb[b < 5 ? b : 0] : 11 * eq + 10) * 2048; };
    u32 wa_[4], wl_[4], pew[2];
#pragma unroll
    for (int n = 0; n < 4; ++n) { wa_[n] = (tra0 + L_W2A + eq * 24576) ^ (32 * n); wl_[n] = (tra0 + eq * 16384) ^ (32 * n); }
#pragma unroll
    for (int m = 0; m < 2; ++m) pew[m] = (L_W2A + eq * 24576 + 8192 + pcw0) ^ (128 * eh + 64 * m);
    u32 per = L_W2A + 8192 + pcr, outo = xro + cg * 128;
    asm volatile("" : "+v"(wa_[0]), "+v"(wa_[1]), "+v"(wa_[2]), "+v"(wa_[3]), "+v"(wl_[0]), "+v"(wl_[1]), "+v"(wl_[2]), "+v"(wl_[3]),
                 "+v"(pew[0]), "+v"(pew[1]), "+v"(per), "+v"(outo));
    auto rd_w2 = [&](int L, int n) {
      const u32 ad = L < 6 ? wa_[n] + L * 4096 : L < 10 ? wl_[n] + (L - 6) * 4096 : wa_[n] + eh * 4096;
      return lds_tr(ad, ad + 2048);
    };
    v4u gf[3][4];
    bf16x8 wc[4], wn[4];
#pragma unroll
    for (int n = 0; n < 4; ++n) wc[n] = rd_w2(0, n);
    pace<D_G, R3 ? 4 : 8>();
    CSTAMP(36);
    ld4(gf[0], l16, sws, gso(0));
    ld4(gf[1], l16, sws, gso(1));
    CSTAMP(37);
#pragma unroll
    for (int b = 0; b < 6; ++b) {
      if (b < 5 || eq < 3) {
        if (eq == 3) { if (b == 1 || b == 2) vmwait<4>(); else if (b == 4) vmwait<0>(); }
        if (b == 1 || b == 2) take4<8>(gf[b % 3], l16, sws, gso(b), spins);
        else if (b < 5) take4<4>(gf[b % 3], l16, sws, gso(b), spins);
        else take4<0>(gf[b % 3], l16, sws, gso(b), spins);
        if (b < 3 || (b == 3 && eq < 3)) ld4(gf[(b + 2) % 3], l16, sws, gso(b + 2));
#pragma unroll
        for (int e = 0; e < (b < 5 ? 2 : 1); ++e) {
          const int L = 2 * b + e;
#pragma unroll
          for (int n = 0; n < 4; ++n) {
#pragma unroll
            for (int m = 0; m < 2; ++m) acc[m][n] = mfma16(asfrag(gf[b % 3][2 * e + m]), wc[n], acc[m][n]);
            if (L < 10) wn[n] = rd_w2(L + 1, n);
            pin();
          }
#pragma unroll
          for (int n = 0; n < 4; ++n) wc[n] = wn[n];
        }
      }
      if (b == 0) {
        bar();
        if (eq < 3) dma4<1024, 1024, 1024, 32768, 32768, 32768>(L_W2A + eq * 24576 + eh * 4096, w2vo, sw2, (11 * eq + 10) * 131072);
      }
      if (b == 2) bar();
      pin();
      CSTAMP(52 + b);
    }
    STAMP(13);
#pragma unroll
    for (int n = 0; n < 4; ++n)
#pragma unroll
      for (int m = 0; m < 2; ++m) lds_wf4(pew[m] + n * 4096, acc[m][n]);
    const float bb = bflo(b2v);
    f32x4 s0 = unlo(hres) + bb, s1 = unhi(hres) + bb;
    u32 per1 = per ^ 16, per2 = per + 49152, per3 = per2 ^ 16;
    asm volatile("" : "+v"(s0), "+v"(s1), "+v"(per1), "+v"(per2), "+v"(per3));
    bar();
    CSTAMP(58);
    f32x4 t[8];
#pragma unroll
    for (int k = 0; k < 2; ++k) {
      t[2 * k] = lds_rf4(per + k * 24576); t[2 * k + 1] = lds_rf4(per1 + k * 24576);
      t[4 + 2 * k] = lds_rf4(per2 + k * 24576); t[5 + 2 * k] = lds_rf4(per3 + k * 24576);
    }
    pin();
    s0 += (t[0] + t[2]) + (t[4] + t[6]);
    s1 += (t[1] + t[3]) + (t[5] + t[7]);
    const v4u ov = pack44(s0, s1);
    asm volatile(".Lmega_pf_end:\n\tbuffer_store_dwordx4 %0, %1, %2, 0 offen" ::"v"(ov), "v"(outo), "s"(mksrd(gout, 2048 * 512 * 2)) : "memory");
  }
  STAMP(14);
}

extern "C" void kernel_launch(void* const* d_in, const int* in_sizes, int n_in, void* d_out, int out_size, void* d_ws, size_t ws_size,
                              hipStream_t stream) {
  (void)hipFuncSetAttribute((const void*)mega, hipFuncAttributeMaxDynamicSharedMemorySize, LDS_BYTES);
  mega<<<256, 512, LDS_BYTES, stream>>>((const u16*)d_in[0], (const u16*)d_in[1], (const u16*)d_in[2], (const u16*)d_in[4], (const u16*)d_in[5],
                                        (const u16*)d_in[6], (const u16*)d_in[7], (u16*)d_out, (char*)d_ws);
}
```

```cpp
#include <hip/hip_runtime.h>
#include <stdint.h>

typedef unsigned u32;
typedef unsigned short u16;
typedef u32 v4u __attribute__((ext_vector_type(4)));
typedef u32 v2u __attribute__((ext_vector_type(2)));
typedef int v4i __attribute__((ext_vector_type(4)));
typedef float f32x2 __attribute__((ext_vector_type(2)));
typedef float f32x4 __attribute__((ext_vector_type(4)));
typedef float f32x16 __attribute__((ext_vector_type(16)));
typedef short bf16x8 __attribute__((ext_vector_type(8)));
typedef short s16x4 __attribute__((ext_vector_type(4)));
typedef __bf16 bf2 __attribute__((ext_vector_type(2)));
#define LAS __attribute__((address_space(3)))
#define DI __device__ __forceinline__

#define ABL 0
#define CODE_PF 1
#define STAMPS 0
#define D_KV 0
#define CLR_POS 2
#define HK_AT 3
#define HV_AT 9
#define W1_0 3
#define W1_A 3
#define W1_B 6
#define W1_C 8
#define W1_D 10
#define NA 2
#define NB 2
#define W2PF 19
#define EROT 3
#define D_O 4
#define D_ST -1
#define D_HN -1
#define D_XP -1
#define D_G -1
#define BG_TABLE 0, 0, 0,   1, 1, 2, 3,   4, 5, 6, 7, 8, 9, 10, 11, 11
#define LATE47 0
#define FLAGW 4
#define BG_SIG 4
#define BG_BAR 13
#define BG_RDY 8
#define BG_FLAG 14
constexpr int NBG = 16;
constexpr int BG_AT[NBG] = {BG_TABLE};
constexpr int bg_after(int op0, int pt) { int n = 0; for (int op = op0 + 1; op < (LATE47 ? 7 : NBG); ++op) n += BG_AT[op] <= pt ? (op >= 3 && op < 7 ? 4 : 1) : 0; return n; }
constexpr bool bg_ok() { for (int op = 1; op < NBG; ++op) if (BG_AT[op] < BG_AT[op - 1]) return false; return BG_AT[2] <= BG_SIG && BG_AT[6] <= BG_RDY && BG_RDY <= BG_BAR && BG_BAR <= BG_FLAG && BG_AT[3] > 0; }
static_assert(bg_ok(), "the table lists the transfers in issue order");
#define SETUP0 2
template <bool B> struct BoolC { static constexpr bool value = B; };

constexpr u32 WS_EPOCH = 0, WS_FLAGA = 1u << 19, WS_FLAGB = WS_FLAGA + 32768, WS_FLAGC = WS_FLAGB + 32768, WS_DBG = 65536;
constexpr u32 WS_KV = 1u << 20;
constexpr u32 WS_O = WS_KV + (4u << 20);
constexpr u32 WS_HN = WS_O + (2u << 20);
constexpr u32 WS_G = WS_HN + (2u << 20);
constexpr u32 WS_ST = WS_G + (2u << 20);
constexpr u32 WS_XP = WS_ST + (1u << 17);
constexpr u32 WS_END = WS_XP + 240 * 18432;
constexpr u32 SENT = 0xFFFFFFFFu;
constexpr u32 OOB = 0x7FFF0000u;
constexpr int SPIN_CAP = 1 << 16;

constexpr u32 L_XN = 0;
constexpr u32 KVT = 8448;
constexpr u32 L_CNT = 85504;
constexpr u32 L_RING = 86016;
constexpr u32 wslot(int st) { return st < 4 ? L_RING + st * 16384 : st == 4 ? 65536 : L_RING + ((st - 5) & 3) * 16384; }
constexpr u32 L_JUNK = L_RING + 65536;
constexpr u32 L_WO = 98304;
constexpr u32 L_PC = 0;
constexpr u32 L_W1 = 65536;
constexpr u32 L_PD = 0;
constexpr u32 L_W2A = 65536;
constexpr u32 LDS_BYTES = 163840;

DI u32 cvtpk(float a, float b) { f32x2 v = {a, b}; bf2 r = __builtin_convertvector(v, bf2); return __builtin_bit_cast(u32, r); }
DI float dot2(u32 a, u32 b, float c) { return __builtin_amdgcn_fdot2_f32_bf16(__builtin_bit_cast(bf2, a), __builtin_bit_cast(bf2, b), c, false); }
DI float bflo(u32 x) { return __uint_as_float(x << 16); }
DI float bfhi(u32 x) { return __uint_as_float(x & 0xffff0000u); }
DI u32 guard(u32 x) { return x < 0xFFFFFFFEu ? x : 0xFFFFFFFEu; }
DI v4u guard4(v4u v) { v4u r = {guard(v.x), guard(v.y), guard(v.z), guard(v.w)}; return r; }
DI u32 umax3(u32 a, u32 b, u32 c) { u32 m = a > b ? a : b; return m > c ? m : c; }
DI u32 smax4(u32 m, v4u v) { return umax3(umax3(m, v.x, v.y), v.z, v.w); }
DI u32 vmax(v4u v) { return umax3(v.x > v.y ? v.x : v.y, v.z, v.w); }
DI u32 chk4(const v4u (&f)[4]) { return umax3(smax4(vmax(f[0]), f[1]), vmax(f[2]), vmax(f[3])); }
DI bool any_sent(u32 m) { return __builtin_amdgcn_ballot_w64(m == SENT) != 0; }
DI bf16x8 asfrag(v4u v) { return __builtin_bit_cast(bf16x8, v); }
DI f32x16 mfma32(bf16x8 a, bf16x8 b, f32x16 c) { return __builtin_amdgcn_mfma_f32_32x32x16_bf16(a, b, c, 0, 0, 0); }
DI f32x4 mfma16(bf16x8 a, bf16x8 b, f32x4 c) { return __builtin_amdgcn_mfma_f32_16x16x32_bf16(a, b, c, 0, 0, 0); }
DI v4u lds_r128(u32 a) { return *(const LAS v4u*)(uintptr_t)a; }
DI f32x4 lds_rf4(u32 a) { return *(const LAS f32x4*)(uintptr_t)a; }
DI void lds_w128(u32 a, v4u v) { *(LAS v4u*)(uintptr_t)a = v; }
DI void lds_wf4(u32 a, f32x4 v) { *(LAS f32x4*)(uintptr_t)a = v; }
DI bf16x8 lds_tr(u32 a0, u32 a1) {
  s16x4 lo = __builtin_amdgcn_ds_read_tr16_b64_v4i16((LAS s16x4*)(uintptr_t)a0);
  s16x4 hi = __builtin_amdgcn_ds_read_tr16_b64_v4i16((LAS s16x4*)(uintptr_t)a1);
  return __builtin_shufflevector(lo, hi, 0, 1, 2, 3, 4, 5, 6, 7);
}
template <int CTRL> DI float dpp(float v) { return __int_as_float(__builtin_amdgcn_update_dpp(0, __float_as_int(v), CTRL, 0xf, 0xf, true)); }
DI float sum8(float v) { v += dpp<0xB1>(v); v += dpp<0x4E>(v); v += dpp<0x141>(v); return v; }
DI v4i mksrd(const void* p, u32 bytes) { v4i s = {(int)(u32)(uintptr_t)p, (int)(u32)((uintptr_t)p >> 32), (int)bytes, 0x00020000}; return s; }
DI __amdgpu_buffer_rsrc_t mkrs(const void* p, u32 bytes) { return __builtin_amdgcn_make_buffer_rsrc((void*)p, 0, bytes, 0x00020000); }
DI void dma16(u32 lds, u32 voff, v4i srd, u32 soff) {
  asm volatile("s_mov_b32 m0, %0\n\ts_nop 0\n\tbuffer_load_dwordx4 %1, %2, %3 offen lds" ::"s"(lds), "v"(voff), "s"(srd), "s"(soff) : "memory");
}
DI v4u aload_sc1(u32 voff, v4i srd, u32 soff) {
  v4u r;
  asm volatile("buffer_load_dwordx4 %0, %1, %2, %3 offen sc1" : "=v"(r) : "v"(voff), "s"(srd), "s"(soff) : "memory");
  return r;
}
DI v4u aload(u32 voff, v4i srd, u32 soff) {
  v4u r;
  asm volatile("buffer_load_dwordx4 %0, %1, %2, %3 offen" : "=v"(r) : "v"(voff), "s"(srd), "s"(soff) : "memory");
  return r;
}
DI void ld4(v4u (&f)[4], u32 voff, v4i srd, u32 soff) {
  asm volatile("buffer_load_dwordx4 %0, %4, %5, %6 offen sc1\n\tbuffer_load_dwordx4 %1, %4, %5, %6 offen offset:1024 sc1\n\t"
               "buffer_load_dwordx4 %2, %4, %5, %6 offen offset:2048 sc1\n\tbuffer_load_dwordx4 %3, %4, %5, %6 offen offset:3072 sc1"
               : "=&v"(f[0]), "=&v"(f[1]), "=&v"(f[2]), "=&v"(f[3]) : "v"(voff), "s"(srd), "s"(soff) : "memory");
}
DI void ld4x(v4u (&f)[4], u32 va, u32 vb, v4i srd, u32 soff) {
  asm volatile("buffer_load_dwordx4 %0, %4, %6, %7 offen sc1\n\tbuffer_load_dwordx4 %1, %5, %6, %7 offen sc1\n\t"
               "buffer_load_dwordx4 %2, %4, %6, %7 offen offset:2048 sc1\n\tbuffer_load_dwordx4 %3, %5, %6, %7 offen offset:2048 sc1"
               : "=&v"(f[0]), "=&v"(f[1]), "=&v"(f[2]), "=&v"(f[3]) : "v"(va), "v"(vb), "s"(srd), "s"(soff) : "memory");
}
template <int N> DI void take4x(v4u (&f)[4], u32 va, u32 vb, v4i srd, u32 soff, int& spins) {
  asm volatile("s_waitcnt vmcnt(%4)" : "+v"(f[0]), "+v"(f[1]), "+v"(f[2]), "+v"(f[3]) : "n"(N) : "memory");
  if (__builtin_expect(any_sent(chk4(f)), 0)) {
#pragma clang loop unroll(disable)
    do {
      asm volatile("s_sleep 1\n\tbuffer_load_dwordx4 %0, %4, %6, %7 offen sc1\n\tbuffer_load_dwordx4 %1, %5, %6, %7 offen sc1\n\t"
                   "buffer_load_dwordx4 %2, %4, %6, %7 offen offset:2048 sc1\n\tbuffer_load_dwordx4 %3, %5, %6, %7 offen offset:2048 sc1\n\ts_waitcnt vmcnt(0)"
                   : "+v"(f[0]), "+v"(f[1]), "+v"(f[2]), "+v"(f[3]) : "v"(va), "v"(vb), "s"(srd), "s"(soff) : "memory");
    } while (any_sent(chk4(f)) && ++spins < SPIN_CAP);
  }
}
DI void ld4s(v4u (&f)[4], const u32 (&voff)[4], v4i srd, const u32 (&so)[4]) {
  asm volatile("buffer_load_dwordx4 %0, %4, %8, %9 offen sc1\n\tbuffer_load_dwordx4 %1, %5, %8, %10 offen sc1\n\t"
               "buffer_load_dwordx4 %2, %6, %8, %11 offen sc1\n\tbuffer_load_dwordx4 %3, %7, %8, %12 offen sc1"
               : "=&v"(f[0]), "=&v"(f[1]), "=&v"(f[2]), "=&v"(f[3])
               : "v"(voff[0]), "v"(voff[1]), "v"(voff[2]), "v"(voff[3]), "s"(srd), "s"(so[0]), "s"(so[1]), "s"(so[2]), "s"(so[3]) : "memory");
}
template <int N> DI void take4s(v4u (&f)[4], const u32 (&voff)[4], v4i srd, const u32 (&so)[4], int& spins) {
  asm volatile("s_waitcnt vmcnt(%4)" : "+v"(f[0]), "+v"(f[1]), "+v"(f[2]), "+v"(f[3]) : "n"(N) : "memory");
  if (__builtin_expect(any_sent(chk4(f)), 0)) {
#pragma clang loop unroll(disable)
    do {
      asm volatile("s_sleep 1\n\tbuffer_load_dwordx4 %0, %4, %8, %9 offen sc1\n\tbuffer_load_dwordx4 %1, %5, %8, %10 offen sc1\n\t"
                   "buffer_load_dwordx4 %2, %6, %8, %11 offen sc1\n\tbuffer_load_dwordx4 %3, %7, %8, %12 offen sc1\n\ts_waitcnt vmcnt(0)"
                   : "+v"(f[0]), "+v"(f[1]), "+v"(f[2]), "+v"(f[3])
                   : "v"(voff[0]), "v"(voff[1]), "v"(voff[2]), "v"(voff[3]), "s"(srd), "s"(so[0]), "s"(so[1]), "s"(so[2]), "s"(so[3]) : "memory");
    } while (any_sent(chk4(f)) && ++spins < SPIN_CAP);
  }
}
template <int N> DI void take4(v4u (&f)[4], u32 voff, v4i srd, u32 soff, int& spins) {
  asm volatile("s_waitcnt vmcnt(%4)" : "+v"(f[0]), "+v"(f[1]), "+v"(f[2]), "+v"(f[3]) : "n"(N) : "memory");
  if (__builtin_expect(any_sent(chk4(f)), 0)) {
#pragma clang loop unroll(disable)
    do {
      asm volatile("s_sleep 1\n\tbuffer_load_dwordx4 %0, %4, %5, %6 offen sc1\n\tbuffer_load_dwordx4 %1, %4, %5, %6 offen offset:1024 sc1\n\t"
                   "buffer_load_dwordx4 %2, %4, %5, %6 offen offset:2048 sc1\n\tbuffer_load_dwordx4 %3, %4, %5, %6 offen offset:3072 sc1\n\t"
                   "s_waitcnt vmcnt(0)"
                   : "+v"(f[0]), "+v"(f[1]), "+v"(f[2]), "+v"(f[3]) : "v"(voff), "s"(srd), "s"(soff) : "memory");
    } while (any_sent(chk4(f)) && ++spins < SPIN_CAP);
  }
}
template <int M1, int M2, int M3, int S1, int S2, int S3>
DI void dma4(u32 lds, u32 voff, v4i srd, u32 soff) {
  u32 t;
  soff = __builtin_amdgcn_readfirstlane(soff);
  lds = __builtin_amdgcn_readfirstlane(lds);
  asm volatile("s_mov_b32 m0, %1\n\ts_mov_b32 %0, %4\n\tbuffer_load_dwordx4 %2, %3, %0 offen lds\n\t"
               "s_add_i32 m0, m0, %5\n\ts_add_i32 %0, %0, %8\n\tbuffer_load_dwordx4 %2, %3, %0 offen lds\n\t"
               "s_add_i32 m0, m0, %6\n\ts_add_i32 %0, %0, %9\n\tbuffer_load_dwordx4 %2, %3, %0 offen lds\n\t"
               "s_add_i32 m0, m0, %7\n\ts_add_i32 %0, %0, %10\n\tbuffer_load_dwordx4 %2, %3, %0 offen lds"
               : "=&s"(t) : "s"(lds), "v"(voff), "s"(srd), "s"(soff), "n"(M1), "n"(M2), "n"(M3), "n"(S1), "n"(S2), "n"(S3) : "memory", "scc");
}
template <int N> DI void vmwait() { asm volatile("s_waitcnt vmcnt(%0)" ::"n"(N) : "memory"); }
DI void bar() { asm volatile("s_waitcnt lgkmcnt(0)\n\ts_barrier" ::: "memory"); }
DI v4u ld_sc1(__amdgpu_buffer_rsrc_t rs, u32 voff, u32 soff) { return __builtin_amdgcn_raw_buffer_load_b128(rs, voff, soff, 16); }
DI void st_sc1(v4u v, v4i srd, u32 voff, u32 soff) {
  asm volatile("buffer_store_dwordx4 %0, %1, %2, %3 offen sc1\n\ts_nop 1" ::"v"(v), "v"(voff), "s"(srd), "s"(soff) : "memory");
}
template <int D, int YOUNGER> DI void pace() { if (D < 0) vmwait<YOUNGER>(); else if (D > 0) __builtin_amdgcn_s_sleep(D < 0 ? 0 : D); }
DI void pin() { __builtin_amdgcn_sched_barrier(0); }

#define STAMP(i) do {} while (0)
#define CSTAMP(i) do {} while (0)

DI void max16(float (&m)[4], const f32x16& s) {
#pragma unroll
  for (int j = 0; j < 8; ++j) m[j & 3] = fmaxf(fmaxf(m[j & 3], s[2 * j]), s[2 * j + 1]);
}
DI v4u pack8(const float* p) { return v4u{cvtpk(p[0], p[1]), cvtpk(p[2], p[3]), cvtpk(p[4], p[5]), cvtpk(p[6], p[7])}; }
DI v4u pack44(f32x4 a, f32x4 b) { return v4u{cvtpk(a.x, a.y), cvtpk(a.z, a.w), cvtpk(b.x, b.y), cvtpk(b.z, b.w)}; }
DI f32x4 unlo(v4u v) { return f32x4{bflo(v.x), bfhi(v.x), bflo(v.y), bfhi(v.y)}; }
DI f32x4 unhi(v4u v) { return f32x4{bflo(v.z), bfhi(v.z), bflo(v.w), bfhi(v.w)}; }
DI v4u gelu8(f32x4 a, f32x4 b) {
  const float c1 = -2.0f * 0.7978845608028654f * 1.4426950408889634f, c2 = c1 * 0.044715f;
  const f32x2 x[4] = {{a.x, a.y}, {a.z, a.w}, {b.x, b.y}, {b.z, b.w}}, k1 = {c1, c1}, k2 = {c2, c2}, one = {1.f, 1.f};
  f32x2 t[4];
#pragma unroll
  for (int i = 0; i < 4; ++i) t[i] = x[i] * x[i];
  pin();
#pragma unroll
  for (int i = 0; i < 4; ++i) t[i] = __builtin_elementwise_fma(t[i], k2, k1);
  pin();
#pragma unroll
  for (int i = 0; i < 4; ++i) t[i] = t[i] * x[i];
  pin();
#pragma unroll
  for (int i = 0; i < 4; ++i) t[i] = f32x2{__builtin_amdgcn_exp2f(t[i].x), __builtin_amdgcn_exp2f(t[i].y)};
  pin();
#pragma unroll
  for (int i = 0; i < 4; ++i) t[i] = t[i] + one;
  pin();
#pragma unroll
  for (int i = 0; i < 4; ++i) t[i] = f32x2{__builtin_amdgcn_rcpf(t[i].x), __builtin_amdgcn_rcpf(t[i].y)};
  pin();
#pragma unroll
  for (int i = 0; i < 4; ++i) t[i] = t[i] * x[i];
  pin();
  return v4u{cvtpk(t[0].x, t[0].y), cvtpk(t[1].x, t[1].y), cvtpk(t[2].x, t[2].y), cvtpk(t[3].x, t[3].y)};
}
struct Frag4 { bf16x8 f[4]; };
template <bool XFIRST, bool RDX>
DI void gemm4(f32x16& acc, const Frag4& xc, const Frag4& wc, Frag4& xn, Frag4& wn, const u32 (&xa)[4], u32 xo, u32 wo) {
#pragma unroll
  for (int s = 0; s < 4; ++s) {
    acc = XFIRST ? mfma32(xc.f[s], wc.f[s], acc) : mfma32(wc.f[s], xc.f[s], acc);
    if (RDX) xn.f[s] = asfrag(lds_r128(xa[s] + xo));
    wn.f[s] = lds_tr(wo + s * 2048, wo + s * 2048 + 512);
    pin();
  }
}
DI void ld3(v4u (&f)[3], u32 voff, v4i srd, u32 soff) {
  asm volatile("buffer_load_dwordx4 %0, %3, %4, %5 offen sc1\n\tbuffer_load_dwordx4 %1, %3, %4, %5 offen offset:1024 sc1\n\t"
               "buffer_load_dwordx4 %2, %3, %4, %5 offen offset:2048 sc1"
               : "=&v"(f[0]), "=&v"(f[1]), "=&v"(f[2]) : "v"(voff), "s"(srd), "s"(soff) : "memory");
}
template <int N> DI void take3(v4u (&f)[3], u32 voff, v4i srd, u32 soff, int& spins) {
  asm volatile("s_waitcnt vmcnt(%3)" : "+v"(f[0]), "+v"(f[1]), "+v"(f[2]) : "n"(N) : "memory");
  if (__builtin_expect(any_sent(umax3(vmax(f[0]), vmax(f[1]), vmax(f[2]))), 0)) {
#pragma clang loop unroll(disable)
    do {
      asm volatile("s_sleep 1\n\tbuffer_load_dwordx4 %0, %3, %4, %5 offen sc1\n\tbuffer_load_dwordx4 %1, %3, %4, %5 offen offset:1024 sc1\n\t"
                   "buffer_load_dwordx4 %2, %3, %4, %5 offen offset:2048 sc1\n\ts_waitcnt vmcnt(0)"
                   : "+v"(f[0]), "+v"(f[1]), "+v"(f[2]) : "v"(voff), "s"(srd), "s"(soff) : "memory");
    } while (any_sent(umax3(vmax(f[0]), vmax(f[1]), vmax(f[2]))) && ++spins < SPIN_CAP);
  }
}
template <int N> DI void signal(u32 cnt, int lane, __amdgpu_buffer_rsrc_t rws, u32 flag, u32 tag) {
  u32 old = 0;
  if (lane == 0) old = __hip_atomic_fetch_add((LAS u32*)(uintptr_t)cnt, 1u, __ATOMIC_RELAXED, __HIP_MEMORY_SCOPE_WORKGROUP);
  if (__builtin_amdgcn_readfirstlane(old) == N - 1 && lane == 0) __builtin_amdgcn_raw_buffer_store_b32(tag, rws, flag, 0, 16);
}
DI int wg_of(int tt, int cg) { return ((tt & 7) + 8 * (cg & 3)) * 8 + 2 * (tt >> 3) + (cg >> 2); }
DI int wg_ofd(int d2, int ft, int kc) { return (d2 + 4 * (ft & 7)) * 8 + 4 * (ft >> 3) + kc; }
DI u32 ld_flag(u32 off, v4i sws) {
  u32 f;
  asm volatile("buffer_load_dword %0, %1, %2, 0 offen sc1" : "=v"(f) : "v"(off), "s"(sws) : "memory");
  return f;
}
DI void wait_flag(u32& f, u32 off, v4i sws, u32 tag, int& spins) {
  if (__builtin_expect(__builtin_amdgcn_ballot_w64(off != OOB && f != tag) != 0, 0)) {
#pragma clang loop unroll(disable)
    do {
      asm volatile("s_sleep 2\n\tbuffer_load_dword %0, %1, %2, 0 offen sc1\n\ts_waitcnt vmcnt(0)" : "+v"(f) : "v"(off), "s"(sws) : "memory");
    } while (__builtin_amdgcn_ballot_w64(off != OOB && f != tag) != 0 && ++spins < SPIN_CAP);
  }
}

__global__ void __launch_bounds__(512, 2)
mega(const u16* gx, const u16* gwqkv, const u16* gwo, const u16* gw1, const u16* gb1, const u16* gw2, const u16* gb2, u16* gout, char* ws) {
  asm volatile("" : "+s"(gx), "+s"(gwqkv), "+s"(gwo), "+s"(gw1), "+s"(gb1), "+s"(gw2), "+s"(gb2), "+s"(gout), "+s"(ws));
  const int tid = threadIdx.x, lane = tid & 63;
  const int w = __builtin_amdgcn_readfirstlane(tid >> 6);
  const int wg = blockIdx.x, xcd = wg & 7, loc = wg >> 3;
  const int tt = 8 * (xcd >> 1) + (loc & 7), cg = 4 * (xcd & 1) + (loc >> 3);
  const int kc = xcd & 3, d2 = loc & 3, ft = 8 * (xcd >> 2) + (loc >> 2);
  const bool don = ft < 15;
  const int rot = ((loc & 7) + 2 * (loc >> 3)) & 7;
  STAMP(0);
  const u32 l16 = lane * 16;
  const int l8 = lane >> 3, c8 = lane & 7;
  int spins = 0;

  v4u xr[8];
  const v4i sx = mksrd(gx, 2048 * 512 * 2);
  u32 xro = (64 * tt + 8 * w + l8) * 1024 + c8 * 16;
  auto load_x = [&]() {
#pragma unroll
    for (int i = 0; i < 8; ++i) xr[i] = aload(xro, sx, ((i + rot) & 7) * 128);
  };
  CSTAMP(42);
  if (w < 4) load_x();
  CSTAMP(43);
  const v4i swqkv = mksrd(gwqkv, 512 * 1536 * 2), sw1 = mksrd(gw1, 2048 * 1376 * 2);
  const __amdgpu_buffer_rsrc_t rws = mkrs(ws, WS_END);
  const v4i sws = mksrd(ws, WS_END);
  const int r32 = lane & 31, h = lane >> 5, blk = (lane >> 4) & 1, q4 = (lane & 15) >> 2, p4 = lane & 3, g4 = lane >> 4;
  const int hh = w & 3, tb = w >> 2;
  const int isV = w & 1, hp = (w >> 1) & 1;
  const int s6 = w < 2 * kc ? w : w - 2;
  v4u sent4 = {SENT, SENT, SENT, SENT};
  u32 kvs = WS_KV + (tt * 8 + cg) * 16384;
  u32 wvq = (8 * w + l8) * 3072 + ((c8 ^ (((l8 >> 1) & 1) << 2)) * 16) + cg * 128;
  u32 ko[8];
#pragma unroll
  for (int i = 0; i < 8; ++i) ko[i] = ((i + rot) & 7) * (64 * 3072);
  u32 ring = w * 1024;
  u32 xa0 = L_XN + (32 * tb + r32) * 128 + ((h ^ ((r32 >> 1) & 7)) * 16);
  u32 wa = 65536 + isV * 8192 + (8 * h + q4) * 128 + (((4 * hp + 2 * blk + (p4 >> 1)) ^ (((q4 >> 1) & 1) << 2)) * 16) + 8 * (p4 & 1);
  u32 xw = L_XN + (8 * w + l8) * 128 + ((c8 ^ (((8 * w + l8) >> 1) & 7)) * 16);
  asm volatile("" : "+v"(sent4), "+v"(xa0), "+v"(wa), "+v"(xw), "+v"(wvq));
  asm volatile("" : "+s"(ko[0]), "+s"(ko[1]), "+s"(ko[2]), "+s"(ko[3]), "+s"(ko[4]), "+s"(ko[5]), "+s"(ko[6]), "+s"(ko[7]), "+s"(kvs), "+s"(ring));
  auto clear_kv = [&]() {
    if (!(ABL & 32)) {
#pragma unroll
      for (int i = 0; i < 4; ++i) st_sc1(sent4, sws, l16, kvs + (w & 3) * 1024 + i * 4096);
    }
  };
  if (CLR_POS == 3 && w >= 4) clear_kv();
  u32 qwa, fao, kvo, kvl, hso, hks[4], vb0, mbits, ovo, swz, fbo, fco, so_o, so_hn, so_st, so_g, so_x, tra0, pcr, pcw0, w1vo, w1so;
  const int hpt = tt - 4 + (w >> 1);
  auto setup = [&](int i) {
    if (i == 0) {
      qwa = L_RING + (8 * h + q4) * 128 + (((2 * hh + (p4 >> 1)) ^ (((q4 >> 1) & 1) << 2)) * 16) + 8 * (p4 & 1);
      kvo = isV ? (2 * hp + (r32 >> 4)) * 1024 + h * 256 + (r32 & 15) * 16 : 2 * hp * 1024 + l16;
      kvl = (8 + tb) * KVT + (isV ? (2 * hp + (r32 >> 4)) * 2048 + 1024 + h * 256 + (r32 & 15) * 16 : 2 * hp * 2048 + l16);
      asm volatile("" : "+v"(qwa), "+v"(kvo), "+v"(kvl));
    } else if (i == 1) {
      fao = (tt - 4 + lane >= 0 && lane < 4) ? WS_FLAGA + wg_of(tt - 4 + lane, cg) * 128 : OOB;
      hso = WS_KV + ((hpt >= 0 ? hpt : 0) * 8 + cg) * 16384 + (w & 1) * 4096 + 8192;
#pragma unroll
      for (int j = 0; j < 4; ++j) hks[j] = WS_KV + cg * 16384 + hh * 1024 + (tt - 4 + j >= 0 ? (tt - 4 + j) * 131072 + tb * 4096 : 0);
      asm volatile("" : "+v"(fao));
      asm volatile("" : "+s"(hso), "+s"(hks[0]), "+s"(hks[1]), "+s"(hks[2]), "+s"(hks[3]));
    } else if (i == 2) {
      vb0 = r32 < 16 ? tb * KVT + hh * 2048 + 1024 + h * 256 + r32 * 16 : tb * KVT + 8192 + ((r32 >= 20 && r32 < 28) ? 64 : 0);
      const int t = r32 - 4 * h, n = t < 0 ? 0 : 4 * (t >> 3) + ((t & 7) < 3 ? (t & 7) + 1 : 4);
      mbits = (1u << n) - 1u;
      fbo = lane < 8 ? WS_FLAGB + wg_of(tt, lane) * 128 : OOB;
      asm volatile("" : "+v"(vb0), "+v"(mbits), "+v"(fbo));
    } else if (i == 3) {
      ovo = (r32 >> 4) * 1024 + ((2 * (hh & 1) + h) * 16 + (r32 & 15)) * 16 + tb * 32768 + (hh >> 1) * 2048;
      swz = (c8 ^ (((l8 >> 1) & 3) << 1)) * 16;
      tra0 = (lane >> 2) * 128 + 8 * (p4 & 1) + (((p4 >> 1) ^ (((lane >> 3) & 3) << 1)) * 16);
      asm volatile("" : "+v"(ovo), "+v"(swz), "+v"(tra0));
    } else if (i == 4) {
      const int pd = w == 6 ? wg_ofd(cg >> 1, lane >> 1, 2 * (cg & 1) + (lane & 1)) : wg_ofd(d2, ft, lane & 3);
      const int pw = w < 4 ? w * 64 + lane : (w == 5 && lane < 16) ? wg_of(8 * kc + (lane & 7), 2 * d2 + (lane >> 3)) : pd;
      const int cnt = w < 4 ? (wg == 0 ? 64 : 0) : w == 5 ? (don ? 20 : 0) : w == 6 ? 30 : 0;
      fco = lane < cnt ? (w < 4 ? WS_FLAGA : WS_FLAGC) + pw * 128 : OOB;
      asm volatile("" : "+v"(fco));
    } else if (i == 5) {
      const int q = 64 * w + lane, row = q / 12, c = (q - 12 * row) ^ (((row >> 2) & 1) << 1);
      w1vo = (don && !(ABL & 8) && (ft < 14 || c < 4)) ? row * 2752 + c * 16 : OOB;
      pcr = L_PC + (8 * w + l8) * 256 + (((2 * c8) ^ ((8 * w + l8) & 15)) * 16);
      pcw0 = (lane & 15) * 256 + ((g4 ^ (lane & 15)) * 16);
      asm volatile("" : "+v"(w1vo), "+v"(pcr), "+v"(pcw0));
    } else {
      so_o = WS_O + tt * 65536 + cg * 4096; so_hn = WS_HN + cg * 262144 + tt * 4096; so_st = WS_ST + tt * 4096;
      so_g = don ? WS_G + ((4 * d2 + kc) * 43 + 3 * ft) * 2048 : 0;
      so_x = don ? WS_XP + (d2 * 15 + ft) * 73728 : 0;
      w1so = don ? kc * (512 * 2752) + ft * 192 : 0;
      asm volatile("" : "+s"(so_o), "+s"(so_hn), "+s"(so_st), "+s"(so_g), "+s"(so_x), "+s"(w1so));
    }
    pin();
  };
#pragma unroll
  for (int i = 0; i < SETUP0; ++i) setup(i);
  CSTAMP(44);
  if (w >= 4) { load_x(); if (CLR_POS == 4) clear_kv(); }
  CSTAMP(45);
  {
    unsigned long long pc;
    int len;
    asm volatile("s_getpc_b64 %0\n.Lmega_pf_start:\n\ts_mov_b32 %1, .Lmega_pf_end-.Lmega_pf_start" : "=s"(pc), "=s"(len));
    if (w == 0) dma16(L_JUNK, l16 + loc * 1024, mksrd((const void*)(pc & ~63ull), len > 0 ? len : 0), 0);
  }
  const u32 wvk = wvq + 1024, wvv = wvq + 2048;
  auto issue_w = [&](int st) {
    dma16(ring + wslot(st), st < 8 ? wvk : wvq, swqkv, st < 8 ? ko[st & 7] : ko[(2 * st) & 7]);
    dma16(ring + wslot(st) + 8192, st < 8 ? wvv : wvq, swqkv, st < 8 ? ko[st & 7] : ko[(2 * st + 1) & 7]);
  };
  auto issue_w03 = [&](int st) {
    if (w < 4) {
      dma4<4096, 4096, 4096, 32 * 3072, 1024 - 32 * 3072, 32 * 3072>(ring + wslot(st), wvk, swqkv, ko[st]);
    }
  };
  u32 epoch;
  {
    f32x2 xv[32];
    float s1[2] = {0.f, 0.f}, s2[2] = {0.f, 0.f};
    auto acc4 = [&](int i) {
      const u32 d[4] = {xr[i].x, xr[i].y, xr[i].z, xr[i].w};
#pragma unroll
      for (int j = 0; j < 4; ++j) {
        s1[j & 1] = dot2(d[j], 0x3F803F80u, s1[j & 1]);
        s2[j & 1] = dot2(d[j], d[j], s2[j & 1]);
      }
    };
    auto unpack = [&](int i) {
      const u32 d[4] = {xr[i].x, xr[i].y, xr[i].z, xr[i].w};
#pragma unroll
      for (int j = 0; j < 4; ++j) xv[4 * i + j] = f32x2{bflo(d[j]), bfhi(d[j])};
      asm volatile("" : "+v"(xv[4 * i]), "+v"(xv[4 * i + 1]), "+v"(xv[4 * i + 2]), "+v"(xv[4 * i + 3]));
    };
    CSTAMP(40);
    asm volatile("s_load_dword %0, %1, 0x0" : "=s"(epoch) : "s"(ws) : "memory");
    if (CLR_POS != 4 || w < 4 || (ABL & 32)) vmwait<6>();
    asm volatile("s_waitcnt vmcnt(10)" : "+v"(xr[0]), "+v"(xr[1])::"memory");
    CSTAMP(41);
    acc4(0); acc4(1); unpack(0); unpack(1); pin();
    if (CLR_POS != 4 || w < 4 || (ABL & 32)) vmwait<4>();
    asm volatile("s_waitcnt vmcnt(8)" : "+v"(xr[2]), "+v"(xr[3])::"memory");
    acc4(2); acc4(3); unpack(2); unpack(3); pin();
    if (CLR_POS != 4 || w < 4 || (ABL & 32)) vmwait<2>();
    asm volatile("s_waitcnt vmcnt(6)" : "+v"(xr[4]), "+v"(xr[5])::"memory");
    acc4(4); acc4(5); unpack(4); unpack(5); pin();
    if (CLR_POS != 4 || w < 4 || (ABL & 32)) vmwait<0>();
    asm volatile("s_waitcnt vmcnt(4)" : "+v"(xr[6]), "+v"(xr[7])::"memory");
    CSTAMP(47);
    acc4(6); acc4(7);
    pin();
    float m1 = s1[0] + s1[1], m2 = s2[0] + s2[1];
    m1 += dpp<0xB1>(m1); m2 += dpp<0xB1>(m2);
    unpack(6); pin();
    m1 += dpp<0x4E>(m1); m2 += dpp<0x4E>(m2);
    unpack(7); pin();
    m1 += dpp<0x141>(m1); m2 += dpp<0x141>(m2);
    m1 *= 1.f / 512.f;
    const float rs = __builtin_amdgcn_rsqf(fmaxf(m2 * (1.f / 512.f) - m1 * m1, 0.f) + 1e-5f), nb = -m1 * rs;
    const f32x2 rs2 = {rs, rs}, nb2 = {nb, nb};
    pin(); issue_w03(0); pin();
#pragma unroll
    for (int i = 0; i < 8; i += 2) {
      f32x2 y[8];
#pragma unroll
      for (int j = 0; j < 8; ++j) y[j] = __builtin_elementwise_fma(xv[4 * i + j], rs2, nb2);
      pin();
      lds_w128(xw + i * 8192, v4u{cvtpk(y[0].x, y[0].y), cvtpk(y[1].x, y[1].y), cvtpk(y[2].x, y[2].y), cvtpk(y[3].x, y[3].y)});
      lds_w128(xw + (i + 1) * 8192, v4u{cvtpk(y[4].x, y[4].y), cvtpk(y[5].x, y[5].y), cvtpk(y[6].x, y[6].y), cvtpk(y[7].x, y[7].y)});
      pin();
      if (i == 2) { issue_w03(1); pin(); }
    }
    if (tid < 5) *(LAS u32*)(uintptr_t)(L_CNT + 4 * tid) = 0;
    pin();
    issue_w03(2); issue_w03(3);
    if (CLR_POS == 2 && w < 4) clear_kv();
    CSTAMP(48);
  }
  asm volatile("s_waitcnt lgkmcnt(0)" : "+s"(epoch)::"memory");
  const u32 tag = epoch + 1;
  STAMP(1);

  Frag4 xk[8], wa_, wb_;
  v4u kva, kvb;
  {
    f32x16 acc;
#pragma unroll
    for (int i = 0; i < 16; ++i) acc[i] = 0.f;
    const u32 xa[4] = {xa0, xa0 ^ 32, xa0 ^ 64, xa0 ^ 96}, wa2 = wa + 65536;
    vmwait<8>(); bar();
    CSTAMP(51);
    auto loop = [&](auto xfirst) {
      constexpr bool XF = decltype(xfirst)::value;
      auto wo = [&](int st) { return (st < 8 && wslot(st) < 131072) ? wa + (wslot(st) - 65536) : wa2 + (wslot(st) - 131072); };
#pragma unroll
      for (int s = 0; s < 4; ++s) { xk[0].f[s] = asfrag(lds_r128(xa[s])); wa_.f[s] = lds_tr(wo(0) + s * 2048, wo(0) + s * 2048 + 512); }
      pin();
      gemm4<XF, true>(acc, xk[0], wa_, xk[1], wb_, xa, 1 * 8192, wo(1));
      issue_w(4);
      vmwait<6>(); bar(); issue_w(5);
      gemm4<XF, true>(acc, xk[1], wb_, xk[2], wa_, xa, 2 * 8192, wo(2));
      issue_w(6);
      CSTAMP(0);
      vmwait<6>(); bar(); issue_w(7);
      gemm4<XF, true>(acc, xk[2], wa_, xk[3], wb_, xa, 3 * 8192, wo(3));
      vmwait<6>(); bar(); issue_w(8);
      gemm4<XF, true>(acc, xk[3], wb_, xk[4], wa_, xa, 4 * 8192, wo(4));
      CSTAMP(2);
      vmwait<6>();
      bar();
      gemm4<XF, true>(acc, xk[4], wa_, xk[5], wb_, xa, 5 * 8192, wo(5));
      vmwait<4>();
      bar(); issue_w(9);
      if (tid == 0) __builtin_amdgcn_raw_buffer_store_b32(tag, rws, WS_FLAGA + wg * 128, 0, 16);
      gemm4<XF, true>(acc, xk[5], wb_, xk[6], wa_, xa, 6 * 8192, wo(6));
      CSTAMP(4);
      vmwait<4>();
      bar(); issue_w(10);
      gemm4<XF, true>(acc, xk[6], wa_, xk[7], wb_, xa, 7 * 8192, wo(7));
      vmwait<2>(); bar(); issue_w(11);
      gemm4<XF, false>(acc, xk[7], wb_, xk[7], wa_, xa, 0, qwa + (wslot(8) - L_RING));
      CSTAMP(6);
      kva = guard4(v4u{cvtpk(acc[0], acc[1]), cvtpk(acc[2], acc[3]), cvtpk(acc[4], acc[5]), cvtpk(acc[6], acc[7])});
      if (!XF) st_sc1(kva, sws, kvo, kvs + tb * 4096);
      kvb = guard4(v4u{cvtpk(acc[8], acc[9]), cvtpk(acc[10], acc[11]), cvtpk(acc[12], acc[13]), cvtpk(acc[14], acc[15])});
      if (!XF) st_sc1(kvb, sws, kvo, kvs + 1024 + tb * 4096);
      lds_w128(kvl, kva);
      lds_w128(kvl + (XF ? 512 : 2048), kvb);
    };
    if (isV) loop(BoolC<true>{}); else loop(BoolC<false>{});
    STAMP(2);
  }
  bf16x8 qf;
  v4u hk[4], hv[4];
  u32 hko[4], fm[16], pm[8];
#pragma unroll
  for (int j = 0; j < 4; ++j) hko[j] = tt - 4 + j >= 0 ? l16 : h * 512;
  const u32 hvo = hpt >= 0 ? l16 : OOB;
  {
    const u32 xa[4] = {0, 0, 0, 0};
    f32x16 acc;
#pragma unroll
    for (int i = 0; i < 16; ++i) acc[i] = 0.f;
    auto qo = [&](int hs) { return qwa + (wslot(8 + (hs >> 1)) - L_RING) + (hs & 1) * 8192; };
    u32 fla = ld_flag(fao, sws);
    CSTAMP(59);
    auto get_k = [&]() {
      asm volatile("s_waitcnt vmcnt(0)" : "+v"(fla)::"memory");
      wait_flag(fla, fao, sws, tag, spins);
      bar();
      CSTAMP(8);
      ld4s(hk, hko, sws, hks);
      if (isV) { st_sc1(kva, sws, kvo, kvs + 8192 + tb * 4096); st_sc1(kvb, sws, kvo, kvs + 8192 + 512 + tb * 4096); }
      CSTAMP(60);
    };
    gemm4<false, false>(acc, xk[0], wa_, xk[0], wb_, xa, 0, qo(1));
    CSTAMP(61);
    if (SETUP0 <= 2) setup(2);
    CSTAMP(62);
    gemm4<false, false>(acc, xk[1], wb_, xk[0], wa_, xa, 0, qo(2));
    if (HK_AT == 2) get_k();
    if (tid < 160) { const u32 one = (tid >> 5) + tt >= 4 ? 0x3F803F80u : 0u; lds_w128((tid >> 4) * KVT + 8192 + (tid & 15) * 16, v4u{one, one, one, one}); }
    gemm4<false, false>(acc, xk[2], wa_, xk[0], wb_, xa, 0, qo(3));
    CSTAMP(7);
    if (HK_AT == 3) get_k();
    gemm4<false, false>(acc, xk[3], wb_, xk[0], wa_, xa, 0, qo(4));
    if (SETUP0 <= 3) setup(3);
    gemm4<false, false>(acc, xk[4], wa_, xk[0], wb_, xa, 0, qo(5));
    setup(6);
    CSTAMP(9);
    gemm4<false, false>(acc, xk[5], wb_, xk[0], wa_, xa, 0, qo(6));
#pragma unroll
    for (int i = 0; i < 16; ++i) fm[i] = (u32)__builtin_amdgcn_sbfe((int)mbits, i, 1);
#pragma unroll
    for (int k = 0; k < 8; ++k) pm[k] = (fm[2 * k] & 0xFFFFu) | (fm[2 * k + 1] & 0xFFFF0000u);
    asm volatile("" : "+v"(pm[0]), "+v"(pm[1]), "+v"(pm[2]), "+v"(pm[3]), "+v"(pm[4]), "+v"(pm[5]), "+v"(pm[6]), "+v"(pm[7]));
    asm volatile("" : "+v"(fm[0]), "+v"(fm[1]), "+v"(fm[2]), "+v"(fm[3]), "+v"(fm[4]), "+v"(fm[5]), "+v"(fm[6]), "+v"(fm[7]), "+v"(fm[8]), "+v"(fm[9]),
                 "+v"(fm[10]), "+v"(fm[11]), "+v"(fm[12]), "+v"(fm[13]), "+v"(fm[14]), "+v"(fm[15]));
    pin();
    gemm4<false, false>(acc, xk[6], wa_, xk[0], wb_, xa, 0, qo(7));
#pragma unroll
    for (int s = 0; s < 4; ++s) acc = mfma32(wb_.f[s], xk[7].f[s], acc);
    CSTAMP(10);
    const float qs = 0.25f * 1.4426950408889634f;
    qf = asfrag(v4u{cvtpk(acc[0] * qs, acc[1] * qs), cvtpk(acc[2] * qs, acc[3] * qs), cvtpk(acc[4] * qs, acc[5] * qs), cvtpk(acc[6] * qs, acc[7] * qs)});
  }
  STAMP(3);

  u32 flb, b2v;
  v4u xres;
  v2u b1a, b1b;
  bf16x8 wf[4][4];
  {
    const bool ldr = w < 4;
    const v4i swo = mksrd(gwo, 512 * 512 * 2);
    const u32 kb = tb * KVT + hh * 2048 + l16, vb1 = r32 < 16 ? vb0 + 512 : vb0;
    const u32 wovo = (ABL & 4) ? OOB : l8 * 1024 + swz + cg * 128;
    const u32 dov = don ? l16 : OOB, so_xc = so_x + kc * 18432 + (w & 3) * 1024;
    auto bg = [&](int op) {
      if (op < 2) st_sc1(sent4, sws, (ABL & 2) ? OOB : l16, so_o + w * 1024 + op * 32768);
      else if (op == 2) st_sc1(sent4, sws, tid < 32 ? l16 : OOB, so_st + cg * 512);
      else if (op < 7) {
        const int kk = 4 * w + ((op - 3 + loc) & 3);
        dma4<1024, 1024, 1024, 16384, -8192, 16384>(L_WO + kk * 4096, wovo, swo, kk * 32768);
      }
      else if (op < 9) st_sc1(sent4, sws, (ABL & 2) ? OOB : l16, so_hn + (w & 3) * 1024 + (op - 7) * 131072);
      else if (op < 11) st_sc1(sent4, sws, (!(ABL & 2) && (op == 9 ? (ft < 14 || (w & 3) < 2) : (ft < 14 && (w & 3) < 2))) ? dov : OOB, so_g + (w & 3) * 1024 + (op - 9) * 4096);
      else st_sc1(sent4, sws, ((op == 15 && (w & 3) >= 2) || (ABL & 1)) ? OOB : dov, so_xc + (op - 11) * 4096);
    };
    auto hook = [&](int pt) {
      if (LATE47 && !ldr) {
#pragma unroll
        for (int op = 7; op < NBG; ++op) if (BG_AT[op] == pt) bg(op);
      }
      if (ldr) {
#pragma unroll
        for (int op = 0; op < (LATE47 ? 7 : NBG); ++op) if (BG_AT[op] == pt) bg(op);
        if (pt == BG_RDY) { vmwait<bg_after(6, BG_RDY)>(); *(volatile LAS u32*)(uintptr_t)(L_CNT + 4 + 4 * w) = 1; }
        if (pt == BG_SIG) { vmwait<bg_after(2, BG_SIG)>(); signal<4>(L_CNT, lane, rws, WS_FLAGB + wg * 128, tag); }
      }
      if (pt == BG_BAR) {
        volatile LAS u32* rdy = (volatile LAS u32*)(uintptr_t)(L_CNT + 4 + 4 * (w & 3));
        if (!ldr) {
#pragma clang loop unroll(disable)
          while (__builtin_amdgcn_readfirstlane(*rdy) == 0 && ++spins < SPIN_CAP) __builtin_amdgcn_s_sleep(1);
          asm volatile("" ::: "memory");
        }
        xres = aload(xro, mksrd(gx, 2048 * 512 * 2), cg * 128);
        asm volatile("buffer_load_ushort %0, %1, %2, 0 offen" : "=v"(b2v) : "v"((xro >> 10) * 2), "s"(mksrd(gb2, 2048 * 2)) : "memory");
        asm volatile("buffer_load_dwordx2 %0, %2, %3, 0 offen\n\tbuffer_load_dwordx2 %1, %2, %3, 0 offen offset:32" : "=&v"(b1a), "=&v"(b1b) : "v"(don ? (96 * ft + 32 * (s6 >> 1) + 4 * g4) * 2 : OOB), "s"(mksrd(gb1, 1376 * 2)) : "memory");
      }
      if (pt == BG_BAR || pt == BG_BAR + 1) {
#pragma unroll
        for (int k = 2 * (pt - BG_BAR); k < 2 * (pt - BG_BAR) + 2; ++k)
#pragma unroll
          for (int n = 0; n < 4; ++n) { const u32 ad = ((L_WO + (w & 3) * 16384 + tra0) ^ (32 * n)) + k * 4096; wf[k][n] = lds_tr(ad, ad + 2048); }
      }
      if (pt == BG_FLAG && w == FLAGW) flb = ld_flag(fbo, sws);
      pin();
    };
    f32x16 z16;
#pragma unroll
    for (int i = 0; i < 16; ++i) z16[i] = 0.f;
    bf16x8 kf[9];
    kf[8] = asfrag(lds_r128(kb + 8 * KVT));
    f32x16 sb = mfma32(kf[8], qf, z16);
    if (!isV) vmwait<0>();
    take4s<2>(hk, hko, sws, hks, spins);
    CSTAMP(11);
    if (HV_AT == 8) ld4(hv, hvo, sws, hso);
#pragma unroll
    for (int j = 0; j < 4; ++j) { lds_w128(kb + 2 * j * KVT, hk[j]); kf[2 * j] = asfrag(hk[j]); }
    float m4[4] = {-3.0e38f, -3.0e38f, -3.0e38f, -3.0e38f};
    f32x16 sc = mfma32(kf[0], qf, z16), sa = mfma32(kf[2], qf, z16), sd = mfma32(kf[4], qf, z16), se = mfma32(kf[6], qf, z16);
    pin();
    if (HV_AT == 9) ld4(hv, hvo, sws, hso);
    bar();
    STAMP(4);
#pragma unroll
    for (int i = 1; i < 8; i += 2) kf[i] = asfrag(lds_r128(kb + i * KVT));
    pin();
    hook(0);
    f32x16 sm;
#pragma unroll
    for (int i = 0; i < 16; ++i) { const float a = sb[i], b = sc[i]; sm[i] = __uint_as_float((fm[i] & __float_as_uint(a)) | (~fm[i] & __float_as_uint(b))); }
    max16(m4, sm);
    pin(); max16(m4, sa); pin();
    sa = mfma32(kf[1], qf, z16); sb = mfma32(kf[3], qf, z16);
    pin(); max16(m4, sd); pin();
    sc = mfma32(kf[5], qf, z16);
    pin(); max16(m4, se); pin();
    sd = mfma32(kf[7], qf, z16);
    pin(); max16(m4, sa); max16(m4, sb); max16(m4, sc); max16(m4, sd);
    float mx = fmaxf(fmaxf(m4[0], m4[1]), fmaxf(m4[2], m4[3]));
    {
      const u32 a = __float_as_uint(mx);
      const auto sw = __builtin_amdgcn_permlane32_swap(a, a, false, false);
      const u32 s0 = sw[0], s1 = sw[1];
      mx = fmaxf(__uint_as_float(s0), __uint_as_float(s1));
    }
    CSTAMP(12);
    f32x16 nm;
#pragma unroll
    for (int i = 0; i < 16; ++i) nm[i] = -mx;
    f32x16 sn = mfma32(kf[1], qf, nm);
    pin();
    u32 pk[8];
#pragma unroll
    for (int k = 0; k < 8; ++k) pk[k] = cvtpk(__builtin_amdgcn_exp2f(sm[2 * k] - mx), __builtin_amdgcn_exp2f(sm[2 * k + 1] - mx));
    v4u pl0 = {pk[0] & pm[0], pk[1] & pm[1], pk[2] & pm[2], pk[3] & pm[3]}, pl1 = {pk[4] & pm[4], pk[5] & pm[5], pk[6] & pm[6], pk[7] & pm[7]};
    asm volatile("" : "+v"(pl0), "+v"(pl1));
    v4u pf0 = {pk[0] ^ pl0.x, pk[1] ^ pl0.y, pk[2] ^ pl0.z, pk[3] ^ pl0.w}, pf1 = {pk[4] ^ pl1.x, pk[5] ^ pl1.y, pk[6] ^ pl1.z, pk[7] ^ pl1.w};
    asm volatile("" : "+v"(pf0), "+v"(pf1));
    pin();
    if (!ldr) vmwait<0>();
    take4<bg_after(-1, 0)>(hv, hvo, sws, hso, spins);
#pragma unroll
    for (int u = 0; u < 4; ++u) lds_w128(w * KVT + u * 2048 + 1024 + l16, hv[u]);
    CSTAMP(13);
    bar();
    v4u v0 = lds_r128(vb0 + 8 * KVT), v1 = lds_r128(vb1 + 8 * KVT), v2 = lds_r128(vb0), v3 = lds_r128(vb1);
    hook(1);
    f32x16 o = z16;
    CSTAMP(14);
#pragma unroll
    for (int i = 1; i <= 7; ++i) {
      const f32x16 s = sn;
      if (i < 7) sn = mfma32(kf[i + 1], qf, nm);
      v4u va = lds_r128(vb0 + i * KVT), vb = lds_r128(vb1 + i * KVT);
      pin();
      float p[8];
#pragma unroll
      for (int j = 0; j < 8; ++j) p[j] = __builtin_amdgcn_exp2f(s[j]);
      const v4u pa = pack8(p);
      if (i == 1) {
        o = mfma32(asfrag(v0), asfrag(pl0), o);
        o = mfma32(asfrag(v1), asfrag(pl1), o);
        o = mfma32(asfrag(v2), asfrag(pf0), o);
        o = mfma32(asfrag(v3), asfrag(pf1), o);
      }
      o = mfma32(asfrag(va), asfrag(pa), o);
      pin();
      hook(2 * i);
#pragma unroll
      for (int j = 0; j < 8; ++j) p[j] = __builtin_amdgcn_exp2f(s[8 + j]);
      const v4u pb = pack8(p);
      o = mfma32(asfrag(vb), asfrag(pb), o);
      pin();
      hook(2 * i + 1);
      CSTAMP(14 + i);
    }
    const float inv = __builtin_amdgcn_rcpf(o[8]);
    const v4u of = guard4(v4u{cvtpk(o[0] * inv, o[1] * inv), cvtpk(o[2] * inv, o[3] * inv), cvtpk(o[4] * inv, o[5] * inv), cvtpk(o[6] * inv, o[7] * inv)});
    st_sc1(of, sws, ovo, so_o);
  }
  STAMP(5);

  u32 w1l[4], w1g[4];
  auto dma_w1 = [&](int i) { if (w < 6) dma16(w1l[i >> 2] + (i & 3) * 6144, w1vo, sw1, w1g[i >> 2] + (i & 3) * (32 * 2752)); };
  const int dq = w >> 1, kh = w & 1;
  const v4i sw2 = mksrd(gw2, 1376 * 2048 * 2);
  u32 sto, hw, hr, w2vo, w1r[2], hna, xvo, ua0, ua1;
  u32 pb[5], hso4[4], w1o[4], xso, xst, gso, hns;
  auto setup_d = [&]() {
#pragma unroll
    for (int i = 0; i < 4; ++i) { const int g = (i + d2) & 3; w1l[i] = L_W1 + g * 24576 + w * 1024; w1g[i] = w1so + g * (128 * 2752); }
    asm volatile("" : "+s"(w1l[0]), "+s"(w1l[1]), "+s"(w1l[2]), "+s"(w1l[3]), "+s"(w1g[0]), "+s"(w1g[1]), "+s"(w1g[2]), "+s"(w1g[3]));
    sto = c8 * 512 + (8 * w + l8) * 8;
    asm volatile("" : "+v"(sto));
    pin();
  };
  auto setup_d2 = [&]() {
    hw = L_PC + w * 2048 + l8 * 128 + swz;
    hr = ((L_PC + (w >> 2) * 8192 + (tra0 & 1023) + (lane >> 5) * 2048) ^ (32 * (w & 3)));
    w2vo = (ABL & 16) ? OOB : l8 * 4096 + swz + tt * 128;
#pragma unroll
    for (int e = 0; e < 2; ++e) w1r[e] = L_W1 + kh * 49152 + (lane >> 2) * 192 + (((2 * e + (p4 >> 1)) ^ ((g4 & 1) << 1)) * 16) + 8 * (p4 & 1);
    hna = l16 + kh * 1024;
    const int sj = (s6 >> 1) & 3, sm_ = s6 & 1;
    xvo = (dq != kc && (ft < 14 || s6 < 2)) ? l16 : OOB;
    ua0 = L_PD + 49152 + sm_ * 6144 + (sj < 3 ? sj : 0) * 2048 + l16;
    ua1 = L_PD + (2 * kc + 1 - sm_) * 6144 + (sj < 3 ? sj : 0) * 2048 + l16;
    asm volatile("" : "+v"(hw), "+v"(hr), "+v"(w2vo), "+v"(w1r[0]), "+v"(w1r[1]), "+v"(hna), "+v"(xvo), "+v"(ua0), "+v"(ua1));
    hns = so_hn + ((w >> 1) & 1) * 131072 + (w >> 2) * 2048 + (w & 1) * 1024;
    asm volatile("" : "+s"(hns));
    pin();
  };
  auto setup_d3 = [&]() {
    const int rr = EROT == 1 ? tt + cg : EROT == 2 ? (cg & 3) : EROT == 3 ? (cg & 3) + (tt >> 3) : 0, r3 = rr % 3, r2 = rr & 1;
#pragma unroll
    for (int b = 0; b < 5; ++b) pb[b] = 11 * dq + 2 * (b < 3 ? (b + r3 >= 3 ? b + r3 - 3 : b + r3) : 3 + ((b - 3) ^ r2));
    const u32 so = WS_HN + (2 * d2 + (dq >> 1)) * 262144 + (dq & 1) * 131072 + (16 * kc + 8 * kh) * 2048;
#pragma unroll
    for (int i = 0; i < 4; ++i) { const int g = (i + (loc >> 2)) & 3; hso4[i] = so + g * 4096; w1o[i] = g * 12288; }
    xso = so_x + kc * 18432 + (dq != kc ? s6 : 0) * 3072;
    xst = so_x + dq * 18432 + kh * 3072 + ((kc - dq - 1) & 3) * 1024;
    gso = so_g + (dq != kc ? s6 * 1024 : 0);
    asm volatile("" : "+s"(pb[0]), "+s"(pb[1]), "+s"(pb[2]), "+s"(pb[3]), "+s"(pb[4]), "+s"(hso4[0]), "+s"(hso4[1]), "+s"(hso4[2]), "+s"(hso4[3]),
                 "+s"(w1o[0]), "+s"(w1o[1]), "+s"(w1o[2]), "+s"(w1o[3]), "+s"(xso), "+s"(xst), "+s"(gso));
    pin();
  };
  v4u hres;
  {
    const u32 oso = WS_O + tt * 65536 + (w >> 2) * 32768 + (w & 3) * 8192;
    if (w == FLAGW) {
      asm volatile("s_waitcnt vmcnt(1)" : "+v"(flb)::"memory");
      wait_flag(flb, fbo, sws, tag, spins);
    }
    CSTAMP(22);
    bar();
    pace<D_O, 0>();
    v4u ofr[2][4];
    ld4(ofr[0], l16, sws, oso);
    ld4(ofr[1], l16, sws, oso + 4096);
    STAMP(6);
    setup(5); setup_d();
    f32x4 acc[2][4];
#pragma unroll
    for (int m = 0; m < 2; ++m)
#pragma unroll
      for (int n = 0; n < 4; ++n) acc[m][n] = f32x4{0.f, 0.f, 0.f, 0.f};
#pragma unroll
    for (int k = 0; k < 4; ++k) {
      if (k == 0) take4<4>(ofr[0], l16, sws, oso, spins);
      if (k == 2) {
        take4<0>(ofr[1], l16, sws, oso + 4096, spins);
        asm volatile("" : "+v"(xres), "+v"(b2v), "+v"(b1a), "+v"(b1b));
#pragma unroll
        for (int i = 0; i < W1_0; ++i) dma_w1(i);
      }
#pragma unroll
      for (int m = 0; m < 2; ++m)
#pragma unroll
        for (int n = 0; n < 4; ++n) acc[m][n] = mfma16(wf[k][n], asfrag(ofr[k >> 1][2 * (k & 1) + m]), acc[m][n]);
      pin();
    }
#pragma unroll
    for (int i = W1_0; i < W1_A; ++i) dma_w1(i);
    STAMP(7);
#pragma unroll
    for (int m = 0; m < 2; ++m)
#pragma unroll
      for (int n = 0; n < 4; ++n) lds_wf4(((L_PC + (w & 3) * 16384 + (w >> 2) * 8192 + pcw0) ^ (64 * n)) + m * 4096, acc[m][n]);
#pragma unroll
    for (int i = W1_A; i < W1_B; ++i) dma_w1(i);
    bar();
    if (tid == 448) __builtin_amdgcn_raw_buffer_store_b32(tag, rws, WS_FLAGC + wg * 128, 0, 16);
    CSTAMP(23);
    f32x4 t[8];
#pragma unroll
    for (int k = 0; k < 4; ++k) { t[2 * k] = lds_rf4(pcr + k * 16384); t[2 * k + 1] = lds_rf4((pcr ^ 16) + k * 16384); }
    pin();
    hres = pack44(unlo(xres) + ((t[0] + t[2]) + (t[4] + t[6])), unhi(xres) + ((t[1] + t[3]) + (t[5] + t[7])));
  }
  {
    float q1 = dot2(hres.x, 0x3F803F80u, 0.f), q2 = dot2(hres.x, hres.x, 0.f), q3 = dot2(hres.y, 0x3F803F80u, 0.f), q4_ = dot2(hres.y, hres.y, 0.f);
    q1 = dot2(hres.z, 0x3F803F80u, q1); q2 = dot2(hres.z, hres.z, q2); q3 = dot2(hres.w, 0x3F803F80u, q3); q4_ = dot2(hres.w, hres.w, q4_);
    q1 += q3; q2 += q4_;
    q1 += dpp<0xB1>(q1); q2 += dpp<0xB1>(q2);
    q1 += dpp<0x4E>(q1); q2 += dpp<0x4E>(q2);
    q1 += dpp<0x141>(q1); q2 += dpp<0x141>(q2);
    const v2u stv = {guard(__float_as_uint(q1)), guard(__float_as_uint(q2))};
    asm volatile("buffer_store_dwordx2 %0, %1, %2, %3 offen sc1" ::"v"(stv), "v"(c8 == 0 ? sto : OOB), "s"(sws), "s"(so_st + cg * 512) : "memory");
    STAMP(8);
#pragma unroll
    for (int i = W1_B; i < W1_C; ++i) dma_w1(i);
    setup(4); setup_d2();
    f32x4 h0 = unlo(hres), h1 = unhi(hres);
    asm volatile("" : "+v"(h0), "+v"(h1));
    if (D_ST < 0 && w >= 6) vmwait<0>();
    pace<D_ST, W1_C - W1_B>();
    u32 flc = ld_flag(fco, sws);
    v2u st;
    asm volatile("buffer_load_dwordx2 %0, %1, %2, %3 offen sc1" : "=v"(st) : "v"(sto), "s"(sws), "s"(so_st) : "memory");
#pragma unroll
    for (int i = W1_C; i < W1_D; ++i) dma_w1(i);
    if (w >= 6) vmwait<0>();
    asm volatile("s_waitcnt vmcnt(%2)" : "+v"(st), "+v"(flc) : "n"(W1_D - W1_C) : "memory");
    if (__builtin_expect(any_sent(st.x > st.y ? st.x : st.y), 0)) {
#pragma clang loop unroll(disable)
      do {
        asm volatile("buffer_load_dwordx2 %0, %1, %2, %3 offen sc1\n\ts_waitcnt vmcnt(0)" : "+v"(st) : "v"(sto), "s"(sws), "s"(so_st) : "memory");
      } while (any_sent(st.x > st.y ? st.x : st.y) && ++spins < SPIN_CAP);
    }
    STAMP(9);
    const u32 sx_ = st.x, sy_ = st.y;
    float m1 = __uint_as_float(sx_), m2 = __uint_as_float(sy_);
    m1 += dpp<0xB1>(m1); m2 += dpp<0xB1>(m2);
    m1 += dpp<0x4E>(m1); m2 += dpp<0x4E>(m2);
    m1 += dpp<0x141>(m1); m2 += dpp<0x141>(m2);
    const float mean = m1 * (1.f / 512.f), var = fmaxf(m2 * (1.f / 512.f) - mean * mean, 0.f);
    const float r1 = __builtin_amdgcn_rsqf(var + 1e-5f);
    const float r2 = __builtin_amdgcn_rsqf(var * r1 * r1 + 1e-5f);
    h0 -= mean; h1 -= mean;
    const float sc = r1 * r2;
    const v4u hn = pack44(h0 * sc, h1 * sc);
    lds_w128(hw, hn);
    wait_flag(flc, fco, sws, tag, spins);
    bar();
    const v4u fr = guard4(__builtin_bit_cast(v4u, lds_tr(hr, hr + 2 * 2048)));
    st_sc1(fr, sws, l16, hns);
    STAMP(10);
    if (wg == 0 && tid == 448) __builtin_amdgcn_raw_buffer_store_b32(tag, rws, WS_EPOCH, 0, 16);
#pragma unroll
    for (int i = W1_D; i < 16; ++i) dma_w1(i);
    setup_d3();
  }
  const int eq = dq, eh = kh;
  auto w2slot = [&](int b) { return b < 3 ? L_W2A + eq * 24576 + (2 * b + eh) * 4096 : eq * 16384 + (2 * (b - 3) + eh) * 4096; };
  auto issue_w2 = [&](int b) { dma4<1024, 1024, 1024, 32768, 32768, 32768>(w2slot(b), w2vo, sw2, (pb[b] + eh) * 131072); };
  if (don) {
    f32x4 acc[6][2];
#pragma unroll
    for (int b = 0; b < 6; ++b)
#pragma unroll
      for (int m = 0; m < 2; ++m) acc[b][m] = f32x4{0.f, 0.f, 0.f, 0.f};
    const u32 hnb = hna ^ 1024;
    v4u hf[4][4];
    vmwait<0>();
    bar();
    pace<D_HN, 0>();
    ld4x(hf[0], hna, hnb, sws, hso4[0]);
    ld4x(hf[1], hna, hnb, sws, hso4[1]);
    CSTAMP(25);
    bf16x8 wc[6], wn[6];
    v4u w2r[3][4], w2l[8];
    auto raw_w2 = [&](int i) {
#pragma unroll
      for (int u = 0; u < 4; ++u) w2r[i][u] = aload(w2vo, sw2, (pb[i] + eh) * 131072 + u * 32768);
    };
    const u32 w2w = w2slot(0) + l16;
    {
      u32 a0 = w1r[0] + w1o[0], a1 = w1r[1] + w1o[0];
      asm volatile("" : "+v"(a0), "+v"(a1));
#pragma unroll
      for (int b = 0; b < 6; ++b) { const u32 ad = ((b & 1) ? a1 : a0) + (b >> 1) * 64; wc[b] = lds_tr(ad, ad + 16 * 192); }
    }
#pragma unroll
    for (int s = 0; s < 8; ++s) {
      if (s == 0) { take4x<4>(hf[0], hna, hnb, sws, hso4[0], spins); ld4x(hf[2], hna, hnb, sws, hso4[2]); }
      if (s == 2) { take4x<4>(hf[1], hna, hnb, sws, hso4[1], spins); ld4x(hf[3], hna, hnb, sws, hso4[3]); }
      if (s == 4) { take4x<4>(hf[2], hna, hnb, sws, hso4[2], spins); raw_w2(0); }
      if (s == 6) { take4x<4>(hf[3], hna, hnb, sws, hso4[3], spins); raw_w2(1); }
      if (s == 7) raw_w2(2);
      const u32 o = w1o[(s + 1) >> 1 & 3] + ((s + 1) & 1) * 6144;
      u32 a0 = w1r[0] + o, a1 = w1r[1] + o;
      asm volatile("" : "+v"(a0), "+v"(a1));
#pragma unroll
      for (int b = 0; b < 6; ++b) {
#pragma unroll
        for (int m = 0; m < 2; ++m) acc[b][m] = mfma16(wc[b], asfrag(hf[s >> 1][2 * (s & 1) + m]), acc[b][m]);
        if (s < 7) { const u32 ad = ((b & 1) ? a1 : a0) + (b >> 1) * 64; wn[b] = lds_tr(ad, ad + 16 * 192); }
        pin();
      }
#pragma unroll
      for (int b = 0; b < 6; ++b) wc[b] = wn[b];
      CSTAMP(26 + s);
    }
    STAMP(11);
    const u32 pd = L_PD + w * 6144 + l16;
#pragma unroll
    for (int b = 0; b < 6; ++b) lds_wf4(pd + b * 1024, acc[b][1]);
    bar();
    CSTAMP(34);
    if (dq == kc) {
#pragma unroll
      for (int b = 0; b < 6; ++b) lds_wf4(L_PD + 49152 + kh * 6144 + l16 + b * 1024, acc[b][0]);
    } else {
      f32x4 t[6];
#pragma unroll
      for (int b = 0; b < 6; ++b) t[b] = lds_rf4(L_PD + (w ^ 1) * 6144 + l16 + b * 1024);
      pin();
#pragma unroll
      for (int j = 0; j < 3; ++j)
        st_sc1(guard4(pack44(acc[2 * j][0] + t[2 * j], acc[2 * j + 1][0] + t[2 * j + 1])), sws, (j > 0 && ft == 14) ? OOB : l16, xst + j * 6144);
    }
    auto late_so = [&](int t) { return (pb[3 + (t >> 2)] + eh) * 131072 + (t & 3) * 32768; };
#pragma unroll
    for (int t = 0; t < NA; ++t) w2l[t] = aload(w2vo, sw2, late_so(t));
    CSTAMP(35);
    bar();
    f32x4 u0 = lds_rf4(ua0), u1 = lds_rf4(ua0 + 1024), u2 = lds_rf4(ua1), u3 = lds_rf4(ua1 + 1024);
    if (dq == kc) vmwait<NA>();
    asm volatile("s_waitcnt vmcnt(%12)" : "+v"(w2r[0][0]), "+v"(w2r[0][1]), "+v"(w2r[0][2]), "+v"(w2r[0][3]), "+v"(w2r[1][0]), "+v"(w2r[1][1]), "+v"(w2r[1][2]),
                 "+v"(w2r[1][3]), "+v"(w2r[2][0]), "+v"(w2r[2][1]), "+v"(w2r[2][2]), "+v"(w2r[2][3]) : "n"(3 + NA) : "memory");
#pragma unroll
    for (int i = 0; i < 3; ++i)
#pragma unroll
      for (int u = 0; u < 4; ++u) lds_w128(w2w + i * 8192 + u * 1024, w2r[i][u]);
    pin();
    pace<D_XP, NA>();
    v4u xp[3];
    ld3(xp, xvo, sws, xso);
#pragma unroll
    for (int t = NA; t < NA + NB; ++t) w2l[t] = aload(w2vo, sw2, late_so(t));
    u0 += u2 + f32x4{bflo(b1a.x), bfhi(b1a.x), bflo(b1a.y), bfhi(b1a.y)};
    u1 += u3 + f32x4{bflo(b1b.x), bfhi(b1b.x), bflo(b1b.y), bfhi(b1b.y)};
    asm volatile("" : "+v"(u0), "+v"(u1));
    take3<NB>(xp, xvo, sws, xso, spins);
    CSTAMP(38);
    {
      const f32x4 a0 = unlo(xp[0]), a1 = unlo(xp[1]), a2 = unlo(xp[2]), b0 = unhi(xp[0]), b1 = unhi(xp[1]), b2 = unhi(xp[2]);
      pin();
      u0 = (u0 + a0) + (a1 + a2); u1 = (u1 + b0) + (b1 + b2);
      pin();
    }
    const v4u gv = guard4(gelu8(u0, u1));
    st_sc1(gv, sws, xvo, gso);
    STAMP(12);
    bar();
#pragma unroll
    for (int t = NA + NB; t < 8; ++t) dma16(w2slot(3 + (t >> 2)) + (t & 3) * 1024, w2vo, sw2, late_so(t));
    vmwait<9 - NA - NB>();
#pragma unroll
    for (int t = 0; t < NA + NB; ++t) {
      asm volatile("" : "+v"(w2l[t]));
      lds_w128(w2slot(3 + (t >> 2)) + (t & 3) * 1024 + l16, w2l[t]);
    }
  } else {
    vmwait<0>();
    bar();
    const u32 p = (xcd - 4) * 4 + (loc & 3);
#pragma clang loop unroll(disable)
    for (int i = 0; i < W2PF; ++i) {
      dma16(w * 4096, l16, sw2, (i < 16 ? 11 * (i & 3) + 6 + (i >> 2) : 11 * (i - 16) + 10) * 131072 + p * 8192 + w * 1024);
      vmwait<6>();
    }
    vmwait<0>();
    bar();
    issue_w2(0); issue_w2(1); issue_w2(2);
    vmwait<0>();
    bar();
    issue_w2(3); issue_w2(4);
    STAMP(12);
  }
  {
    f32x4 acc[2][4];
#pragma unroll
    for (int m = 0; m < 2; ++m)
#pragma unroll
      for (int n = 0; n < 4; ++n) acc[m][n] = f32x4{0.f, 0.f, 0.f, 0.f};
    const u32 so = WS_G + (cg * 2 + eh) * (43 * 2048);
    auto gso = [&](int b) { return so + (b < 5 ? pb[b < 5 ? b : 0] : 11 * eq + 10) * 2048; };
    u32 wa_[4], wl_[4], pew[2];
#pragma unroll
    for (int n = 0; n < 4; ++n) { wa_[n] = (tra0 + L_W2A + eq * 24576) ^ (32 * n); wl_[n] = (tra0 + eq * 16384) ^ (32 * n); }
#pragma unroll
    for (int m = 0; m < 2; ++m) pew[m] = (L_W2A + eq * 24576 + 8192 + pcw0) ^ (128 * eh + 64 * m);
    u32 per = L_W2A + 8192 + pcr, outo = xro + cg * 128;
    asm volatile("" : "+v"(wa_[0]), "+v"(wa_[1]), "+v"(wa_[2]), "+v"(wa_[3]), "+v"(wl_[0]), "+v"(wl_[1]), "+v"(wl_[2]), "+v"(wl_[3]),
                 "+v"(pew[0]), "+v"(pew[1]), "+v"(per), "+v"(outo));
    auto rd_w2 = [&](int L, int n) {
      const u32 ad = L < 6 ? wa_[n] + L * 4096 : L < 10 ? wl_[n] + (L - 6) * 4096 : wa_[n] + eh * 4096;
      return lds_tr(ad, ad + 2048);
    };
    v4u gf[3][4];
    bf16x8 wc[4], wn[4];
#pragma unroll
    for (int n = 0; n < 4; ++n) wc[n] = rd_w2(0, n);
    pace<D_G, 8 - NA - NB>();
    CSTAMP(36);
    ld4(gf[0], l16, sws, gso(0));
    ld4(gf[1], l16, sws, gso(1));
    CSTAMP(37);
#pragma unroll
    for (int b = 0; b < 6; ++b) {
      if (b < 5 || eq < 3) {
        if (eq == 3) { if (b == 1 || b == 2) vmwait<4>(); else if (b == 4) vmwait<0>(); }
        if (b == 1 || b == 2) take4<8>(gf[b % 3], l16, sws, gso(b), spins);
        else if (b < 5) take4<4>(gf[b % 3], l16, sws, gso(b), spins);
        else take4<0>(gf[b % 3], l16, sws, gso(b), spins);
        if (b < 3 || (b == 3 && eq < 3)) ld4(gf[(b + 2) % 3], l16, sws, gso(b + 2));
#pragma unroll
        for (int e = 0; e < (b < 5 ? 2 : 1); ++e) {
          const int L = 2 * b + e;
#pragma unroll
          for (int n = 0; n < 4; ++n) {
#pragma unroll
            for (int m = 0; m < 2; ++m) acc[m][n] = mfma16(asfrag(gf[b % 3][2 * e + m]), wc[n], acc[m][n]);
            if (L < 10) wn[n] = rd_w2(L + 1, n);
            pin();
          }
#pragma unroll
          for (int n = 0; n < 4; ++n) wc[n] = wn[n];
        }
      }
      if (b == 0) {
        bar();
        if (eq < 3) dma4<1024, 1024, 1024, 32768, 32768, 32768>(L_W2A + eq * 24576 + eh * 4096, w2vo, sw2, (11 * eq + 10) * 131072);
      }
      if (b == 2) bar();
      pin();
      CSTAMP(52 + b);
    }
    STAMP(13);
#pragma unroll
    for (int n = 0; n < 4; ++n)
#pragma unroll
      for (int m = 0; m < 2; ++m) lds_wf4(pew[m] + n * 4096, acc[m][n]);
    const float bb = bflo(b2v);
    f32x4 s0 = unlo(hres) + bb, s1 = unhi(hres) + bb;
    u32 per1 = per ^ 16, per2 = per + 49152, per3 = per2 ^ 16;
    asm volatile("" : "+v"(s0), "+v"(s1), "+v"(per1), "+v"(per2), "+v"(per3));
    bar();
    CSTAMP(58);
    f32x4 t[8];
#pragma unroll
    for (int k = 0; k < 2; ++k) {
      t[2 * k] = lds_rf4(per + k * 24576); t[2 * k + 1] = lds_rf4(per1 + k * 24576);
      t[4 + 2 * k] = lds_rf4(per2 + k * 24576); t[5 + 2 * k] = lds_rf4(per3 + k * 24576);
    }
    pin();
    s0 += (t[0] + t[2]) + (t[4] + t[6]);
    s1 += (t[1] + t[3]) + (t[5] + t[7]);
    const v4u ov = pack44(s0, s1);
    asm volatile(".Lmega_pf_end:\n\tbuffer_store_dwordx4 %0, %1, %2, 0 offen" ::"v"(ov), "v"(outo), "s"(mksrd(gout, 2048 * 512 * 2)) : "memory");
  }
  STAMP(14);
}

extern "C" void kernel_launch(void* const* d_in, const int* in_sizes, int n_in, void* d_out, int out_size, void* d_ws, size_t ws_size,
                              hipStream_t stream) {
  (void)hipFuncSetAttribute((const void*)mega, hipFuncAttributeMaxDynamicSharedMemorySize, LDS_BYTES);
  mega<<<256, 512, LDS_BYTES, stream>>>((const u16*)d_in[0], (const u16*)d_in[1], (const u16*)d_in[2], (const u16*)d_in[4], (const u16*)d_in[5],
                                        (const u16*)d_in[6], (const u16*)d_in[7], (u16*)d_out, (char*)d_ws);
}
```

```cpp
#include <hip/hip_runtime.h>
#include <stdint.h>

typedef unsigned u32;
typedef unsigned short u16;
typedef u32 v4u __attribute__((ext_vector_type(4)));
typedef u32 v2u __attribute__((ext_vector_type(2)));
typedef int v4i __attribute__((ext_vector_type(4)));
typedef float f32x2 __attribute__((ext_vector_type(2)));
typedef float f32x4 __attribute__((ext_vector_type(4)));
typedef float f32x16 __attribute__((ext_vector_type(16)));
typedef short bf16x8 __attribute__((ext_vector_type(8)));
typedef short s16x4 __attribute__((ext_vector_type(4)));
typedef __bf16 bf2 __attribute__((ext_vector_type(2)));
#define LAS __attribute__((address_space(3)))
#define DI __device__ __forceinline__

#define ABL 0
#define CODE_PF 1
#define STAMPS 0
#define D_KV 0
#define CLR_POS 2
#define HK_AT 3
#define HV_AT 9
#define W1_0 3
#define W1_A 3
#define W1_B 6
#define W1_C 8
#define W1_D 10
#define NA 0
#define NB 0
#define NR 2
#define B2_AT 0
#define W2PF 19
#define EROT 3
#define D_O 4
#define D_ST -1
#define D_HN -1
#define D_XP -1
#define D_G -1
#define BG_TABLE 0, 0, 0,   1, 1, 2, 3,   4, 5, 6, 7, 8, 9, 10, 11, 11
#define LATE47 0
#define FLAGW 4
#define BG_SIG 4
#define BG_BAR 13
#define BG_RDY 8
#define BG_FLAG 14
constexpr int NBG = 16;
constexpr int BG_AT[NBG] = {BG_TABLE};
constexpr int bg_after(int op0, int pt) { int n = 0; for (int op = op0 + 1; op < (LATE47 ? 7 : NBG); ++op) n += BG_AT[op] <= pt ? (op >= 3 && op < 7 ? 4 : 1) : 0; return n; }
constexpr bool bg_ok() { for (int op = 1; op < NBG; ++op) if (BG_AT[op] < BG_AT[op - 1]) return false; return BG_AT[2] <= BG_SIG && BG_AT[6] <= BG_RDY && BG_RDY <= BG_BAR && BG_BAR <= BG_FLAG && BG_AT[3] > 0; }
static_assert(bg_ok(), "the table lists the transfers in issue order");
#define SETUP0 2
template <bool B> struct BoolC { static constexpr bool value = B; };

constexpr u32 WS_EPOCH = 0, WS_FLAGA = 1u << 19, WS_FLAGB = WS_FLAGA + 32768, WS_FLAGC = WS_FLAGB + 32768, WS_DBG = 65536;
constexpr u32 WS_KV = 1u << 20;
constexpr u32 WS_O = WS_KV + (4u << 20);
constexpr u32 WS_HN = WS_O + (2u << 20);
constexpr u32 WS_G = WS_HN + (2u << 20);
constexpr u32 WS_ST = WS_G + (2u << 20);
constexpr u32 WS_XP = WS_ST + (1u << 17);
constexpr u32 WS_END = WS_XP + 240 * 18432;
constexpr u32 SENT = 0xFFFFFFFFu;
constexpr u32 OOB = 0x7FFF0000u;
constexpr int SPIN_CAP = 1 << 16;

constexpr u32 L_XN = 0;
constexpr u32 KVT = 8448;
constexpr u32 L_CNT = 85504;
constexpr u32 L_RING = 86016;
constexpr u32 wslot(int st) { return st < 4 ? L_RING + st * 16384 : st == 4 ? 65536 : L_RING + ((st - 5) & 3) * 16384; }
constexpr u32 L_JUNK = L_RING + 65536;
constexpr u32 L_WO = 98304;
constexpr u32 L_PC = 0;
constexpr u32 L_W1 = 65536;
constexpr u32 L_PD = 0;
constexpr u32 L_W2A = 65536;
constexpr u32 LDS_BYTES = 163840;

DI u32 cvtpk(float a, float b) { f32x2 v = {a, b}; bf2 r = __builtin_convertvector(v, bf2); return __builtin_bit_cast(u32, r); }
DI float dot2(u32 a, u32 b, float c) { return __builtin_amdgcn_fdot2_f32_bf16(__builtin_bit_cast(bf2, a), __builtin_bit_cast(bf2, b), c, false); }
DI float bflo(u32 x) { return __uint_as_float(x << 16); }
DI float bfhi(u32 x) { return __uint_as_float(x & 0xffff0000u); }
DI u32 guard(u32 x) { return x < 0xFFFFFFFEu ? x : 0xFFFFFFFEu; }
DI v4u guard4(v4u v) { v4u r = {guard(v.x), guard(v.y), guard(v.z), guard(v.w)}; return r; }
DI u32 umax3(u32 a, u32 b, u32 c) { u32 m = a > b ? a : b; return m > c ? m : c; }
DI u32 smax4(u32 m, v4u v) { return umax3(umax3(m, v.x, v.y), v.z, v.w); }
DI u32 vmax(v4u v) { return umax3(v.x > v.y ? v.x : v.y, v.z, v.w); }
DI u32 chk4(const v4u (&f)[4]) { return umax3(smax4(vmax(f[0]), f[1]), vmax(f[2]), vmax(f[3])); }
DI bool any_sent(u32 m) { return __builtin_amdgcn_ballot_w64(m == SENT) != 0; }
DI bf16x8 asfrag(v4u v) { return __builtin_bit_cast(bf16x8, v); }
DI f32x16 mfma32(bf16x8 a, bf16x8 b, f32x16 c) { return __builtin_amdgcn_mfma_f32_32x32x16_bf16(a, b, c, 0, 0, 0); }
DI f32x4 mfma16(bf16x8 a, bf16x8 b, f32x4 c) { return __builtin_amdgcn_mfma_f32_16x16x32_bf16(a, b, c, 0, 0, 0); }
DI v4u lds_r128(u32 a) { return *(const LAS v4u*)(uintptr_t)a; }
DI f32x4 lds_rf4(u32 a) { return *(const LAS f32x4*)(uintptr_t)a; }
DI void lds_w128(u32 a, v4u v) { *(LAS v4u*)(uintptr_t)a = v; }
DI void lds_wf4(u32 a, f32x4 v) { *(LAS f32x4*)(uintptr_t)a = v; }
DI bf16x8 lds_tr(u32 a0, u32 a1) {
  s16x4 lo = __builtin_amdgcn_ds_read_tr16_b64_v4i16((LAS s16x4*)(uintptr_t)a0);
  s16x4 hi = __builtin_amdgcn_ds_read_tr16_b64_v4i16((LAS s16x4*)(uintptr_t)a1);
  return __builtin_shufflevector(lo, hi, 0, 1, 2, 3, 4, 5, 6, 7);
}
template <int CTRL> DI float dpp(float v) { return __int_as_float(__builtin_amdgcn_update_dpp(0, __float_as_int(v), CTRL, 0xf, 0xf, true)); }
DI float sum8(float v) { v += dpp<0xB1>(v); v += dpp<0x4E>(v); v += dpp<0x141>(v); return v; }
DI v4i mksrd(const void* p, u32 bytes) { v4i s = {(int)(u32)(uintptr_t)p, (int)(u32)((uintptr_t)p >> 32), (int)bytes, 0x00020000}; return s; }
DI __amdgpu_buffer_rsrc_t mkrs(const void* p, u32 bytes) { return __builtin_amdgcn_make_buffer_rsrc((void*)p, 0, bytes, 0x00020000); }
DI void dma16(u32 lds, u32 voff, v4i srd, u32 soff) {
  asm volatile("s_mov_b32 m0, %0\n\ts_nop 0\n\tbuffer_load_dwordx4 %1, %2, %3 offen lds" ::"s"(lds), "v"(voff), "s"(srd), "s"(soff) : "memory");
}
DI v4u aload_sc1(u32 voff, v4i srd, u32 soff) {
  v4u r;
  asm volatile("buffer_load_dwordx4 %0, %1, %2, %3 offen sc1" : "=v"(r) : "v"(voff), "s"(srd), "s"(soff) : "memory");
  return r;
}
DI v4u aload(u32 voff, v4i srd, u32 soff) {
  v4u r;
  asm volatile("buffer_load_dwordx4 %0, %1, %2, %3 offen" : "=v"(r) : "v"(voff), "s"(srd), "s"(soff) : "memory");
  return r;
}
DI void ld4(v4u (&f)[4], u32 voff, v4i srd, u32 soff) {
  asm volatile("buffer_load_dwordx4 %0, %4, %5, %6 offen sc1\n\tbuffer_load_dwordx4 %1, %4, %5, %6 offen offset:1024 sc1\n\t"
               "buffer_load_dwordx4 %2, %4, %5, %6 offen offset:2048 sc1\n\tbuffer_load_dwordx4 %3, %4, %5, %6 offen offset:3072 sc1"
               : "=&v"(f[0]), "=&v"(f[1]), "=&v"(f[2]), "=&v"(f[3]) : "v"(voff), "s"(srd), "s"(soff) : "memory");
}
DI void ld4x(v4u (&f)[4], u32 va, u32 vb, v4i srd, u32 soff) {
  asm volatile("buffer_load_dwordx4 %0, %4, %6, %7 offen sc1\n\tbuffer_load_dwordx4 %1, %5, %6, %7 offen sc1\n\t"
               "buffer_load_dwordx4 %2, %4, %6, %7 offen offset:2048 sc1\n\tbuffer_load_dwordx4 %3, %5, %6, %7 offen offset:2048 sc1"
               : "=&v"(f[0]), "=&v"(f[1]), "=&v"(f[2]), "=&v"(f[3]) : "v"(va), "v"(vb), "s"(srd), "s"(soff) : "memory");
}
template <int N> DI void take4x(v4u (&f)[4], u32 va, u32 vb, v4i srd, u32 soff, int& spins) {
  asm volatile("s_waitcnt vmcnt(%4)" : "+v"(f[0]), "+v"(f[1]), "+v"(f[2]), "+v"(f[3]) : "n"(N) : "memory");
  if (__builtin_expect(any_sent(chk4(f)), 0)) {
#pragma clang loop unroll(disable)
    do {
      asm volatile("s_sleep 1\n\tbuffer_load_dwordx4 %0, %4, %6, %7 offen sc1\n\tbuffer_load_dwordx4 %1, %5, %6, %7 offen sc1\n\t"
                   "buffer_load_dwordx4 %2, %4, %6, %7 offen offset:2048 sc1\n\tbuffer_load_dwordx4 %3, %5, %6, %7 offen offset:2048 sc1\n\ts_waitcnt vmcnt(0)"
                   : "+v"(f[0]), "+v"(f[1]), "+v"(f[2]), "+v"(f[3]) : "v"(va), "v"(vb), "s"(srd), "s"(soff) : "memory");
    } while (any_sent(chk4(f)) && ++spins < SPIN_CAP);
  }
}
DI void ld4s(v4u (&f)[4], const u32 (&voff)[4], v4i srd, const u32 (&so)[4]) {
  asm volatile("buffer_load_dwordx4 %0, %4, %8, %9 offen sc1\n\tbuffer_load_dwordx4 %1, %5, %8, %10 offen sc1\n\t"
               "buffer_load_dwordx4 %2, %6, %8, %11 offen sc1\n\tbuffer_load_dwordx4 %3, %7, %8, %12 offen sc1"
               : "=&v"(f[0]), "=&v"(f[1]), "=&v"(f[2]), "=&v"(f[3])
               : "v"(voff[0]), "v"(voff[1]), "v"(voff[2]), "v"(voff[3]), "s"(srd), "s"(so[0]), "s"(so[1]), "s"(so[2]), "s"(so[3]) : "memory");
}
template <int N> DI void take4s(v4u (&f)[4], const u32 (&voff)[4], v4i srd, const u32 (&so)[4], int& spins) {
  asm volatile("s_waitcnt vmcnt(%4)" : "+v"(f[0]), "+v"(f[1]), "+v"(f[2]), "+v"(f[3]) : "n"(N) : "memory");
  if (__builtin_expect(any_sent(chk4(f)), 0)) {
#pragma clang loop unroll(disable)
    do {
      asm volatile("s_sleep 1\n\tbuffer_load_dwordx4 %0, %4, %8, %9 offen sc1\n\tbuffer_load_dwordx4 %1, %5, %8, %10 offen sc1\n\t"
                   "buffer_load_dwordx4 %2, %6, %8, %11 offen sc1\n\tbuffer_load_dwordx4 %3, %7, %8, %12 offen sc1\n\ts_waitcnt vmcnt(0)"
                   : "+v"(f[0]), "+v"(f[1]), "+v"(f[2]), "+v"(f[3])
                   : "v"(voff[0]), "v"(voff[1]), "v"(voff[2]), "v"(voff[3]), "s"(srd), "s"(so[0]), "s"(so[1]), "s"(so[2]), "s"(so[3]) : "memory");
    } while (any_sent(chk4(f)) && ++spins < SPIN_CAP);
  }
}
template <int N> DI void take4(v4u (&f)[4], u32 voff, v4i srd, u32 soff, int& spins) {
  asm volatile("s_waitcnt vmcnt(%4)" : "+v"(f[0]), "+v"(f[1]), "+v"(f[2]), "+v"(f[3]) : "n"(N) : "memory");
  if (__builtin_expect(any_sent(chk4(f)), 0)) {
#pragma clang loop unroll(disable)
    do {
      asm volatile("s_sleep 1\n\tbuffer_load_dwordx4 %0, %4, %5, %6 offen sc1\n\tbuffer_load_dwordx4 %1, %4, %5, %6 offen offset:1024 sc1\n\t"
                   "buffer_load_dwordx4 %2, %4, %5, %6 offen offset:2048 sc1\n\tbuffer_load_dwordx4 %3, %4, %5, %6 offen offset:3072 sc1\n\t"
                   "s_waitcnt vmcnt(0)"
                   : "+v"(f[0]), "+v"(f[1]), "+v"(f[2]), "+v"(f[3]) : "v"(voff), "s"(srd), "s"(soff) : "memory");
    } while (any_sent(chk4(f)) && ++spins < SPIN_CAP);
  }
}
template <int M1, int M2, int M3, int S1, int S2, int S3>
DI void dma4(u32 lds, u32 voff, v4i srd, u32 soff) {
  u32 t;
  soff = __builtin_amdgcn_readfirstlane(soff);
  lds = __builtin_amdgcn_readfirstlane(lds);
  asm volatile("s_mov_b32 m0, %1\n\ts_mov_b32 %0, %4\n\tbuffer_load_dwordx4 %2, %3, %0 offen lds\n\t"
               "s_add_i32 m0, m0, %5\n\ts_add_i32 %0, %0, %8\n\tbuffer_load_dwordx4 %2, %3, %0 offen lds\n\t"
               "s_add_i32 m0, m0, %6\n\ts_add_i32 %0, %0, %9\n\tbuffer_load_dwordx4 %2, %3, %0 offen lds\n\t"
               "s_add_i32 m0, m0, %7\n\ts_add_i32 %0, %0, %10\n\tbuffer_load_dwordx4 %2, %3, %0 offen lds"
               : "=&s"(t) : "s"(lds), "v"(voff), "s"(srd), "s"(soff), "n"(M1), "n"(M2), "n"(M3), "n"(S1), "n"(S2), "n"(S3) : "memory", "scc");
}
template <int N> DI void vmwait() { asm volatile("s_waitcnt vmcnt(%0)" ::"n"(N) : "memory"); }
DI void bar() { asm volatile("s_waitcnt lgkmcnt(0)\n\ts_barrier" ::: "memory"); }
DI v4u ld_sc1(__amdgpu_buffer_rsrc_t rs, u32 voff, u32 soff) { return __builtin_amdgcn_raw_buffer_load_b128(rs, voff, soff, 16); }
DI void st_sc1(v4u v, v4i srd, u32 voff, u32 soff) {
  asm volatile("buffer_store_dwordx4 %0, %1, %2, %3 offen sc1\n\ts_nop 1" ::"v"(v), "v"(voff), "s"(srd), "s"(soff) : "memory");
}
template <int D, int YOUNGER> DI void pace() { if (D < 0) vmwait<YOUNGER>(); else if (D > 0) __builtin_amdgcn_s_sleep(D < 0 ? 0 : D); }
DI void pin() { __builtin_amdgcn_sched_barrier(0); }

#define STAMP(i) do {} while (0)
#define CSTAMP(i) do {} while (0)

DI void max16(float (&m)[4], const f32x16& s) {
#pragma unroll
  for (int j = 0; j < 8; ++j) m[j & 3] = fmaxf(fmaxf(m[j & 3], s[2 * j]), s[2 * j + 1]);
}
DI v4u pack8(const float* p) { return v4u{cvtpk(p[0], p[1]), cvtpk(p[2], p[3]), cvtpk(p[4], p[5]), cvtpk(p[6], p[7])}; }
DI v4u pack44(f32x4 a, f32x4 b) { return v4u{cvtpk(a.x, a.y), cvtpk(a.z, a.w), cvtpk(b.x, b.y), cvtpk(b.z, b.w)}; }
DI f32x4 unlo(v4u v) { return f32x4{bflo(v.x), bfhi(v.x), bflo(v.y), bfhi(v.y)}; }
DI f32x4 unhi(v4u v) { return f32x4{bflo(v.z), bfhi(v.z), bflo(v.w), bfhi(v.w)}; }
DI v4u gelu8(f32x4 a, f32x4 b) {
  const float c1 = -2.0f * 0.7978845608028654f * 1.4426950408889634f, c2 = c1 * 0.044715f;
  const f32x2 x[4] = {{a.x, a.y}, {a.z, a.w}, {b.x, b.y}, {b.z, b.w}}, k1 = {c1, c1}, k2 = {c2, c2}, one = {1.f, 1.f};
  f32x2 t[4];
#pragma unroll
  for (int i = 0; i < 4; ++i) t[i] = x[i] * x[i];
  pin();
#pragma unroll
  for (int i = 0; i < 4; ++i) t[i] = __builtin_elementwise_fma(t[i], k2, k1);
  pin();
#pragma unroll
  for (int i = 0; i < 4; ++i) t[i] = t[i] * x[i];
  pin();
#pragma unroll
  for (int i = 0; i < 4; ++i) t[i] = f32x2{__builtin_amdgcn_exp2f(t[i].x), __builtin_amdgcn_exp2f(t[i].y)};
  pin();
#pragma unroll
  for (int i = 0; i < 4; ++i) t[i] = t[i] + one;
  pin();
#pragma unroll
  for (int i = 0; i < 4; ++i) t[i] = f32x2{__builtin_amdgcn_rcpf(t[i].x), __builtin_amdgcn_rcpf(t[i].y)};
  pin();
#pragma unroll
  for (int i = 0; i < 4; ++i) t[i] = t[i] * x[i];
  pin();
  return v4u{cvtpk(t[0].x, t[0].y), cvtpk(t[1].x, t[1].y), cvtpk(t[2].x, t[2].y), cvtpk(t[3].x, t[3].y)};
}
struct Frag4 { bf16x8 f[4]; };
template <bool XFIRST, bool RDX>
DI void gemm4(f32x16& acc, const Frag4& xc, const Frag4& wc, Frag4& xn, Frag4& wn, const u32 (&xa)[4], u32 xo, u32 wo) {
#pragma unroll
  for (int s = 0; s < 4; ++s) {
    acc = XFIRST ? mfma32(xc.f[s], wc.f[s], acc) : mfma32(wc.f[s], xc.f[s], acc);
    if (RDX) xn.f[s] = asfrag(lds_r128(xa[s] + xo));
    wn.f[s] = lds_tr(wo + s * 2048, wo + s * 2048 + 512);
    pin();
  }
}
DI void ld3(v4u (&f)[3], u32 voff, v4i srd, u32 soff) {
  asm volatile("buffer_load_dwordx4 %0, %3, %4, %5 offen sc1\n\tbuffer_load_dwordx4 %1, %3, %4, %5 offen offset:1024 sc1\n\t"
               "buffer_load_dwordx4 %2, %3, %4, %5 offen offset:2048 sc1"
               : "=&v"(f[0]), "=&v"(f[1]), "=&v"(f[2]) : "v"(voff), "s"(srd), "s"(soff) : "memory");
}
template <int N> DI void take3(v4u (&f)[3], u32 voff, v4i srd, u32 soff, int& spins) {
  asm volatile("s_waitcnt vmcnt(%3)" : "+v"(f[0]), "+v"(f[1]), "+v"(f[2]) : "n"(N) : "memory");
  if (__builtin_expect(any_sent(umax3(vmax(f[0]), vmax(f[1]), vmax(f[2]))), 0)) {
#pragma clang loop unroll(disable)
    do {
      asm volatile("s_sleep 1\n\tbuffer_load_dwordx4 %0, %3, %4, %5 offen sc1\n\tbuffer_load_dwordx4 %1, %3, %4, %5 offen offset:1024 sc1\n\t"
                   "buffer_load_dwordx4 %2, %3, %4, %5 offen offset:2048 sc1\n\ts_waitcnt vmcnt(0)"
                   : "+v"(f[0]), "+v"(f[1]), "+v"(f[2]) : "v"(voff), "s"(srd), "s"(soff) : "memory");
    } while (any_sent(umax3(vmax(f[0]), vmax(f[1]), vmax(f[2]))) && ++spins < SPIN_CAP);
  }
}
template <int N> DI void signal(u32 cnt, int lane, __amdgpu_buffer_rsrc_t rws, u32 flag, u32 tag) {
  u32 old = 0;
  if (lane == 0) old = __hip_atomic_fetch_add((LAS u32*)(uintptr_t)cnt, 1u, __ATOMIC_RELAXED, __HIP_MEMORY_SCOPE_WORKGROUP);
  if (__builtin_amdgcn_readfirstlane(old) == N - 1 && lane == 0) __builtin_amdgcn_raw_buffer_store_b32(tag, rws, flag, 0, 16);
}
DI int wg_of(int tt, int cg) { return ((tt & 7) + 8 * (cg & 3)) * 8 + 2 * (tt >> 3) + (cg >> 2); }
DI int wg_ofd(int d2, int ft, int kc) { return (d2 + 4 * (ft & 7)) * 8 + 4 * (ft >> 3) + kc; }
DI u32 ld_flag(u32 off, v4i sws) {
  u32 f;
  asm volatile("buffer_load_dword %0, %1, %2, 0 offen sc1" : "=v"(f) : "v"(off), "s"(sws) : "memory");
  return f;
}
DI void wait_flag(u32& f, u32 off, v4i sws, u32 tag, int& spins) {
  if (__builtin_expect(__builtin_amdgcn_ballot_w64(off != OOB && f != tag) != 0, 0)) {
#pragma clang loop unroll(disable)
    do {
      asm volatile("s_sleep 2\n\tbuffer_load_dword %0, %1, %2, 0 offen sc1\n\ts_waitcnt vmcnt(0)" : "+v"(f) : "v"(off), "s"(sws) : "memory");
    } while (__builtin_amdgcn_ballot_w64(off != OOB && f != tag) != 0 && ++spins < SPIN_CAP);
  }
}

__global__ void __launch_bounds__(512, 2)
mega(const u16* gx, const u16* gwqkv, const u16* gwo, const u16* gw1, const u16* gb1, const u16* gw2, const u16* gb2, u16* gout, char* ws) {
  asm volatile("" : "+s"(gx), "+s"(gwqkv), "+s"(gwo), "+s"(gw1), "+s"(gb1), "+s"(gw2), "+s"(gb2), "+s"(gout), "+s"(ws));
  const int tid = threadIdx.x, lane = tid & 63;
  const int w = __builtin_amdgcn_readfirstlane(tid >> 6);
  const int wg = blockIdx.x, xcd = wg & 7, loc = wg >> 3;
  const int tt = 8 * (xcd >> 1) + (loc & 7), cg = 4 * (xcd & 1) + (loc >> 3);
  const int kc = xcd & 3, d2 = loc & 3, ft = 8 * (xcd >> 2) + (loc >> 2);
  const bool don = ft < 15;
  const int rot = ((loc & 7) + 2 * (loc >> 3)) & 7;
  STAMP(0);
  const u32 l16 = lane * 16;
  const int l8 = lane >> 3, c8 = lane & 7;
  int spins = 0;

  v4u xr[8];
  const v4i sx = mksrd(gx, 2048 * 512 * 2);
  u32 xro = (64 * tt + 8 * w + l8) * 1024 + c8 * 16;
  auto load_x = [&]() {
#pragma unroll
    for (int i = 0; i < 8; ++i) xr[i] = aload(xro, sx, ((i + rot) & 7) * 128);
  };
  CSTAMP(42);
  if (w < 4) load_x();
  CSTAMP(43);
  const v4i swqkv = mksrd(gwqkv, 512 * 1536 * 2), sw1 = mksrd(gw1, 2048 * 1376 * 2);
  const __amdgpu_buffer_rsrc_t rws = mkrs(ws, WS_END);
  const v4i sws = mksrd(ws, WS_END);
  const int r32 = lane & 31, h = lane >> 5, blk = (lane >> 4) & 1, q4 = (lane & 15) >> 2, p4 = lane & 3, g4 = lane >> 4;
  const int hh = w & 3, tb = w >> 2;
  const int isV = w & 1, hp = (w >> 1) & 1;
  const int s6 = w < 2 * kc ? w : w - 2;
  v4u sent4 = {SENT, SENT, SENT, SENT};
  u32 kvs = WS_KV + (tt * 8 + cg) * 16384;
  u32 wvq = (8 * w + l8) * 3072 + ((c8 ^ (((l8 >> 1) & 1) << 2)) * 16) + cg * 128;
  u32 ko[8];
#pragma unroll
  for (int i = 0; i < 8; ++i) ko[i] = ((i + rot) & 7) * (64 * 3072);
  u32 ring = w * 1024;
  u32 xa0 = L_XN + (32 * tb + r32) * 128 + ((h ^ ((r32 >> 1) & 7)) * 16);
  u32 wa = 65536 + isV * 8192 + (8 * h + q4) * 128 + (((4 * hp + 2 * blk + (p4 >> 1)) ^ (((q4 >> 1) & 1) << 2)) * 16) + 8 * (p4 & 1);
  u32 xw = L_XN + (8 * w + l8) * 128 + ((c8 ^ (((8 * w + l8) >> 1) & 7)) * 16);
  asm volatile("" : "+v"(sent4), "+v"(xa0), "+v"(wa), "+v"(xw), "+v"(wvq));
  asm volatile("" : "+s"(ko[0]), "+s"(ko[1]), "+s"(ko[2]), "+s"(ko[3]), "+s"(ko[4]), "+s"(ko[5]), "+s"(ko[6]), "+s"(ko[7]), "+s"(kvs), "+s"(ring));
  auto clear_kv = [&]() {
    if (!(ABL & 32)) {
#pragma unroll
      for (int i = 0; i < 4; ++i) st_sc1(sent4, sws, l16, kvs + (w & 3) * 1024 + i * 4096);
    }
  };
  if (CLR_POS == 3 && w >= 4) clear_kv();
  u32 qwa, fao, kvo, kvl, hso, hks[4], vb0, mbits, ovo, swz, fbo, fco, so_o, so_hn, so_st, so_g, so_x, tra0, pcr, pcw0, w1vo, w1so;
  const int hpt = tt - 4 + (w >> 1);
  auto setup = [&](int i) {
    if (i == 0) {
      qwa = L_RING + (8 * h + q4) * 128 + (((2 * hh + (p4 >> 1)) ^ (((q4 >> 1) & 1) << 2)) * 16) + 8 * (p4 & 1);
      kvo = isV ? (2 * hp + (r32 >> 4)) * 1024 + h * 256 + (r32 & 15) * 16 : 2 * hp * 1024 + l16;
      kvl = (8 + tb) * KVT + (isV ? (2 * hp + (r32 >> 4)) * 2048 + 1024 + h * 256 + (r32 & 15) * 16 : 2 * hp * 2048 + l16);
      asm volatile("" : "+v"(qwa), "+v"(kvo), "+v"(kvl));
    } else if (i == 1) {
      fao = (tt - 4 + lane >= 0 && lane < 4) ? WS_FLAGA + wg_of(tt - 4 + lane, cg) * 128 : OOB;
      hso = WS_KV + ((hpt >= 0 ? hpt : 0) * 8 + cg) * 16384 + (w & 1) * 4096 + 8192;
#pragma unroll
      for (int j = 0; j < 4; ++j) hks[j] = WS_KV + cg * 16384 + hh * 1024 + (tt - 4 + j >= 0 ? (tt - 4 + j) * 131072 + tb * 4096 : 0);
      asm volatile("" : "+v"(fao));
      asm volatile("" : "+s"(hso), "+s"(hks[0]), "+s"(hks[1]), "+s"(hks[2]), "+s"(hks[3]));
    } else if (i == 2) {
      vb0 = r32 < 16 ? tb * KVT + hh * 2048 + 1024 + h * 256 + r32 * 16 : tb * KVT + 8192 + ((r32 >= 20 && r32 < 28) ? 64 : 0);
      const int t = r32 - 4 * h, n = t < 0 ? 0 : 4 * (t >> 3) + ((t & 7) < 3 ? (t & 7) + 1 : 4);
      mbits = (1u << n) - 1u;
      fbo = lane < 8 ? WS_FLAGB + wg_of(tt, lane) * 128 : OOB;
      asm volatile("" : "+v"(vb0), "+v"(mbits), "+v"(fbo));
    } else if (i == 3) {
      ovo = (r32 >> 4) * 1024 + ((2 * (hh & 1) + h) * 16 + (r32 & 15)) * 16 + tb * 32768 + (hh >> 1) * 2048;
      swz = (c8 ^ (((l8 >> 1) & 3) << 1)) * 16;
      tra0 = (lane >> 2) * 128 + 8 * (p4 & 1) + (((p4 >> 1) ^ (((lane >> 3) & 3) << 1)) * 16);
      asm volatile("" : "+v"(ovo), "+v"(swz), "+v"(tra0));
    } else if (i == 4) {
      const int pd = w == 6 ? wg_ofd(cg >> 1, lane >> 1, 2 * (cg & 1) + (lane & 1)) : wg_ofd(d2, ft, lane & 3);
      const int pw = w < 4 ? w * 64 + lane : (w == 5 && lane < 16) ? wg_of(8 * kc + (lane & 7), 2 * d2 + (lane >> 3)) : pd;
      const int cnt = w < 4 ? (wg == 0 ? 64 : 0) : w == 5 ? (don ? 20 : 0) : w == 6 ? 30 : 0;
      fco = lane < cnt ? (w < 4 ? WS_FLAGA : WS_FLAGC) + pw * 128 : OOB;
      asm volatile("" : "+v"(fco));
    } else if (i == 5) {
      const int q = 64 * w + lane, row = q / 12, c = (q - 12 * row) ^ (((row >> 2) & 1) << 1);
      w1vo = (don && !(ABL & 8) && (ft < 14 || c < 4)) ? row * 2752 + c * 16 : OOB;
      pcr = L_PC + (8 * w + l8) * 256 + (((2 * c8) ^ ((8 * w + l8) & 15)) * 16);
      pcw0 = (lane & 15) * 256 + ((g4 ^ (lane & 15)) * 16);
      asm volatile("" : "+v"(w1vo), "+v"(pcr), "+v"(pcw0));
    } else {
      so_o = WS_O + tt * 65536 + cg * 4096; so_hn = WS_HN + cg * 262144 + tt * 4096; so_st = WS_ST + tt * 4096;
      so_g = don ? WS_G + ((4 * d2 + kc) * 43 + 3 * ft) * 2048 : 0;
      so_x = don ? WS_XP + (d2 * 15 + ft) * 73728 : 0;
      w1so = don ? kc * (512 * 2752) + ft * 192 : 0;
      asm volatile("" : "+s"(so_o), "+s"(so_hn), "+s"(so_st), "+s"(so_g), "+s"(so_x), "+s"(w1so));
    }
    pin();
  };
#pragma unroll
  for (int i = 0; i < SETUP0; ++i) setup(i);
  CSTAMP(44);
  if (w >= 4) { load_x(); if (CLR_POS == 4) clear_kv(); }
  CSTAMP(45);
  {
    unsigned long long pc;
    int len;
    asm volatile("s_getpc_b64 %0\n.Lmega_pf_start:\n\ts_mov_b32 %1, .Lmega_pf_end-.Lmega_pf_start" : "=s"(pc), "=s"(len));
    if (w == 0) dma16(L_JUNK, l16 + loc * 1024, mksrd((const void*)(pc & ~63ull), len > 0 ? len : 0), 0);
  }
  const u32 wvk = wvq + 1024, wvv = wvq + 2048;
  auto issue_w = [&](int st) {
    dma16(ring + wslot(st), st < 8 ? wvk : wvq, swqkv, st < 8 ? ko[st & 7] : ko[(2 * st) & 7]);
    dma16(ring + wslot(st) + 8192, st < 8 ? wvv : wvq, swqkv, st < 8 ? ko[st & 7] : ko[(2 * st + 1) & 7]);
  };
  auto issue_w03 = [&](int st) {
    if (w < 4) {
      dma4<4096, 4096, 4096, 32 * 3072, 1024 - 32 * 3072, 32 * 3072>(ring + wslot(st), wvk, swqkv, ko[st]);
    }
  };
  u32 epoch;
  {
    f32x2 xv[32];
    float s1[2] = {0.f, 0.f}, s2[2] = {0.f, 0.f};
    auto acc4 = [&](int i) {
      const u32 d[4] = {xr[i].x, xr[i].y, xr[i].z, xr[i].w};
#pragma unroll
      for (int j = 0; j < 4; ++j) {
        s1[j & 1] = dot2(d[j], 0x3F803F80u, s1[j & 1]);
        s2[j & 1] = dot2(d[j], d[j], s2[j & 1]);
      }
    };
    auto unpack = [&](int i) {
      const u32 d[4] = {xr[i].x, xr[i].y, xr[i].z, xr[i].w};
#pragma unroll
      for (int j = 0; j < 4; ++j) xv[4 * i + j] = f32x2{bflo(d[j]), bfhi(d[j])};
      asm volatile("" : "+v"(xv[4 * i]), "+v"(xv[4 * i + 1]), "+v"(xv[4 * i + 2]), "+v"(xv[4 * i + 3]));
    };
    CSTAMP(40);
    asm volatile("s_load_dword %0, %1, 0x0" : "=s"(epoch) : "s"(ws) : "memory");
    if (CLR_POS != 4 || w < 4 || (ABL & 32)) vmwait<6>();
    asm volatile("s_waitcnt vmcnt(10)" : "+v"(xr[0]), "+v"(xr[1])::"memory");
    CSTAMP(41);
    acc4(0); acc4(1); unpack(0); unpack(1); pin();
    if (CLR_POS != 4 || w < 4 || (ABL & 32)) vmwait<4>();
    asm volatile("s_waitcnt vmcnt(8)" : "+v"(xr[2]), "+v"(xr[3])::"memory");
    acc4(2); acc4(3); unpack(2); unpack(3); pin();
    if (CLR_POS != 4 || w < 4 || (ABL & 32)) vmwait<2>();
    asm volatile("s_waitcnt vmcnt(6)" : "+v"(xr[4]), "+v"(xr[5])::"memory");
    acc4(4); acc4(5); unpack(4); unpack(5); pin();
    if (CLR_POS != 4 || w < 4 || (ABL & 32)) vmwait<0>();
    asm volatile("s_waitcnt vmcnt(4)" : "+v"(xr[6]), "+v"(xr[7])::"memory");
    CSTAMP(47);
    acc4(6); acc4(7);
    pin();
    float m1 = s1[0] + s1[1], m2 = s2[0] + s2[1];
    m1 += dpp<0xB1>(m1); m2 += dpp<0xB1>(m2);
    unpack(6); pin();
    m1 += dpp<0x4E>(m1); m2 += dpp<0x4E>(m2);
    unpack(7); pin();
    m1 += dpp<0x141>(m1); m2 += dpp<0x141>(m2);
    m1 *= 1.f / 512.f;
    const float rs = __builtin_amdgcn_rsqf(fmaxf(m2 * (1.f / 512.f) - m1 * m1, 0.f) + 1e-5f), nb = -m1 * rs;
    const f32x2 rs2 = {rs, rs}, nb2 = {nb, nb};
    pin(); issue_w03(0); pin();
#pragma unroll
    for (int i = 0; i < 8; i += 2) {
      f32x2 y[8];
#pragma unroll
      for (int j = 0; j < 8; ++j) y[j] = __builtin_elementwise_fma(xv[4 * i + j], rs2, nb2);
      pin();
      lds_w128(xw + i * 8192, v4u{cvtpk(y[0].x, y[0].y), cvtpk(y[1].x, y[1].y), cvtpk(y[2].x, y[2].y), cvtpk(y[3].x, y[3].y)});
      lds_w128(xw + (i + 1) * 8192, v4u{cvtpk(y[4].x, y[4].y), cvtpk(y[5].x, y[5].y), cvtpk(y[6].x, y[6].y), cvtpk(y[7].x, y[7].y)});
      pin();
      if (i == 2) { issue_w03(1); pin(); }
    }
    if (tid < 5) *(LAS u32*)(uintptr_t)(L_CNT + 4 * tid) = 0;
    pin();
    issue_w03(2); issue_w03(3);
    if (CLR_POS == 2 && w < 4) clear_kv();
    CSTAMP(48);
  }
  asm volatile("s_waitcnt lgkmcnt(0)" : "+s"(epoch)::"memory");
  const u32 tag = epoch + 1;
  STAMP(1);

  Frag4 xk[8], wa_, wb_;
  v4u kva, kvb;
  {
    f32x16 acc;
#pragma unroll
    for (int i = 0; i < 16; ++i) acc[i] = 0.f;
    const u32 xa[4] = {xa0, xa0 ^ 32, xa0 ^ 64, xa0 ^ 96}, wa2 = wa + 65536;
    vmwait<8>(); bar();
    CSTAMP(51);
    auto loop = [&](auto xfirst) {
      constexpr bool XF = decltype(xfirst)::value;
      auto wo = [&](int st) { return (st < 8 && wslot(st) < 131072) ? wa + (wslot(st) - 65536) : wa2 + (wslot(st) - 131072); };
#pragma unroll
      for (int s = 0; s < 4; ++s) { xk[0].f[s] = asfrag(lds_r128(xa[s])); wa_.f[s] = lds_tr(wo(0) + s * 2048, wo(0) + s * 2048 + 512); }
      pin();
      gemm4<XF, true>(acc, xk[0], wa_, xk[1], wb_, xa, 1 * 8192, wo(1));
      issue_w(4);
      vmwait<6>(); bar(); issue_w(5);
      gemm4<XF, true>(acc, xk[1], wb_, xk[2], wa_, xa, 2 * 8192, wo(2));
      issue_w(6);
      CSTAMP(0);
      vmwait<6>(); bar(); issue_w(7);
      gemm4<XF, true>(acc, xk[2], wa_, xk[3], wb_, xa, 3 * 8192, wo(3));
      vmwait<6>(); bar(); issue_w(8);
      gemm4<XF, true>(acc, xk[3], wb_, xk[4], wa_, xa, 4 * 8192, wo(4));
      CSTAMP(2);
      vmwait<6>();
      bar();
      gemm4<XF, true>(acc, xk[4], wa_, xk[5], wb_, xa, 5 * 8192, wo(5));
      vmwait<4>();
      bar(); issue_w(9);
      if (tid == 0) __builtin_amdgcn_raw_buffer_store_b32(tag, rws, WS_FLAGA + wg * 128, 0, 16);
      gemm4<XF, true>(acc, xk[5], wb_, xk[6], wa_, xa, 6 * 8192, wo(6));
      CSTAMP(4);
      vmwait<4>();
      bar(); issue_w(10);
      gemm4<XF, true>(acc, xk[6], wa_, xk[7], wb_, xa, 7 * 8192, wo(7));
      vmwait<2>(); bar(); issue_w(11);
      gemm4<XF, false>(acc, xk[7], wb_, xk[7], wa_, xa, 0, qwa + (wslot(8) - L_RING));
      CSTAMP(6);
      kva = guard4(v4u{cvtpk(acc[0], acc[1]), cvtpk(acc[2], acc[3]), cvtpk(acc[4], acc[5]), cvtpk(acc[6], acc[7])});
      if (!XF) st_sc1(kva, sws, kvo, kvs + tb * 4096);
      kvb = guard4(v4u{cvtpk(acc[8], acc[9]), cvtpk(acc[10], acc[11]), cvtpk(acc[12], acc[13]), cvtpk(acc[14], acc[15])});
      if (!XF) st_sc1(kvb, sws, kvo, kvs + 1024 + tb * 4096);
      lds_w128(kvl, kva);
      lds_w128(kvl + (XF ? 512 : 2048), kvb);
    };
    if (isV) loop(BoolC<true>{}); else loop(BoolC<false>{});
    STAMP(2);
  }
  bf16x8 qf;
  v4u hk[4], hv[4];
  u32 hko[4], fm[16], pm[8];
#pragma unroll
  for (int j = 0; j < 4; ++j) hko[j] = tt - 4 + j >= 0 ? l16 : h * 512;
  const u32 hvo = hpt >= 0 ? l16 : OOB;
  {
    const u32 xa[4] = {0, 0, 0, 0};
    f32x16 acc;
#pragma unroll
    for (int i = 0; i < 16; ++i) acc[i] = 0.f;
    auto qo = [&](int hs) { return qwa + (wslot(8 + (hs >> 1)) - L_RING) + (hs & 1) * 8192; };
    u32 fla = ld_flag(fao, sws);
    CSTAMP(59);
    auto get_k = [&]() {
      asm volatile("s_waitcnt vmcnt(0)" : "+v"(fla)::"memory");
      wait_flag(fla, fao, sws, tag, spins);
      bar();
      CSTAMP(8);
      ld4s(hk, hko, sws, hks);
      if (isV) { st_sc1(kva, sws, kvo, kvs + 8192 + tb * 4096); st_sc1(kvb, sws, kvo, kvs + 8192 + 512 + tb * 4096); }
      CSTAMP(60);
    };
    gemm4<false, false>(acc, xk[0], wa_, xk[0], wb_, xa, 0, qo(1));
    CSTAMP(61);
    if (SETUP0 <= 2) setup(2);
    CSTAMP(62);
    gemm4<false, false>(acc, xk[1], wb_, xk[0], wa_, xa, 0, qo(2));
    if (HK_AT == 2) get_k();
    if (tid < 160) { const u32 one = (tid >> 5) + tt >= 4 ? 0x3F803F80u : 0u; lds_w128((tid >> 4) * KVT + 8192 + (tid & 15) * 16, v4u{one, one, one, one}); }
    gemm4<false, false>(acc, xk[2], wa_, xk[0], wb_, xa, 0, qo(3));
    CSTAMP(7);
    if (HK_AT == 3) get_k();
    gemm4<false, false>(acc, xk[3], wb_, xk[0], wa_, xa, 0, qo(4));
    if (SETUP0 <= 3) setup(3);
    gemm4<false, false>(acc, xk[4], wa_, xk[0], wb_, xa, 0, qo(5));
    setup(6);
    CSTAMP(9);
    gemm4<false, false>(acc, xk[5], wb_, xk[0], wa_, xa, 0, qo(6));
#pragma unroll
    for (int i = 0; i < 16; ++i) fm[i] = (u32)__builtin_amdgcn_sbfe((int)mbits, i, 1);
#pragma unroll
    for (int k = 0; k < 8; ++k) pm[k] = (fm[2 * k] & 0xFFFFu) | (fm[2 * k + 1] & 0xFFFF0000u);
    asm volatile("" : "+v"(pm[0]), "+v"(pm[1]), "+v"(pm[2]), "+v"(pm[3]), "+v"(pm[4]), "+v"(pm[5]), "+v"(pm[6]), "+v"(pm[7]));
    asm volatile("" : "+v"(fm[0]), "+v"(fm[1]), "+v"(fm[2]), "+v"(fm[3]), "+v"(fm[4]), "+v"(fm[5]), "+v"(fm[6]), "+v"(fm[7]), "+v"(fm[8]), "+v"(fm[9]),
                 "+v"(fm[10]), "+v"(fm[11]), "+v"(fm[12]), "+v"(fm[13]), "+v"(fm[14]), "+v"(fm[15]));
    pin();
    gemm4<false, false>(acc, xk[6], wa_, xk[0], wb_, xa, 0, qo(7));
#pragma unroll
    for (int s = 0; s < 4; ++s) acc = mfma32(wb_.f[s], xk[7].f[s], acc);
    CSTAMP(10);
    const float qs = 0.25f * 1.4426950408889634f;
    qf = asfrag(v4u{cvtpk(acc[0] * qs, acc[1] * qs), cvtpk(acc[2] * qs, acc[3] * qs), cvtpk(acc[4] * qs, acc[5] * qs), cvtpk(acc[6] * qs, acc[7] * qs)});
  }
  STAMP(3);

  u32 flb, b2v;
  v4u xres;
  v2u b1a, b1b;
  bf16x8 wf[4][4];
  {
    const bool ldr = w < 4;
    const v4i swo = mksrd(gwo, 512 * 512 * 2);
    const u32 kb = tb * KVT + hh * 2048 + l16, vb1 = r32 < 16 ? vb0 + 512 : vb0;
    const u32 wovo = (ABL & 4) ? OOB : l8 * 1024 + swz + cg * 128;
    const u32 dov = don ? l16 : OOB, so_xc = so_x + kc * 18432 + (w & 3) * 1024;
    auto bg = [&](int op) {
      if (op < 2) st_sc1(sent4, sws, (ABL & 2) ? OOB : l16, so_o + w * 1024 + op * 32768);
      else if (op == 2) st_sc1(sent4, sws, tid < 32 ? l16 : OOB, so_st + cg * 512);
      else if (op < 7) {
        const int kk = 4 * w + ((op - 3 + loc) & 3);
        dma4<1024, 1024, 1024, 16384, -8192, 16384>(L_WO + kk * 4096, wovo, swo, kk * 32768);
      }
      else if (op < 9) st_sc1(sent4, sws, (ABL & 2) ? OOB : l16, so_hn + (w & 3) * 1024 + (op - 7) * 131072);
      else if (op < 11) st_sc1(sent4, sws, (!(ABL & 2) && (op == 9 ? (ft < 14 || (w & 3) < 2) : (ft < 14 && (w & 3) < 2))) ? dov : OOB, so_g + (w & 3) * 1024 + (op - 9) * 4096);
      else st_sc1(sent4, sws, ((op == 15 && (w & 3) >= 2) || (ABL & 1)) ? OOB : dov, so_xc + (op - 11) * 4096);
    };
    auto hook = [&](int pt) {
      if (LATE47 && !ldr) {
#pragma unroll
        for (int op = 7; op < NBG; ++op) if (BG_AT[op] == pt) bg(op);
      }
      if (ldr) {
#pragma unroll
        for (int op = 0; op < (LATE47 ? 7 : NBG); ++op) if (BG_AT[op] == pt) bg(op);
        if (pt == BG_RDY) { vmwait<bg_after(6, BG_RDY)>(); *(volatile LAS u32*)(uintptr_t)(L_CNT + 4 + 4 * w) = 1; }
        if (pt == BG_SIG) { vmwait<bg_after(2, BG_SIG)>(); signal<4>(L_CNT, lane, rws, WS_FLAGB + wg * 128, tag); }
      }
      if (pt == BG_BAR) {
        volatile LAS u32* rdy = (volatile LAS u32*)(uintptr_t)(L_CNT + 4 + 4 * (w & 3));
        if (!ldr) {
#pragma clang loop unroll(disable)
          while (__builtin_amdgcn_readfirstlane(*rdy) == 0 && ++spins < SPIN_CAP) __builtin_amdgcn_s_sleep(1);
          asm volatile("" ::: "memory");
        }
        xres = aload(xro, mksrd(gx, 2048 * 512 * 2), cg * 128);
        asm volatile("buffer_load_ushort %0, %1, %2, 0 offen" : "=v"(b2v) : "v"((xro >> 10) * 2), "s"(mksrd(gb2, 2048 * 2)) : "memory");
        asm volatile("buffer_load_dwordx2 %0, %2, %3, 0 offen\n\tbuffer_load_dwordx2 %1, %2, %3, 0 offen offset:32" : "=&v"(b1a), "=&v"(b1b) : "v"(don ? (96 * ft + 32 * (s6 >> 1) + 4 * g4) * 2 : OOB), "s"(mksrd(gb1, 1376 * 2)) : "memory");
      }
      if (pt == BG_BAR || pt == BG_BAR + 1) {
#pragma unroll
        for (int k = 2 * (pt - BG_BAR); k < 2 * (pt - BG_BAR) + 2; ++k)
#pragma unroll
          for (int n = 0; n < 4; ++n) { const u32 ad = ((L_WO + (w & 3) * 16384 + tra0) ^ (32 * n)) + k * 4096; wf[k][n] = lds_tr(ad, ad + 2048); }
      }
      if (pt == BG_FLAG && w == FLAGW) flb = ld_flag(fbo, sws);
      pin();
    };
    f32x16 z16;
#pragma unroll
    for (int i = 0; i < 16; ++i) z16[i] = 0.f;
    bf16x8 kf[9];
    kf[8] = asfrag(lds_r128(kb + 8 * KVT));
    f32x16 sb = mfma32(kf[8], qf, z16);
    if (!isV) vmwait<0>();
    take4s<2>(hk, hko, sws, hks, spins);
    CSTAMP(11);
    if (HV_AT == 8) ld4(hv, hvo, sws, hso);
#pragma unroll
    for (int j = 0; j < 4; ++j) { lds_w128(kb + 2 * j * KVT, hk[j]); kf[2 * j] = asfrag(hk[j]); }
    float m4[4] = {-3.0e38f, -3.0e38f, -3.0e38f, -3.0e38f};
    f32x16 sc = mfma32(kf[0], qf, z16), sa = mfma32(kf[2], qf, z16), sd = mfma32(kf[4], qf, z16), se = mfma32(kf[6], qf, z16);
    pin();
    if (HV_AT == 9) ld4(hv, hvo, sws, hso);
    bar();
    STAMP(4);
#pragma unroll
    for (int i = 1; i < 8; i += 2) kf[i] = asfrag(lds_r128(kb + i * KVT));
    pin();
    hook(0);
    f32x16 sm;
#pragma unroll
    for (int i = 0; i < 16; ++i) { const float a = sb[i], b = sc[i]; sm[i] = __uint_as_float((fm[i] & __float_as_uint(a)) | (~fm[i] & __float_as_uint(b))); }
    max16(m4, sm);
    pin(); max16(m4, sa); pin();
    sa = mfma32(kf[1], qf, z16); sb = mfma32(kf[3], qf, z16);
    pin(); max16(m4, sd); pin();
    sc = mfma32(kf[5], qf, z16);
    pin(); max16(m4, se); pin();
    sd = mfma32(kf[7], qf, z16);
    pin(); max16(m4, sa); max16(m4, sb); max16(m4, sc); max16(m4, sd);
    float mx = fmaxf(fmaxf(m4[0], m4[1]), fmaxf(m4[2], m4[3]));
    {
      const u32 a = __float_as_uint(mx);
      const auto sw = __builtin_amdgcn_permlane32_swap(a, a, false, false);
      const u32 s0 = sw[0], s1 = sw[1];
      mx = fmaxf(__uint_as_float(s0), __uint_as_float(s1));
    }
    CSTAMP(12);
    f32x16 nm;
#pragma unroll
    for (int i = 0; i < 16; ++i) nm[i] = -mx;
    f32x16 sn = mfma32(kf[1], qf, nm);
    pin();
    u32 pk[8];
#pragma unroll
    for (int k = 0; k < 8; ++k) pk[k] = cvtpk(__builtin_amdgcn_exp2f(sm[2 * k] - mx), __builtin_amdgcn_exp2f(sm[2 * k + 1] - mx));
    v4u pl0 = {pk[0] & pm[0], pk[1] & pm[1], pk[2] & pm[2], pk[3] & pm[3]}, pl1 = {pk[4] & pm[4], pk[5] & pm[5], pk[6] & pm[6], pk[7] & pm[7]};
    asm volatile("" : "+v"(pl0), "+v"(pl1));
    v4u pf0 = {pk[0] ^ pl0.x, pk[1] ^ pl0.y, pk[2] ^ pl0.z, pk[3] ^ pl0.w}, pf1 = {pk[4] ^ pl1.x, pk[5] ^ pl1.y, pk[6] ^ pl1.z, pk[7] ^ pl1.w};
    asm volatile("" : "+v"(pf0), "+v"(pf1));
    pin();
    if (!ldr) vmwait<0>();
    take4<bg_after(-1, 0)>(hv, hvo, sws, hso, spins);
#pragma unroll
    for (int u = 0; u < 4; ++u) lds_w128(w * KVT + u * 2048 + 1024 + l16, hv[u]);
    CSTAMP(13);
    bar();
    v4u v0 = lds_r128(vb0 + 8 * KVT), v1 = lds_r128(vb1 + 8 * KVT), v2 = lds_r128(vb0), v3 = lds_r128(vb1);
    hook(1);
    f32x16 o = z16;
    CSTAMP(14);
#pragma unroll
    for (int i = 1; i <= 7; ++i) {
      const f32x16 s = sn;
      if (i < 7) sn = mfma32(kf[i + 1], qf, nm);
      v4u va = lds_r128(vb0 + i * KVT), vb = lds_r128(vb1 + i * KVT);
      pin();
      float p[8];
#pragma unroll
      for (int j = 0; j < 8; ++j) p[j] = __builtin_amdgcn_exp2f(s[j]);
      const v4u pa = pack8(p);
      if (i == 1) {
        o = mfma32(asfrag(v0), asfrag(pl0), o);
        o = mfma32(asfrag(v1), asfrag(pl1), o);
        o = mfma32(asfrag(v2), asfrag(pf0), o);
        o = mfma32(asfrag(v3), asfrag(pf1), o);
      }
      o = mfma32(asfrag(va), asfrag(pa), o);
      pin();
      hook(2 * i);
#pragma unroll
      for (int j = 0; j < 8; ++j) p[j] = __builtin_amdgcn_exp2f(s[8 + j]);
      const v4u pb = pack8(p);
      o = mfma32(asfrag(vb), asfrag(pb), o);
      pin();
      hook(2 * i + 1);
      CSTAMP(14 + i);
    }
    const float inv = __builtin_amdgcn_rcpf(o[8]);
    const v4u of = guard4(v4u{cvtpk(o[0] * inv, o[1] * inv), cvtpk(o[2] * inv, o[3] * inv), cvtpk(o[4] * inv, o[5] * inv), cvtpk(o[6] * inv, o[7] * inv)});
    st_sc1(of, sws, ovo, so_o);
  }
  STAMP(5);

  u32 w1l[4], w1g[4];
  auto dma_w1 = [&](int i) { if (w < 6) dma16(w1l[i >> 2] + (i & 3) * 6144, w1vo, sw1, w1g[i >> 2] + (i & 3) * (32 * 2752)); };
  const int dq = w >> 1, kh = w & 1;
  const v4i sw2 = mksrd(gw2, 1376 * 2048 * 2);
  u32 sto, hw, hr, w2vo, w1r[2], hna, xvo, ua0, ua1;
  u32 pb[5], hso4[4], w1o[4], xso, xst, gso, hns;
  auto setup_d = [&]() {
#pragma unroll
    for (int i = 0; i < 4; ++i) { const int g = (i + d2) & 3; w1l[i] = L_W1 + g * 24576 + w * 1024; w1g[i] = w1so + g * (128 * 2752); }
    asm volatile("" : "+s"(w1l[0]), "+s"(w1l[1]), "+s"(w1l[2]), "+s"(w1l[3]), "+s"(w1g[0]), "+s"(w1g[1]), "+s"(w1g[2]), "+s"(w1g[3]));
    sto = c8 * 512 + (8 * w + l8) * 8;
    asm volatile("" : "+v"(sto));
    pin();
  };
  auto setup_d2 = [&]() {
    hw = L_PC + w * 2048 + l8 * 128 + swz;
    hr = ((L_PC + (w >> 2) * 8192 + (tra0 & 1023) + (lane >> 5) * 2048) ^ (32 * (w & 3)));
    w2vo = (ABL & 16) ? OOB : l8 * 4096 + swz + tt * 128;
#pragma unroll
    for (int e = 0; e < 2; ++e) w1r[e] = L_W1 + kh * 49152 + (lane >> 2) * 192 + (((2 * e + (p4 >> 1)) ^ ((g4 & 1) << 1)) * 16) + 8 * (p4 & 1);
    hna = l16 + kh * 1024;
    const int sj = (s6 >> 1) & 3, sm_ = s6 & 1;
    xvo = (dq != kc && (ft < 14 || s6 < 2)) ? l16 : OOB;
    ua0 = L_PD + 49152 + sm_ * 6144 + (sj < 3 ? sj : 0) * 2048 + l16;
    ua1 = L_PD + (2 * kc + 1 - sm_) * 6144 + (sj < 3 ? sj : 0) * 2048 + l16;
    asm volatile("" : "+v"(hw), "+v"(hr), "+v"(w2vo), "+v"(w1r[0]), "+v"(w1r[1]), "+v"(hna), "+v"(xvo), "+v"(ua0), "+v"(ua1));
    hns = so_hn + ((w >> 1) & 1) * 131072 + (w >> 2) * 2048 + (w & 1) * 1024;
    asm volatile("" : "+s"(hns));
    pin();
  };
  auto setup_d3 = [&]() {
    const int rr = EROT == 1 ? tt + cg : EROT == 2 ? (cg & 3) : EROT == 3 ? (cg & 3) + (tt >> 3) : 0, r3 = rr % 3, r2 = rr & 1;
#pragma unroll
    for (int b = 0; b < 5; ++b) pb[b] = 11 * dq + 2 * (b < 3 ? (b + r3 >= 3 ? b + r3 - 3 : b + r3) : 3 + ((b - 3) ^ r2));
    const u32 so = WS_HN + (2 * d2 + (dq >> 1)) * 262144 + (dq & 1) * 131072 + (16 * kc + 8 * kh) * 2048;
#pragma unroll
    for (int i = 0; i < 4; ++i) { const int g = (i + (loc >> 2)) & 3; hso4[i] = so + g * 4096; w1o[i] = g * 12288; }
    xso = so_x + kc * 18432 + (dq != kc ? s6 : 0) * 3072;
    xst = so_x + dq * 18432 + kh * 3072 + ((kc - dq - 1) & 3) * 1024;
    gso = so_g + (dq != kc ? s6 * 1024 : 0);
    asm volatile("" : "+s"(pb[0]), "+s"(pb[1]), "+s"(pb[2]), "+s"(pb[3]), "+s"(pb[4]), "+s"(hso4[0]), "+s"(hso4[1]), "+s"(hso4[2]), "+s"(hso4[3]),
                 "+s"(w1o[0]), "+s"(w1o[1]), "+s"(w1o[2]), "+s"(w1o[3]), "+s"(xso), "+s"(xst), "+s"(gso));
    pin();
  };
  v4u hres;
  {
    const u32 oso = WS_O + tt * 65536 + (w >> 2) * 32768 + (w & 3) * 8192;
    if (w == FLAGW) {
      asm volatile("s_waitcnt vmcnt(1)" : "+v"(flb)::"memory");
      wait_flag(flb, fbo, sws, tag, spins);
    }
    CSTAMP(22);
    bar();
    pace<D_O, 0>();
    v4u ofr[2][4];
    ld4(ofr[0], l16, sws, oso);
    ld4(ofr[1], l16, sws, oso + 4096);
    STAMP(6);
    setup(5); setup_d();
    f32x4 acc[2][4];
#pragma unroll
    for (int m = 0; m < 2; ++m)
#pragma unroll
      for (int n = 0; n < 4; ++n) acc[m][n] = f32x4{0.f, 0.f, 0.f, 0.f};
#pragma unroll
    for (int k = 0; k < 4; ++k) {
      if (k == 0) take4<4>(ofr[0], l16, sws, oso, spins);
      if (k == 2) {
        take4<0>(ofr[1], l16, sws, oso + 4096, spins);
        asm volatile("" : "+v"(xres), "+v"(b2v), "+v"(b1a), "+v"(b1b));
#pragma unroll
        for (int i = 0; i < W1_0; ++i) dma_w1(i);
      }
#pragma unroll
      for (int m = 0; m < 2; ++m)
#pragma unroll
        for (int n = 0; n < 4; ++n) acc[m][n] = mfma16(wf[k][n], asfrag(ofr[k >> 1][2 * (k & 1) + m]), acc[m][n]);
      pin();
    }
#pragma unroll
    for (int i = W1_0; i < W1_A; ++i) dma_w1(i);
    STAMP(7);
#pragma unroll
    for (int m = 0; m < 2; ++m)
#pragma unroll
      for (int n = 0; n < 4; ++n) lds_wf4(((L_PC + (w & 3) * 16384 + (w >> 2) * 8192 + pcw0) ^ (64 * n)) + m * 4096, acc[m][n]);
#pragma unroll
    for (int i = W1_A; i < W1_B; ++i) dma_w1(i);
    bar();
    if (tid == 448) __builtin_amdgcn_raw_buffer_store_b32(tag, rws, WS_FLAGC + wg * 128, 0, 16);
    CSTAMP(23);
    f32x4 t[8];
#pragma unroll
    for (int k = 0; k < 4; ++k) { t[2 * k] = lds_rf4(pcr + k * 16384); t[2 * k + 1] = lds_rf4((pcr ^ 16) + k * 16384); }
    pin();
    hres = pack44(unlo(xres) + ((t[0] + t[2]) + (t[4] + t[6])), unhi(xres) + ((t[1] + t[3]) + (t[5] + t[7])));
  }
  {
    float q1 = dot2(hres.x, 0x3F803F80u, 0.f), q2 = dot2(hres.x, hres.x, 0.f), q3 = dot2(hres.y, 0x3F803F80u, 0.f), q4_ = dot2(hres.y, hres.y, 0.f);
    q1 = dot2(hres.z, 0x3F803F80u, q1); q2 = dot2(hres.z, hres.z, q2); q3 = dot2(hres.w, 0x3F803F80u, q3); q4_ = dot2(hres.w, hres.w, q4_);
    q1 += q3; q2 += q4_;
    q1 += dpp<0xB1>(q1); q2 += dpp<0xB1>(q2);
    q1 += dpp<0x4E>(q1); q2 += dpp<0x4E>(q2);
    q1 += dpp<0x141>(q1); q2 += dpp<0x141>(q2);
    const v2u stv = {guard(__float_as_uint(q1)), guard(__float_as_uint(q2))};
    asm volatile("buffer_store_dwordx2 %0, %1, %2, %3 offen sc1" ::"v"(stv), "v"(c8 == 0 ? sto : OOB), "s"(sws), "s"(so_st + cg * 512) : "memory");
    STAMP(8);
#pragma unroll
    for (int i = W1_B; i < W1_C; ++i) dma_w1(i);
    setup(4); setup_d2();
    f32x4 h0 = unlo(hres), h1 = unhi(hres);
    asm volatile("" : "+v"(h0), "+v"(h1));
    if (D_ST < 0 && w >= 6) vmwait<0>();
    pace<D_ST, W1_C - W1_B>();
    u32 flc = ld_flag(fco, sws);
    v2u st;
    asm volatile("buffer_load_dwordx2 %0, %1, %2, %3 offen sc1" : "=v"(st) : "v"(sto), "s"(sws), "s"(so_st) : "memory");
#pragma unroll
    for (int i = W1_C; i < W1_D; ++i) dma_w1(i);
    if (w >= 6) vmwait<0>();
    asm volatile("s_waitcnt vmcnt(%2)" : "+v"(st), "+v"(flc) : "n"(W1_D - W1_C) : "memory");
    if (__builtin_expect(any_sent(st.x > st.y ? st.x : st.y), 0)) {
#pragma clang loop unroll(disable)
      do {
        asm volatile("buffer_load_dwordx2 %0, %1, %2, %3 offen sc1\n\ts_waitcnt vmcnt(0)" : "+v"(st) : "v"(sto), "s"(sws), "s"(so_st) : "memory");
      } while (any_sent(st.x > st.y ? st.x : st.y) && ++spins < SPIN_CAP);
    }
    STAMP(9);
    const u32 sx_ = st.x, sy_ = st.y;
    float m1 = __uint_as_float(sx_), m2 = __uint_as_float(sy_);
    m1 += dpp<0xB1>(m1); m2 += dpp<0xB1>(m2);
    m1 += dpp<0x4E>(m1); m2 += dpp<0x4E>(m2);
    m1 += dpp<0x141>(m1); m2 += dpp<0x141>(m2);
    const float mean = m1 * (1.f / 512.f), var = fmaxf(m2 * (1.f / 512.f) - mean * mean, 0.f);
    const float r1 = __builtin_amdgcn_rsqf(var + 1e-5f);
    const float r2 = __builtin_amdgcn_rsqf(var * r1 * r1 + 1e-5f);
    h0 -= mean; h1 -= mean;
    const float sc = r1 * r2;
    const v4u hn = pack44(h0 * sc, h1 * sc);
    lds_w128(hw, hn);
    wait_flag(flc, fco, sws, tag, spins);
    bar();
    const v4u fr = guard4(__builtin_bit_cast(v4u, lds_tr(hr, hr + 2 * 2048)));
    st_sc1(fr, sws, l16, hns);
    STAMP(10);
    if (wg == 0 && tid == 448) __builtin_amdgcn_raw_buffer_store_b32(tag, rws, WS_EPOCH, 0, 16);
#pragma unroll
    for (int i = W1_D; i < 16; ++i) dma_w1(i);
    setup_d3();
  }
  const int eq = dq, eh = kh;
  auto w2slot = [&](int b) { return b < 3 ? L_W2A + eq * 24576 + (2 * b + eh) * 4096 : eq * 16384 + (2 * (b - 3) + eh) * 4096; };
  auto issue_w2 = [&](int b) { dma4<1024, 1024, 1024, 32768, 32768, 32768>(w2slot(b), w2vo, sw2, (pb[b] + eh) * 131072); };
  if (don) {
    f32x4 acc[6][2];
#pragma unroll
    for (int b = 0; b < 6; ++b)
#pragma unroll
      for (int m = 0; m < 2; ++m) acc[b][m] = f32x4{0.f, 0.f, 0.f, 0.f};
    const u32 hnb = hna ^ 1024;
    v4u hf[4][4];
    vmwait<0>();
    bar();
    pace<D_HN, 0>();
    ld4x(hf[0], hna, hnb, sws, hso4[0]);
    ld4x(hf[1], hna, hnb, sws, hso4[1]);
    CSTAMP(25);
    bf16x8 wc[6], wn[6];
    v4u w2r[3][4], w2l[8];
    auto raw_w2 = [&](int i) {
#pragma unroll
      for (int u = 0; u < 4; ++u) w2r[i][u] = aload(w2vo, sw2, (pb[i] + eh) * 131072 + u * 32768);
    };
    const u32 w2w = w2slot(0) + l16;
    {
      u32 a0 = w1r[0] + w1o[0], a1 = w1r[1] + w1o[0];
      asm volatile("" : "+v"(a0), "+v"(a1));
#pragma unroll
      for (int b = 0; b < 6; ++b) { const u32 ad = ((b & 1) ? a1 : a0) + (b >> 1) * 64; wc[b] = lds_tr(ad, ad + 16 * 192); }
    }
#pragma unroll
    for (int s = 0; s < 8; ++s) {
      if (s == 0) { take4x<4>(hf[0], hna, hnb, sws, hso4[0], spins); ld4x(hf[2], hna, hnb, sws, hso4[2]); }
      if (s == 2) { take4x<4>(hf[1], hna, hnb, sws, hso4[1], spins); ld4x(hf[3], hna, hnb, sws, hso4[3]); }
      if (s == 4) { take4x<4>(hf[2], hna, hnb, sws, hso4[2], spins); raw_w2(0); }
      if (s == 6) { take4x<4>(hf[3], hna, hnb, sws, hso4[3], spins); raw_w2(1); }
      if (s == 7 && NR == 3) raw_w2(2);
      const u32 o = w1o[(s + 1) >> 1 & 3] + ((s + 1) & 1) * 6144;
      u32 a0 = w1r[0] + o, a1 = w1r[1] + o;
      asm volatile("" : "+v"(a0), "+v"(a1));
#pragma unroll
      for (int b = 0; b < 6; ++b) {
#pragma unroll
        for (int m = 0; m < 2; ++m) acc[b][m] = mfma16(wc[b], asfrag(hf[s >> 1][2 * (s & 1) + m]), acc[b][m]);
        if (s < 7) { const u32 ad = ((b & 1) ? a1 : a0) + (b >> 1) * 64; wn[b] = lds_tr(ad, ad + 16 * 192); }
        pin();
      }
#pragma unroll
      for (int b = 0; b < 6; ++b) wc[b] = wn[b];
      CSTAMP(26 + s);
    }
    STAMP(11);
    const u32 pd = L_PD + w * 6144 + l16;
#pragma unroll
    for (int b = 0; b < 6; ++b) lds_wf4(pd + b * 1024, acc[b][1]);
    bar();
    CSTAMP(34);
    if (dq == kc) {
#pragma unroll
      for (int b = 0; b < 6; ++b) lds_wf4(L_PD + 49152 + kh * 6144 + l16 + b * 1024, acc[b][0]);
    } else {
      f32x4 t[6];
#pragma unroll
      for (int b = 0; b < 6; ++b) t[b] = lds_rf4(L_PD + (w ^ 1) * 6144 + l16 + b * 1024);
      pin();
#pragma unroll
      for (int j = 0; j < 3; ++j)
        st_sc1(guard4(pack44(acc[2 * j][0] + t[2 * j], acc[2 * j + 1][0] + t[2 * j + 1])), sws, (j > 0 && ft == 14) ? OOB : l16, xst + j * 6144);
    }
    auto late_so = [&](int t) { return (pb[3 + (t >> 2)] + eh) * 131072 + (t & 3) * 32768; };
#pragma unroll
    for (int t = 0; t < NA; ++t) w2l[t] = aload(w2vo, sw2, late_so(t));
    if (NR == 2 && B2_AT == 0) issue_w2(2);
    CSTAMP(35);
    bar();
    f32x4 u0 = lds_rf4(ua0), u1 = lds_rf4(ua0 + 1024), u2 = lds_rf4(ua1), u3 = lds_rf4(ua1 + 1024);
    constexpr int NX = NA + (NR == 2 && B2_AT == 0 ? 4 : 0);
    if (dq == kc) vmwait<NX>();
    asm volatile("s_waitcnt vmcnt(%8)" : "+v"(w2r[0][0]), "+v"(w2r[0][1]), "+v"(w2r[0][2]), "+v"(w2r[0][3]), "+v"(w2r[1][0]), "+v"(w2r[1][1]), "+v"(w2r[1][2]),
                 "+v"(w2r[1][3]) : "n"(3 + NX) : "memory");
    if (NR == 3) asm volatile("" : "+v"(w2r[2][0]), "+v"(w2r[2][1]), "+v"(w2r[2][2]), "+v"(w2r[2][3]));
#pragma unroll
    for (int i = 0; i < NR; ++i)
#pragma unroll
      for (int u = 0; u < 4; ++u) lds_w128(w2w + i * 8192 + u * 1024, w2r[i][u]);
    pin();
    pace<D_XP, NX>();
    v4u xp[3];
    ld3(xp, xvo, sws, xso);
#pragma unroll
    for (int t = NA; t < NA + NB; ++t) w2l[t] = aload(w2vo, sw2, late_so(t));
    if (NR == 2 && B2_AT == 1) issue_w2(2);
    u0 += u2 + f32x4{bflo(b1a.x), bfhi(b1a.x), bflo(b1a.y), bfhi(b1a.y)};
    u1 += u3 + f32x4{bflo(b1b.x), bfhi(b1b.x), bflo(b1b.y), bfhi(b1b.y)};
    asm volatile("" : "+v"(u0), "+v"(u1));
    take3<NB + (NR == 2 && B2_AT == 1 ? 4 : 0)>(xp, xvo, sws, xso, spins);
    CSTAMP(38);
    {
      const f32x4 a0 = unlo(xp[0]), a1 = unlo(xp[1]), a2 = unlo(xp[2]), b0 = unhi(xp[0]), b1 = unhi(xp[1]), b2 = unhi(xp[2]);
      pin();
      u0 = (u0 + a0) + (a1 + a2); u1 = (u1 + b0) + (b1 + b2);
      pin();
    }
    const v4u gv = guard4(gelu8(u0, u1));
    st_sc1(gv, sws, xvo, gso);
    STAMP(12);
    CSTAMP(39);
    bar();
    CSTAMP(24);
#pragma unroll
    for (int t = NA + NB; t < 8; ++t) dma16(w2slot(3 + (t >> 2)) + (t & 3) * 1024, w2vo, sw2, late_so(t));
    vmwait<9 - NA - NB>();
#pragma unroll
    for (int t = 0; t < NA + NB; ++t) {
      asm volatile("" : "+v"(w2l[t]));
      lds_w128(w2slot(3 + (t >> 2)) + (t & 3) * 1024 + l16, w2l[t]);
    }
  } else {
    vmwait<0>();
    bar();
    const u32 p = (xcd - 4) * 4 + (loc & 3);
#pragma clang loop unroll(disable)
    for (int i = 0; i < W2PF; ++i) {
      dma16(w * 4096, l16, sw2, (i < 16 ? 11 * (i & 3) + 6 + (i >> 2) : 11 * (i - 16) + 10) * 131072 + p * 8192 + w * 1024);
      vmwait<6>();
    }
    vmwait<0>();
    bar();
    issue_w2(0); issue_w2(1); issue_w2(2);
    vmwait<0>();
    bar();
    issue_w2(3); issue_w2(4);
    STAMP(12);
  }
  {
    f32x4 acc[2][4];
#pragma unroll
    for (int m = 0; m < 2; ++m)
#pragma unroll
      for (int n = 0; n < 4; ++n) acc[m][n] = f32x4{0.f, 0.f, 0.f, 0.f};
    const u32 so = WS_G + (cg * 2 + eh) * (43 * 2048);
    auto gso = [&](int b) { return so + (b < 5 ? pb[b < 5 ? b : 0] : 11 * eq + 10) * 2048; };
    u32 wa_[4], wl_[4], pew[2];
#pragma unroll
    for (int n = 0; n < 4; ++n) { wa_[n] = (tra0 + L_W2A + eq * 24576) ^ (32 * n); wl_[n] = (tra0 + eq * 16384) ^ (32 * n); }
#pragma unroll
    for (int m = 0; m < 2; ++m) pew[m] = (L_W2A + eq * 24576 + 8192 + pcw0) ^ (128 * eh + 64 * m);
    u32 per = L_W2A + 8192 + pcr, outo = xro + cg * 128;
    asm volatile("" : "+v"(wa_[0]), "+v"(wa_[1]), "+v"(wa_[2]), "+v"(wa_[3]), "+v"(wl_[0]), "+v"(wl_[1]), "+v"(wl_[2]), "+v"(wl_[3]),
                 "+v"(pew[0]), "+v"(pew[1]), "+v"(per), "+v"(outo));
    auto rd_w2 = [&](int L, int n) {
      const u32 ad = L < 6 ? wa_[n] + L * 4096 : L < 10 ? wl_[n] + (L - 6) * 4096 : wa_[n] + eh * 4096;
      return lds_tr(ad, ad + 2048);
    };
    v4u gf[3][4];
    bf16x8 wc[4], wn[4];
#pragma unroll
    for (int n = 0; n < 4; ++n) wc[n] = rd_w2(0, n);
    pace<D_G, 8 - NA - NB>();
    CSTAMP(36);
    ld4(gf[0], l16, sws, gso(0));
    ld4(gf[1], l16, sws, gso(1));
    CSTAMP(37);
#pragma unroll
    for (int b = 0; b < 6; ++b) {
      if (b < 5 || eq < 3) {
        if (eq == 3) { if (b == 1 || b == 2) vmwait<4>(); else if (b == 4) vmwait<0>(); }
        if (b == 1 || b == 2) take4<8>(gf[b % 3], l16, sws, gso(b), spins);
        else if (b < 5) take4<4>(gf[b % 3], l16, sws, gso(b), spins);
        else take4<0>(gf[b % 3], l16, sws, gso(b), spins);
        if (b < 3) CSTAMP(1 + 2 * b);
        if (b < 3 || (b == 3 && eq < 3)) ld4(gf[(b + 2) % 3], l16, sws, gso(b + 2));
#pragma unroll
        for (int e = 0; e < (b < 5 ? 2 : 1); ++e) {
          const int L = 2 * b + e;
#pragma unroll
          for (int n = 0; n < 4; ++n) {
#pragma unroll
            for (int m = 0; m < 2; ++m) acc[m][n] = mfma16(asfrag(gf[b % 3][2 * e + m]), wc[n], acc[m][n]);
            if (L < 10) wn[n] = rd_w2(L + 1, n);
            pin();
          }
#pragma unroll
          for (int n = 0; n < 4; ++n) wc[n] = wn[n];
        }
      }
      if (b == 0) {
        bar();
        if (eq < 3) dma4<1024, 1024, 1024, 32768, 32768, 32768>(L_W2A + eq * 24576 + eh * 4096, w2vo, sw2, (11 * eq + 10) * 131072);
      }
      if (b == 2) bar();
      pin();
      CSTAMP(52 + b);
    }
    STAMP(13);
#pragma unroll
    for (int n = 0; n < 4; ++n)
#pragma unroll
      for (int m = 0; m < 2; ++m) lds_wf4(pew[m] + n * 4096, acc[m][n]);
    const float bb = bflo(b2v);
    f32x4 s0 = unlo(hres) + bb, s1 = unhi(hres) + bb;
    u32 per1 = per ^ 16, per2 = per + 49152, per3 = per2 ^ 16;
    asm volatile("" : "+v"(s0), "+v"(s1), "+v"(per1), "+v"(per2), "+v"(per3));
    bar();
    CSTAMP(58);
    f32x4 t[8];
#pragma unroll
    for (int k = 0; k < 2; ++k) {
      t[2 * k] = lds_rf4(per + k * 24576); t[2 * k + 1] = lds_rf4(per1 + k * 24576);
      t[4 + 2 * k] = lds_rf4(per2 + k * 24576); t[5 + 2 * k] = lds_rf4(per3 + k * 24576);
    }
    pin();
    s0 += (t[0] + t[2]) + (t[4] + t[6]);
    s1 += (t[1] + t[3]) + (t[5] + t[7]);
    const v4u ov = pack44(s0, s1);
    asm volatile(".Lmega_pf_end:\n\tbuffer_store_dwordx4 %0, %1, %2, 0 offen" ::"v"(ov), "v"(outo), "s"(mksrd(gout, 2048 * 512 * 2)) : "memory");
  }
  STAMP(14);
}

extern "C" void kernel_launch(void* const* d_in, const int* in_sizes, int n_in, void* d_out, int out_size, void* d_ws, size_t ws_size,
                              hipStream_t stream) {
  (void)hipFuncSetAttribute((const void*)mega, hipFuncAttributeMaxDynamicSharedMemorySize, LDS_BYTES);
  mega<<<256, 512, LDS_BYTES, stream>>>((const u16*)d_in[0], (const u16*)d_in[1], (const u16*)d_in[2], (const u16*)d_in[4], (const u16*)d_in[5],
                                        (const u16*)d_in[6], (const u16*)d_in[7], (u16*)d_out, (char*)d_ws);
}
```
